```python
import torch

def _softmax(x, axis=-1):
    return torch.softmax(x, dim=axis)
def _gelu(x):
    return torch.nn.functional.gelu(x, approximate="tanh")
def _silu(x):
    return torch.nn.functional.silu(x)
def _sigmoid(x):
    return torch.sigmoid(x)
def _layernorm(x, axis=-1, eps=1e-5):
    m = x.mean(dim=axis, keepdim=True)
    v = x.var(dim=axis, keepdim=True, unbiased=False)
    return (x - m) / torch.sqrt(v + eps)
def _scatter_add(tgt, idx, src, axis=0):
    return tgt.clone().index_add_(axis, idx, src)
def _segment_sum(src, seg, n):
    out = torch.zeros((n,) + src.shape[1:], dtype=src.dtype)
    return out.index_add_(0, seg, src)
def _pad(x, pw):
    flat = tuple(v for pair in reversed(pw) for v in pair)
    return torch.nn.functional.pad(x, flat)

kernel_name = 'concat_heads__compressed_kv_cla__gqa_block_cached__decode'

def setup_inputs():

    _w = (lambda a: (lambda t: t.to(torch.bfloat16) if t.is_floating_point() else t)(torch.as_tensor(a)))
    def _A_setup():
        B, H, Sq, Sk, D, M = 2, 32, 1, 4096, 128, 512
        Dc = 4 * D
        return {
            "x": _w((lambda *s: torch.randn(*s, dtype=torch.bfloat16) * 3.2)(B, Sq, M)),
            "c_kv": _w((lambda *s: torch.randn(*s, dtype=torch.bfloat16) * 0.1)(B, Sk, Dc)),
            "w_q": _w((lambda *s: torch.randn(*s, dtype=torch.bfloat16) * 6.4)(M, H, D) * (M ** -0.5)),
            "w_uk": _w((lambda *s: torch.randn(*s, dtype=torch.bfloat16) * 6.4)(Dc, H, D) * (Dc ** -0.5)),
            "w_uv": _w((lambda *s: torch.randn(*s, dtype=torch.bfloat16) * 0.1)(Dc, H, D) * (Dc ** -0.5)),
            "w_o": _w((lambda *s: torch.randn(*s, dtype=torch.bfloat16) * 0.1)(H, D, M) * ((H * D) ** -0.5)),
        }
    _A_in = _A_setup()

    def _B_setup():
        B, Sq, D = 2, 1, 512
        Hq, Hkv, Sk, Dh = 32, 32, 4096, 128
        assert Hq % Hkv == 0, (Hq, Hkv)
        return {
            "x": _w((lambda *s: torch.randn(*s, dtype=torch.bfloat16) * 1.6)(B, Sq, D)),
            "k_cache": _w((lambda *s: torch.randn(*s, dtype=torch.bfloat16) * 1.6)(B, Hkv, Sk, Dh)),
            "v_cache": _w((lambda *s: torch.randn(*s, dtype=torch.bfloat16) * 0.05)(B, Hkv, Sk, Dh)),
            "wq": _w((lambda *s: torch.randn(*s, dtype=torch.bfloat16) * 1.6)(D, Hq * Dh) * (D ** -0.5)),
            "wo": _w((lambda *s: torch.randn(*s, dtype=torch.bfloat16) * 0.05)(Hq * Dh, D) * ((Hq * Dh) ** -0.5)),
        }
    _B_in = _B_setup()

    return {
        "A_x": _A_in["x"],
        "A_c_kv": _A_in["c_kv"],
        "A_w_q": _A_in["w_q"],
        "A_w_uk": _A_in["w_uk"],
        "A_w_uv": _A_in["w_uv"],
        "A_w_o": _A_in["w_o"],
        "B_x": _B_in["x"],
        "B_k_cache": _B_in["k_cache"],
        "B_v_cache": _B_in["v_cache"],
        "B_wq": _B_in["wq"],
        "B_wo": _B_in["wo"],
    }

def reference(A_x, A_c_kv, A_w_q, A_w_uk, A_w_uv, A_w_o, B_x, B_k_cache, B_v_cache, B_wq, B_wo):
    def _A_ref(x, c_kv, w_q, w_uk, w_uv, w_o):
        B, Sq, M = x.shape[0], x.shape[1], x.shape[2]
        Sk = c_kv.shape[1]
        H, D = w_q.shape[1], w_q.shape[2]
        q = torch.einsum("bqm,mhd->bhqd", x, w_q)
        k = torch.einsum("bkc,chd->bhkd", c_kv, w_uk)
        v = torch.einsum("bkc,chd->bhkd", c_kv, w_uv)
        scores = torch.einsum("bhqd,bhkd->bhqk", q, k) * (D ** -0.5)
        off = Sk - Sq
        qi = torch.arange(Sq)[:, None]
        kj = torch.arange(Sk)[None, :]
        cmask = kj <= (qi + off)
        scores = torch.where(cmask[None, None], scores, -1e9)
        p = _softmax(scores, axis=-1)
        ctx = torch.einsum("bhqk,bhkd->bhqd", p, v)
        return torch.einsum("bhqd,hdm->bqm", ctx, w_o)

    def _B_ref(x, k_cache, v_cache, wq, wo):
        B, Sq, D = x.shape
        Hq, Dh = 32, 128
        Hkv = k_cache.shape[1]
        rep = Hq // Hkv
        q = (lambda x, *d: x.permute(*d))((x @ wq).reshape(B, Sq, Hq, Dh), 0, 2, 1, 3)
        k = (lambda x, n, axis: torch.repeat_interleave(x, n, dim=axis))(k_cache, rep, axis=1)
        v = (lambda x, n, axis: torch.repeat_interleave(x, n, dim=axis))(v_cache, rep, axis=1)
        scores = torch.einsum("bhqd,bhkd->bhqk", q, k) * (Dh ** -0.5)
        p = _softmax(scores, axis=-1)
        o = torch.einsum("bhqk,bhkd->bhqd", p, v)
        return (lambda x, *d: x.permute(*d))(o, 0, 2, 1, 3).reshape(B, Sq, Hq * Dh) @ wo

    a = _A_ref(A_x, A_c_kv, A_w_q, A_w_uk, A_w_uv, A_w_o)
    b = _B_ref(B_x, B_k_cache, B_v_cache, B_wq, B_wo)
    return torch.concatenate([a, b], axis=-1)
```

```cpp
#define PROBE 4
#include <hip/hip_runtime.h>
#include <hip/hip_bf16.h>
#include <stdint.h>

#define DI __device__ __forceinline__
typedef __attribute__((ext_vector_type(4))) unsigned u32x4;
typedef __attribute__((ext_vector_type(2))) unsigned u32x2;
typedef __attribute__((ext_vector_type(4))) float f32x4;
typedef __attribute__((ext_vector_type(2))) float f32x2;
typedef __attribute__((ext_vector_type(16))) float f32x16;
typedef __attribute__((ext_vector_type(8))) __bf16 bf16x8_t;
typedef __attribute__((ext_vector_type(2))) __bf16 bf16x2_t;
typedef __attribute__((ext_vector_type(4))) short s16x4;
typedef __attribute__((ext_vector_type(8))) short s16x8;
typedef unsigned short u16;
typedef unsigned long long u64;

constexpr int NS0 = 14, NS1 = 16, NS2 = 15, NS3 = 19;
constexpr int RA2 = 0xC0, RA3 = 0x02;
constexpr int AHL = 1;
constexpr u64 TL0 = 0xbeb96069644b1c0ull, TH0 = 0xfbcdf3ull, TL1 = 0x9217595523c9142ull, TH1 = 0xef8d30b28ull;
constexpr u64 TL2 = 0x9a279b5d338a183ull, TH2 = 0x3adb2baaull, TL3 = 0x7dc61440d308101ull, TH3 = 0x3ff79d71b699e3ull;
constexpr u64 LA2 = TH2 >> (6 * (NS2 - 11)), LA3 = TH3 >> (6 * (NS3 - 11));
constexpr u64 chunk_mask(u64 lo, u64 hi, int n) { u64 m = 0; for (int i = 0; i < n; ++i) m |= 1ull << ((i < 10 ? lo >> (6 * i) : hi >> (6 * (i - 10))) & 63); return m; }
static_assert((chunk_mask(TL0, TH0, NS0) | chunk_mask(TL1, TH1, NS1) | chunk_mask(TL2, TH2, NS2) | chunk_mask(TL3, TH3, NS3)) == ~0ull && (RA2 & RA3) == 0, "every chunk once");
static_assert(NS0 >= 12 && NS1 >= 12 && NS2 >= 13 && NS3 >= 13 && NS0 + NS1 + NS2 + NS3 == 64, "split sizes");
constexpr int SK = 4096;
constexpr float C2 = 0.08838834764831845f * 1.4426950408889634f;
constexpr u64 GOLD = 0x9E3779B97F4A7C15ull;

constexpr unsigned WS_EP = 0;
constexpr unsigned WS_QX = 4096;
constexpr unsigned WS_QF = WS_QX + 2 * 2 * 32 * 64 * 8;
constexpr unsigned WS_QA = WS_QF + 64 * 4 * 8;
constexpr unsigned WS_AML = WS_QA + 2 * 32 * 512 * 2;
constexpr unsigned WS_APO = WS_AML + 32 * 2 * 64 * 16;
constexpr unsigned WS_CX = WS_APO + 32 * 2 * 64 * 256 * 2;
constexpr unsigned WS_GB = WS_CX + 32 * 2 * 2 * 128 * 8;
constexpr unsigned WS_GZ = WS_GB + 2 * 128 * 512 * 8;
constexpr unsigned WS_GA = WS_GZ + 2 * 8 * 128 * 8;
constexpr unsigned WS_END = WS_GA + 64 * 2 * 512 * 8;

#define STAMP(i) do {} while (0)
#define STAMP_DECL const u64 pt0 = wall_clock64(); if (PROBE == 3 && blockIdx.x == 0 && threadIdx.x == 0) *reinterpret_cast<u64*>(ws + 2048) = pt0
#define STAMP_FLUSH(grp) do { \
    if (PROBE == 1) { if (blockIdx.x == 0 && threadIdx.x == 0) st8c(rs, 2048, u32x2{(unsigned)pt0, tg.hi}); \
      u32x2 g_; for (int sp_ = 0; sp_ < SPIN_MAX; ++sp_) { COMPILER_FENCE(); g_ = ld8c(rs, 2048); if (g_[1] == tg.hi) break; __builtin_amdgcn_s_sleep(8); } \
      const int d_ = (int)((unsigned)pt0 - g_[0]); const u64 tend = pt0 - (long long)d_ + 6000; while (wall_clock64() < tend) __builtin_amdgcn_s_sleep(2); } \
    if (PROBE == 2) { const u32x2 g_ = ld8c(rs, 2048); const u64 tend = (((u64)g_[1] << 32) | g_[0]) + 6000; for (int sp_ = 0; sp_ < SPIN_MAX && wall_clock64() < tend; ++sp_) __builtin_amdgcn_s_sleep(2); } \
  } while (0)

DI float bflo(unsigned u) { return __uint_as_float(u << 16); }
DI float bfhi(unsigned u) { return __uint_as_float(u & 0xffff0000u); }
DI unsigned pk_bf(float a, float b) { const f32x2 v = {a, b}; return __builtin_bit_cast(unsigned, __builtin_convertvector(v, bf16x2_t)); }
DI float dot2(unsigned k, unsigned q, float acc) {
  return __builtin_amdgcn_fdot2_f32_bf16(__builtin_bit_cast(bf16x2_t, k), __builtin_bit_cast(bf16x2_t, q), acc, false);
}
DI unsigned plo(unsigned w0, unsigned w1) { return __builtin_amdgcn_perm(w1, w0, 0x05040100u); }
DI unsigned phi(unsigned w0, unsigned w1) { return __builtin_amdgcn_perm(w1, w0, 0x07060302u); }
template <int CTRL> DI float dpp(float x) {
  return __builtin_bit_cast(float, __builtin_amdgcn_mov_dpp(__builtin_bit_cast(int, x), CTRL, 0xf, 0xf, true));
}
DI float row16_sum(float x) { x += dpp<0xB1>(x); x += dpp<0x4E>(x); x += dpp<0x141>(x); x += dpp<0x128>(x); return x; }
DI float row16_max(float x) { x = fmaxf(x, dpp<0xB1>(x)); x = fmaxf(x, dpp<0x4E>(x)); x = fmaxf(x, dpp<0x141>(x)); x = fmaxf(x, dpp<0x128>(x)); return x; }
DI float x16_sum(float x) { auto s = __builtin_amdgcn_permlane16_swap(__float_as_uint(x), __float_as_uint(x), false, false); return __uint_as_float(s[0]) + __uint_as_float(s[1]); }
DI float x16_max(float x) { auto s = __builtin_amdgcn_permlane16_swap(__float_as_uint(x), __float_as_uint(x), false, false); return fmaxf(__uint_as_float(s[0]), __uint_as_float(s[1])); }
DI float xhalf_max(float x) { auto t = __builtin_amdgcn_permlane32_swap(__float_as_uint(x), __float_as_uint(x), false, false); return fmaxf(__uint_as_float(t[0]), __uint_as_float(t[1])); }
DI float xhalf_sum(float x) { auto t = __builtin_amdgcn_permlane32_swap(__float_as_uint(x), __float_as_uint(x), false, false); return __uint_as_float(t[0]) + __uint_as_float(t[1]); }
DI float xrow_max(float x) { return xhalf_max(x16_max(x)); }
DI float xrow_sum(float x) { return xhalf_sum(x16_sum(x)); }
DI u32x4 ld16(const void* p) { return *reinterpret_cast<const u32x4*>(p); }
DI u32x4 ld16nt(const void* p) { return __builtin_nontemporal_load(reinterpret_cast<const u32x4*>(p)); }
DI float ex2(float x) { return __builtin_amdgcn_exp2f(x); }
#define SCHED() __builtin_amdgcn_sched_barrier(0)

typedef __attribute__((address_space(1))) u64 gu64;
typedef __amdgpu_buffer_rsrc_t rsrc_t;
DI u32x4 ld16c(rsrc_t rs, unsigned off) { return __builtin_amdgcn_raw_buffer_load_b128(rs, (int)off, 0, 16); }
DI u32x2 ld8c(rsrc_t rs, unsigned off) { return __builtin_amdgcn_raw_buffer_load_b64(rs, (int)off, 0, 16); }
DI void st16c(rsrc_t rs, unsigned off, u32x4 v) { __builtin_amdgcn_raw_buffer_store_b128(v, rs, (int)off, 0, 16); }
DI void st8c(rsrc_t rs, unsigned off, u32x2 v) { __builtin_amdgcn_raw_buffer_store_b64(v, rs, (int)off, 0, 16); }
constexpr int SPIN_MAX = 1 << 20;
constexpr unsigned OOB = 0x7ffffff0u;
DI void rawbar() { __builtin_amdgcn_sched_barrier(0); __builtin_amdgcn_s_barrier(); __builtin_amdgcn_sched_barrier(0); }
struct Tag { unsigned hi, lo8; };
DI unsigned g24(float v, const Tag& t) { return t.lo8 | ((__float_as_uint(v) + 0x80u) >> 8); }
DI bool ok24(unsigned lo, unsigned hi, const Tag& t) { return (hi == t.hi) & ((lo & 0xff000000u) == t.lo8); }
DI float v24(unsigned lo) { return __uint_as_float(lo << 8); }
#define COMPILER_FENCE() asm volatile("" ::: "memory")


template <int N> struct IC { static constexpr int v = N; };
template <int I, int N, class F> DI void sfor(F&& f) { if constexpr (I < N) { f(IC<I>{}); sfor<I + 1, N>(f); } }
typedef __attribute__((address_space(3))) unsigned lu32;
DI void lds_wait(unsigned* cnt, unsigned target) {
#pragma unroll 1
  for (int spin = 0; spin < SPIN_MAX; ++spin) {
    if (__builtin_amdgcn_readfirstlane(__hip_atomic_load((lu32*)cnt, __ATOMIC_RELAXED, __HIP_MEMORY_SCOPE_WORKGROUP)) >= target) break;
    __builtin_amdgcn_s_sleep(1);
  }
  COMPILER_FENCE();
}
DI void lds_hold(unsigned* flag) {
#pragma unroll 1
  for (int spin = 0; spin < SPIN_MAX; ++spin) {
    if (__builtin_amdgcn_readfirstlane(__hip_atomic_load((lu32*)flag, __ATOMIC_RELAXED, __HIP_MEMORY_SCOPE_WORKGROUP)) == 0u) break;
    __builtin_amdgcn_s_sleep(2);
  }
}
DI unsigned lds_arrive_nw(unsigned addr) {
  unsigned old, one = 1u;
  asm volatile("s_mov_b64 exec, 1\n\tds_add_rtn_u32 %0, %1, %2\n\ts_mov_b64 exec, -1" : "=v"(old) : "v"(addr), "v"(one) : "memory");
  return old;
}
DI unsigned lds_done(unsigned old) { asm volatile("s_waitcnt lgkmcnt(0)" : "+v"(old) :: "memory"); return __builtin_amdgcn_readfirstlane(old); }
DI unsigned lds_arrive(unsigned* cnt, int) { return lds_done(lds_arrive_nw((unsigned)(size_t)(lu32*)cnt)); }
DI void lds_add(unsigned* cnt, unsigned v, int) {
  asm volatile("s_mov_b64 exec, 1\n\tds_add_u32 %0, %1\n\ts_mov_b64 exec, -1" :: "v"((unsigned)(size_t)(lu32*)cnt), "v"(v) : "memory");
}
DI void sbar4(unsigned* cnt, int lane) { if (lds_arrive(cnt, lane) < 3u) lds_wait(cnt, 4u); }
DI float rs32(float a, float b) { auto t = __builtin_amdgcn_permlane32_swap(__float_as_uint(a), __float_as_uint(b), false, false); return __uint_as_float(t[0]) + __uint_as_float(t[1]); }
DI float rs16(float a, float b) { auto t = __builtin_amdgcn_permlane16_swap(__float_as_uint(a), __float_as_uint(b), false, false); return __uint_as_float(t[0]) + __uint_as_float(t[1]); }
template <int N> DI unsigned rowbcast(unsigned x) { return (unsigned)__builtin_amdgcn_mov_dpp((int)x, 0x150 + N, 0xf, 0xf, true); }
DI void pin2(u32x4& a, u32x4& b) { asm volatile("" : "+v"(a), "+v"(b)); }
DI float rdlane(float x, int l) { return __builtin_bit_cast(float, __builtin_amdgcn_readlane(__builtin_bit_cast(int, x), l)); }
DI bool nz8(const u32x4& v) { return ((v[0] | v[1]) != 0u) & ((v[2] | v[3]) != 0u); }

DI unsigned toff(unsigned row, unsigned ch) { return 256u * row + 16u * (ch ^ (((row & 3) << 2) | ((row >> 2) & 3))); }
constexpr int SMEM = 160 * 1024;
constexpr int L_SCR = 131072;
constexpr int L_MRG = 65536;
constexpr int L_CNT = L_SCR + 32 * 1008;

struct KV { u32x4 k[4], v[4]; };

__global__ __launch_bounds__(512, 2) void kmain(const u16* __restrict__ xA, const u16* __restrict__ ckv, const u16* __restrict__ w_q, const u16* __restrict__ w_uk,
                                                const u16* __restrict__ w_uv, const u16* __restrict__ w_o,
                                                const u16* __restrict__ xB, const u16* __restrict__ kc, const u16* __restrict__ vc,
                                                const u16* __restrict__ wq, const u16* __restrict__ wo,
                                                char* __restrict__ ws, u16* __restrict__ out) {
  __shared__ __attribute__((aligned(16))) char smem[SMEM];
  const int tid = threadIdx.x, blk = blockIdx.x, lane = tid & 63;
  const int wave = __builtin_amdgcn_readfirstlane(tid >> 6), w4 = wave & 3;
  const int cls = (blk >> 3) & 3;
  int idx = 8 * (blk & 7) + (blk >> 5);
  if (wave < 4) idx = (idx & ~7) | ((idx + (cls == 3 ? 2 : (cls == 2 ? 4 : 0))) & 7);
  const int h = idx >> 1, b = idx & 1;
  const rsrc_t rs = __builtin_amdgcn_make_buffer_rsrc(ws, 0, (int)WS_END, 0x00020000);
  const int r4 = lane >> 4, c16 = lane & 15;
  unsigned* cnt = reinterpret_cast<unsigned*>(smem + L_CNT);
  STAMP_DECL;
  STAMP(0);
  asm volatile("" :: "s"(xA), "s"(ckv), "s"(w_q), "s"(w_uk), "s"(w_uv), "s"(w_o), "s"(xB), "s"(kc), "s"(vc), "s"(wq), "s"(wo), "s"(ws), "s"(out));
  Tag tg;
  unsigned tlow;
  const u32x2 epw = __builtin_amdgcn_raw_buffer_load_b64(rs, (int)WS_EP, 0, 16);
  auto take_epoch = [&]() {
    const u64 t64 = (((u64)(unsigned)__builtin_amdgcn_readfirstlane(epw[1]) << 32) | (unsigned)__builtin_amdgcn_readfirstlane(epw[0])) + GOLD;
    tg.hi = (unsigned)(t64 >> 32); tlow = (unsigned)t64;
    asm volatile("s_waitcnt lgkmcnt(0)" : "+s"(tg.hi), "+s"(tlow) :: "memory");
    tg.lo8 = tlow & 0xff000000u;
  };
  if (wave >= 4) {
    if (tid - 256 < 16) cnt[tid - 256] = 0u;
    asm volatile("s_waitcnt lgkmcnt(0)" ::: "memory");
    rawbar();
    const int e2 = (RA2 >> (idx & 7)) & 1, e3 = (RA3 >> (idx & 7)) & 1;
    const int ns = cls == 0 ? NS0 : (cls == 1 ? NS1 + e2 + e3 : (cls == 2 ? NS2 - e2 : NS3 - e3));
    const size_t kvo = ((size_t)(b * 32 + h) * SK + w4 * 16) * 256;
    const char* kb = reinterpret_cast<const char*>(kc) + kvo;
    const char* vb = reinterpret_cast<const char*>(vc) + kvo;
    const unsigned lo = lane * 16u;
    u64 tlo = cls == 0 ? TL0 : (cls == 1 ? TL1 : (cls == 2 ? TL2 : TL3));
    u64 thi = cls == 0 ? TH0 : (cls == 1 ? TH1 | ((e2 ? LA2 : LA3) << (6 * (NS1 - 10))) : (cls == 2 ? TH2 : TH3));
    auto nexts = [&](unsigned) { const unsigned o = (((unsigned)tlo + (unsigned)idx) & 63u) << 14; tlo = (tlo >> 6) | ((thi & 63u) << 54); thi >>= 6; return o; };
    unsigned so = nexts(0u);
    KV A, B;
    u32x4 W[8];
    u32x4 T[4][8];
    auto ldp = [&](KV& S, auto P, unsigned o) {
      constexpr int p = P.v;
      constexpr int o0 = (p & 1) * 2048, o1 = o0 + 1024;
      if constexpr (p < 2) { const char* q = kb + o; S.k[2 * p] = ld16nt(q + lo + o0); S.k[2 * p + 1] = ld16nt(q + lo + o1); }
      else { const char* q = vb + o; S.v[2 * p - 4] = ld16nt(q + lo + o0); S.v[2 * p - 3] = ld16nt(q + lo + o1); }
    };
    auto landed = [&](KV& S, auto P) {
      constexpr int p = P.v;
      if constexpr (p < 2) asm volatile("" : "+v"(S.k[2 * p]), "+v"(S.k[2 * p + 1])); else asm volatile("" : "+v"(S.v[2 * p - 4]), "+v"(S.v[2 * p - 3]));
    };
    KV X;
    {
      const unsigned so1 = nexts(so), so2 = nexts(so1);
      sfor<0, 12>([&](auto I) {
        constexpr int i = I.v;
        if constexpr (i >= 4) { if constexpr (i < 8) landed(X, IC<i & 3>{}); else landed(B, IC<i & 3>{}); }
        if constexpr (i < 4) ldp(X, IC<i & 3>{}, so); else if constexpr (i < 8) ldp(B, IC<i & 3>{}, so1); else ldp(A, IC<i & 3>{}, so2);
        SCHED();
      });
      so = so2;
    }
    take_epoch();
    const char* wp = reinterpret_cast<const char*>(wo) + ((size_t)h * 128 + r4 * 8) * 1024 + w4 * 256 + c16 * 16;
    u32x4 qpk;
    {
      const u32x2 tw = ld8c(rs, lane == 0 ? WS_GB + ((b * 128 + h * 4 + cls) * 512 + 128 * w4) * 8 : (lane < 3 ? WS_GZ + ((b * 8 + w4 * 2 + lane - 1) * 128 + h * 4 + cls) * 8 : OOB));
      const unsigned qo = WS_QX + (((2 + b) * 32 + h) * 64 + 4 * c16) * 8;
#pragma unroll 1
      for (int spin = 0; spin < SPIN_MAX; ++spin) {
        COMPILER_FENCE();
        const u32x4 g0 = ld16c(rs, qo), g1 = ld16c(rs, qo + 16);
        qpk = u32x4{g0[0], g0[2], g1[0], g1[2]};
        if (__all((g0[1] == tg.hi) & (g0[3] == tg.hi) & (g1[1] == tg.hi) & (g1[3] == tg.hi))) break;
      }
      asm volatile("" :: "v"(tw));
    }
    STAMP(7);
    const bool holds = cls == 0;
    float m = -INFINITY, l = 0.f, acc[8];
#pragma unroll
    for (int k = 0; k < 8; ++k) acc[k] = 0.f;
    auto step = [&](KV& S, KV& N, auto REFILL, auto WR) {
      constexpr bool refill = REFILL.v;
      constexpr int wr = WR.v;
      unsigned sn = 0u;
      if constexpr (refill) sn = nexts(so);
      constexpr int ah = wr < 0 ? AHL : 0;
      auto point = [&](auto P) {
        if constexpr (refill) {
          if constexpr (P.v == 0 && ah > 0) { if (holds) lds_hold(cnt + 8); sfor<0, ah>([&](auto Q) { ldp(N, Q, sn); }); SCHED(); }
          landed(S, P);
          if constexpr (P.v + ah < 4) { if constexpr ((P.v & 1) == 0) { if (holds) lds_hold(cnt + 8); } ldp(N, IC<(P.v + ah) & 3>{}, sn); }
          if constexpr (wr >= 0 && wr < 8) W[(4 * wr + P.v) & 7] = ld16(wp + (((4 * wr + P.v) >> 3) * 32 + ((4 * wr + P.v) & 7)) * 1024);
          SCHED();
        }
      };
      point(IC<0>{});
      point(IC<1>{});
      float x[4];
#pragma unroll
      for (int t = 0; t < 4; ++t) {
        float d = 0.f;
#pragma unroll
        for (int k = 0; k < 4; ++k) d = dot2(S.k[t][k], qpk[k], d);
        x[t] = row16_sum(d);
      }
      const float mn = fmaxf(fmaxf(m, fmaxf(x[0], x[1])), fmaxf(x[2], x[3]));
      const float alpha = ex2((m - mn) * C2);
      m = mn;
      const float mc = mn * C2;
      unsigned p01 = pk_bf(ex2(fmaf(x[0], C2, -mc)), ex2(fmaf(x[1], C2, -mc))), p23 = pk_bf(ex2(fmaf(x[2], C2, -mc)), ex2(fmaf(x[3], C2, -mc)));
      l = dot2(p23, 0x3f803f80u, dot2(p01, 0x3f803f80u, l * alpha));
#pragma unroll
      for (int k = 0; k < 8; ++k) acc[k] *= alpha;
      asm volatile("" : "+v"(p01), "+v"(p23), "+v"(l));
      asm volatile("" : "+v"(acc[0]), "+v"(acc[1]), "+v"(acc[2]), "+v"(acc[3]), "+v"(acc[4]), "+v"(acc[5]), "+v"(acc[6]), "+v"(acc[7]));
      SCHED();
      point(IC<2>{});
#pragma unroll
      for (int k = 0; k < 4; ++k) { acc[2 * k] = dot2(plo(S.v[0][k], S.v[1][k]), p01, acc[2 * k]); acc[2 * k + 1] = dot2(phi(S.v[0][k], S.v[1][k]), p01, acc[2 * k + 1]); }
      asm volatile("" : "+v"(acc[0]), "+v"(acc[1]), "+v"(acc[2]), "+v"(acc[3]), "+v"(acc[4]), "+v"(acc[5]), "+v"(acc[6]), "+v"(acc[7]));
      SCHED();
      point(IC<3>{});
#pragma unroll
      for (int k = 0; k < 4; ++k) { acc[2 * k] = dot2(plo(S.v[2][k], S.v[3][k]), p23, acc[2 * k]); acc[2 * k + 1] = dot2(phi(S.v[2][k], S.v[3][k]), p23, acc[2 * k + 1]); }
      asm volatile("" : "+v"(acc[0]), "+v"(acc[1]), "+v"(acc[2]), "+v"(acc[3]), "+v"(acc[4]), "+v"(acc[5]), "+v"(acc[6]), "+v"(acc[7]));
      if constexpr (wr >= 1) {
        constexpr int t = (wr - 1) >> 1, j0 = ((wr - 1) & 1) * 2;
#pragma unroll
        for (int jj = 0; jj < 2; ++jj)
#pragma unroll
          for (int e = 0; e < 4; ++e) {
            const unsigned a_ = W[(4 * wr - 4 + 2 * jj) & 7][e], b_ = W[(4 * wr - 3 + 2 * jj) & 7][e];
            T[t][2 * e][j0 + jj] = plo(a_, b_); T[t][2 * e + 1][j0 + jj] = phi(a_, b_);
          }
#pragma unroll
        for (int e = 0; e < 8; e += 2) pin2(T[t][e], T[t][e + 1]);
      }
      if constexpr (refill) so = sn;
      SCHED();
    };
    step(B, B, IC<0>{}, IC<-1>{});
    step(A, B, IC<1>{}, IC<0>{});
    step(X, X, IC<0>{}, IC<-1>{});
    sfor<0, 4>([&](auto I) {
      step(B, A, IC<1>{}, IC<2 * I.v + 1>{});
      step(A, B, IC<1>{}, IC<2 * I.v + 2>{});
    });
    unsigned zoff = lane < 2 ? WS_GZ + ((b * 8 + w4 * 2 + lane) * 128 + h * 4 + cls) * 8 : OOB, yoff = WS_GB + ((b * 128 + h * 4 + cls) * 512 + 128 * w4 + 8 * c16 + 2 * r4) * 8;
    unsigned rwo = L_MRG + (w4 * 4 + r4) * 272 + c16 * 16, rro = L_MRG + c16 * 272 + r4 * 16, mwo = L_MRG + 16 * 272 + ((c16 & 1) * 16 + w4 * 4 + r4) * 4, mro = L_MRG + 16 * 272 + c16 * 4, m4o = L_MRG + 16 * 272 + r4 * 16;
    asm volatile("" : "+v"(zoff), "+v"(yoff), "+v"(rwo), "+v"(rro), "+v"(mwo), "+v"(mro), "+v"(m4o));
    const int nit = (ns - 12) >> 1;
#pragma unroll 1
    for (int i = 0; i < nit; ++i) {
      step(B, A, IC<1>{}, IC<-1>{});
      step(A, B, IC<1>{}, IC<-1>{});
    }
    lds_wait(cnt + 15, 4u);
    if (cls == 0) lds_wait(cnt + 1, 4u);
    if (ns & 1) { step(B, A, IC<1>{}, IC<-1>{}); step(A, A, IC<0>{}, IC<-1>{}); }
    else step(B, B, IC<0>{}, IC<-1>{});
    STAMP(1);
    *reinterpret_cast<u32x4*>(smem + rwo) = u32x4{pk_bf(acc[0], acc[1]), pk_bf(acc[2], acc[3]), pk_bf(acc[4], acc[5]), pk_bf(acc[6], acc[7])};
    *reinterpret_cast<float*>(smem + mwo) = (c16 & 1) ? l : m;
    {
      u32x4 af[4];
      float mr, lr;
      f32x4 m4;
      unsigned got = lds_arrive_nw(L_CNT), need = 3u;
#pragma unroll 1
      for (int spin = 0; spin < SPIN_MAX; ++spin) {
#pragma unroll
        for (int t = 0; t < 4; ++t) af[t] = *reinterpret_cast<const u32x4*>(smem + rro + t * 64);
        mr = *reinterpret_cast<const float*>(smem + mro); lr = *reinterpret_cast<const float*>(smem + mro + 64);
        m4 = *reinterpret_cast<const f32x4*>(smem + m4o);
        SCHED();
        if (lds_done(got) >= need) break;
        need = 4u;
        __builtin_amdgcn_s_sleep(1);
        asm volatile("ds_read_b32 %0, %1" : "=v"(got) : "v"((unsigned)L_CNT) : "memory");
      }
      SCHED();
      f32x4 d[8];
#pragma unroll
      for (int e = 0; e < 8; ++e) {
        d[e] = f32x4{0.f, 0.f, 0.f, 0.f};
#pragma unroll
        for (int t = 0; t < 4; ++t) d[e] = __builtin_amdgcn_mfma_f32_16x16x32_bf16(__builtin_bit_cast(bf16x8_t, af[t]), __builtin_bit_cast(bf16x8_t, T[t][e]), d[e], 0, 0, 0);
      }
      const float M = row16_max(mr);
      const float L = row16_sum(ex2((mr - M) * C2) * lr);
      const float z = fmaf(M, C2, __builtin_amdgcn_logf(L));
      st8c(rs, zoff, u32x2{__float_as_uint(z), tg.hi});
      float wt[4], a[8];
#pragma unroll
      for (int i = 0; i < 4; ++i) wt[i] = ex2(fmaf(m4[i], C2, -z));
#pragma unroll
      for (int e = 0; e < 8; ++e) a[e] = fmaf(wt[3], d[e][3], fmaf(wt[2], d[e][2], fmaf(wt[1], d[e][1], wt[0] * d[e][0])));
      float v4[4];
#pragma unroll
      for (int k = 0; k < 4; ++k) v4[k] = rs32(a[k], a[k + 4]);
      const float y0 = rs16(v4[0], v4[2]), y1 = rs16(v4[1], v4[3]);
      st16c(rs, yoff, u32x4{g24(y0, tg), tg.hi, g24(y1, tg), tg.hi});
    }
    STAMP(2);
    STAMP_FLUSH(1);
    return;
  }
  auto fin = [&](float y0, float y1, u16* dst) {
    y0 += dpp<0x128>(y0); y1 += dpp<0x128>(y1);
    const float y = xhalf_sum(rs16(y0, y1));
    float* fr = reinterpret_cast<float*>(smem + 32768);
    if (lane < 32 && (lane & 8) == 0) fr[w4 * 16 + 2 * (lane & 7) + r4] = y;
    COMPILER_FENCE();
    const unsigned got = lds_arrive_nw(L_CNT + 24);
    f32x2 t[4];
#pragma unroll
    for (int w = 0; w < 4; ++w) t[w] = *reinterpret_cast<const f32x2*>(fr + w * 16 + 2 * (lane & 7));
    SCHED();
    if (lds_done(got) == 3u) {
      const f32x2 r = (t[0] + t[1]) + (t[2] + t[3]);
      if (lane < 8) *reinterpret_cast<unsigned*>(dst + 2 * lane) = pk_bf(r[0], r[1]);
    }
  };
  u32x4 Wa[16], Wb[16], xv[4];
  const u16* pb; size_t sb, sb2;
  const unsigned qa_off = WS_QA + (((c16 >> 1) & 1) * 32 + h) * 1024 + (b * 256 + w4 * 64 + 16 * r4 + 8 * (c16 & 1)) * 2;
  {
    const u16* pa; size_t sa;
    if (cls & 1) {
      pa = (cls == 1 ? w_q : wq) + (size_t)(w4 * 128 + 16 * (lane >> 3)) * 4096 + h * 128 + b * 64 + 8 * (lane & 7); sa = 4096;
      pb = w_o + ((size_t)h * 128 + b * 64 + w4 * 2) * 512 + 8 * lane; sb = 512;
    } else if (cls == 2) {
      const size_t o_ = ((size_t)(b * 256 + w4 * 64 + 16 * r4) * 32 + h) * 128 + 8 * c16;
      pa = w_uk + o_; pb = w_uv + o_; sa = sb = 4096;
      if (c16 < 4) st16c(rs, qa_off, u32x4{0u, 0u, 0u, 0u});
    } else { pa = ckv + ((size_t)idx * 128 + w4) * 512 + 8 * lane; sa = sb = 2048; pb = pa + 64 * 512; }
    sb2 = cls == 1 ? 4096 : 2 * sb;
    const u16* xp = (cls == 1 ? xA : xB) + w4 * 128 + 16 * (lane >> 3);
#pragma unroll
    for (int i = 0; i < 4; ++i) xv[i] = ld16(xp + (i >> 1) * 512 + 8 * (i & 1));
    SCHED();
#pragma unroll
    for (int i = 0; i < 16; ++i) Wa[i] = ld16nt(pa + i * sa);
    SCHED();
    rawbar();
    take_epoch();
    lds_add(cnt + 15, 1u, lane);
  }
  STAMP(13);
  float* red = reinterpret_cast<float*>(smem);
  if (cls & 1) {
    float acc[2][8];
#pragma unroll
    for (int bb = 0; bb < 2; ++bb)
#pragma unroll
      for (int k = 0; k < 8; ++k) acc[bb][k] = 0.f;
#pragma unroll
    for (int ip = 0; ip < 8; ++ip) {
      const unsigned xp0 = xv[ip >> 2][ip & 3], xp1 = xv[2 + (ip >> 2)][ip & 3];
#pragma unroll
      for (int k = 0; k < 4; ++k) {
        const unsigned lo = plo(Wa[2 * ip][k], Wa[2 * ip + 1][k]), hi = phi(Wa[2 * ip][k], Wa[2 * ip + 1][k]);
        acc[0][2 * k] = dot2(lo, xp0, acc[0][2 * k]); acc[0][2 * k + 1] = dot2(hi, xp0, acc[0][2 * k + 1]);
        acc[1][2 * k] = dot2(lo, xp1, acc[1][2 * k]); acc[1][2 * k + 1] = dot2(hi, xp1, acc[1][2 * k + 1]);
      }
    }
    {
      float v8[8], v4[4];
#pragma unroll
      for (int k = 0; k < 8; ++k) v8[k] = rs32(acc[0][k], acc[1][k]);
#pragma unroll
      for (int k = 0; k < 4; ++k) { const float t = rs16(v8[k], v8[k + 4]); v4[k] = t + dpp<0x128>(t); }
      *reinterpret_cast<f32x4*>(red + (w4 * 2 + (lane >> 5)) * 64 + 8 * (lane & 7) + 4 * (r4 & 1)) = f32x4{v4[0], v4[1], v4[2], v4[3]};
    }
    COMPILER_FENCE();
    const unsigned got = lds_arrive_nw(L_CNT + 8);
    f32x4 t4[4];
#pragma unroll
    for (int w = 0; w < 4; ++w) t4[w] = *reinterpret_cast<const f32x4*>(red + w * 128 + 4 * (lane & 31));
    SCHED();
    if (lds_done(got) == 3u) {
      const f32x4 t = (t4[0] + t4[1]) + (t4[2] + t4[3]);
      st16c(rs, lane < 32 ? WS_QX + ((((cls >> 1) * 2 + r4) * 32 + h) * 64 + b * 32 + 2 * c16) * 8 : OOB, u32x4{pk_bf(t[0], t[1]), tg.hi, pk_bf(t[2], t[3]), tg.hi});
      SCHED();
      lds_add(cnt + 2, 1u, lane);
    } else lds_wait(cnt + 2, 5u);
    {
      const unsigned tw = __builtin_nontemporal_load(reinterpret_cast<const unsigned*>(out + (idx >> 5) * 1024 + (cls >> 1) * 512 + 16 * (idx & 31)));
      asm volatile("" :: "v"(tw));
    }
    STAMP(8);
  } else if (cls == 2) {
    asm volatile("s_waitcnt vmcnt(16)" ::: "memory");
    if (lane == 0) st8c(rs, WS_QF + (idx * 4 + w4) * 8, u32x2{0u, tg.hi});
    u32x4 q0, q1;
    const unsigned qo = WS_QX + (h * 64 + 4 * c16) * 8;
#pragma unroll 1
    for (int spin = 0; spin < SPIN_MAX; ++spin) {
      COMPILER_FENCE();
      const u32x4 g0 = ld16c(rs, qo), g1 = ld16c(rs, qo + 16), g2 = ld16c(rs, qo + 16384), g3 = ld16c(rs, qo + 16400);
      q0 = u32x4{g0[0], g0[2], g1[0], g1[2]}; q1 = u32x4{g2[0], g2[2], g3[0], g3[2]};
      if (__all((g0[1] == tg.hi) & (g0[3] == tg.hi) & (g1[1] == tg.hi) & (g1[3] == tg.hi) & (g2[1] == tg.hi) & (g2[3] == tg.hi) & (g3[1] == tg.hi) & (g3[3] == tg.hi))) break;
      __builtin_amdgcn_s_sleep(1);
    }
    STAMP(9);
    float v[8];
    {
      float u[16];
#pragma unroll
      for (int i = 0; i < 16; ++i) {
        float a = 0.f, c = 0.f;
#pragma unroll
        for (int k = 0; k < 4; ++k) { a = dot2(Wa[i][k], q0[k], a); c = dot2(Wa[i][k], q1[k], c); }
        u[i] = (c16 & 2 ? c : a) + dpp<0x4E>(c16 & 2 ? a : c);
        asm volatile("" : "+v"(u[i]));
      }
#pragma unroll
      for (int i = 0; i < 8; ++i) {
        float t = (c16 & 1 ? u[i + 8] : u[i]) + dpp<0xB1>(c16 & 1 ? u[i] : u[i + 8]);
        asm volatile("" : "+v"(t));
        t += dpp<0x124>(t);
        asm volatile("" : "+v"(t));
        t += dpp<0x128>(t);
        asm volatile("" : "+v"(t));
        v[i] = t;
      }
    }
    u32x4 o = {pk_bf(v[0], v[1]), pk_bf(v[2], v[3]), pk_bf(v[4], v[5]), pk_bf(v[6], v[7])};
    if ((o[0] | o[1]) == 0u) o[0] = 0x8000u;
    if ((o[2] | o[3]) == 0u) o[2] = 0x8000u;
    if (c16 < 4) st16c(rs, qa_off, o);
    if (lane == 0) st8c(rs, WS_QF + (idx * 4 + w4) * 8, u32x2{1u, tg.hi});
    SCHED();
    sbar4(cnt + 2, lane);
    STAMP(10);
  }
  asm volatile("" :: "v"(xv[0]), "v"(xv[1]), "v"(xv[2]), "v"(xv[3]));
  SCHED();
  if (cls == 3) {
    const int fb = idx >> 5, fc = 16 * (idx & 31), hd = 8 * w4 + (lane >> 3);
    const unsigned doff = WS_GB + ((fb * 128 + hd * 4) * 512 + fc + 2 * (lane & 7)) * 8, zoff = WS_GZ + ((idx >> 2) * 128 + hd * 4) * 8;
    lds_wait(cnt + 0, 4u);
    STAMP(3);
    u32x4 d[4], z0, z1;
    bool ok = false;
#pragma unroll 1
    for (int spin = 0; spin < SPIN_MAX; ++spin) {
      COMPILER_FENCE();
      if (!ok) {
#pragma unroll
        for (int j = 0; j < 4; ++j) d[j] = ld16c(rs, doff + j * 4096);
        z0 = ld16c(rs, zoff); z1 = ld16c(rs, zoff + 16);
      }
      unsigned eh = (z0[1] ^ tg.hi) | (z0[3] ^ tg.hi) | (z1[1] ^ tg.hi) | (z1[3] ^ tg.hi), el = 0u;
#pragma unroll
      for (int j = 0; j < 4; ++j) { eh |= (d[j][1] ^ tg.hi) | (d[j][3] ^ tg.hi); el |= (d[j][0] ^ tg.lo8) | (d[j][2] ^ tg.lo8); }
      ok = (eh | (el & 0xff000000u)) == 0u;
      if (__all(ok)) break;
    }
    STAMP(14);
    const float zz[4] = {__uint_as_float(z0[0]), __uint_as_float(z0[2]), __uint_as_float(z1[0]), __uint_as_float(z1[2])};
    const float Z = fmaxf(fmaxf(zz[0], zz[1]), fmaxf(zz[2], zz[3]));
    float L = 0.f, y0 = 0.f, y1 = 0.f;
#pragma unroll
    for (int j = 0; j < 4; ++j) { const float wt = ex2(zz[j] - Z); L += wt; y0 = fmaf(wt, v24(d[j][0]), y0); y1 = fmaf(wt, v24(d[j][2]), y1); }
    const float rl = __builtin_amdgcn_rcpf(L);
    fin(y0 * rl, y1 * rl, out + fb * 1024 + 512 + fc);
    STAMP(6);
    STAMP_FLUSH(0);
    return;
  }
#pragma unroll
  for (int i = 0; i < 16; ++i) {
    Wb[i] = ld16nt(pb + (i >> 1) * sb2 + (i & 1) * sb);
    if (i >= 2 && (i & 1)) asm volatile("" : "+v"(Wb[i - 2]));
    SCHED();
  }
  if (cls == 0) {
    char* scr = smem + L_SCR;
    const int ab = idx >> 5, r = lane & 31, hh = lane >> 5;
#pragma unroll
    for (int i = 0; i < 16; ++i) *reinterpret_cast<u32x4*>(smem + ((i >> 3) * 4 + r4) * 8192 + toff(4 * (i & 7) + w4, c16)) = Wa[i];
#pragma unroll
    for (int i = 0; i < 16; ++i) *reinterpret_cast<u32x4*>(smem + ((2 + (i >> 3)) * 4 + r4) * 8192 + toff(4 * (i & 7) + w4, c16)) = Wb[i];
    STAMP(8);
    u32x4 qlast;
    {
      if (w4 == 0) {
#pragma unroll 1
        for (int spin = 0; spin < SPIN_MAX; ++spin) {
          COMPILER_FENCE();
          const u32x4 f0 = ld16c(rs, WS_QF + lane * 32), f1 = ld16c(rs, WS_QF + lane * 32 + 16);
          if (__all((f0[1] == tg.hi) & (f0[3] == tg.hi) & (f1[1] == tg.hi) & (f1[3] == tg.hi) & ((f0[0] & f0[2] & f1[0] & f1[2]) == 1u))) break;
        }
        lds_add(cnt + 7, 1u, lane);
      } else lds_wait(cnt + 7, 1u);
      STAMP(12);
      u32x4 g[9];
      unsigned pend = 0x1ffu;
      const unsigned o8 = WS_QA + (ab * 32 + w4 * 8) * 1024 + lane * 16, ol = WS_QA + (ab * 32 + r) * 1024 + 992 + 16 * hh;
#pragma unroll 1
      for (int spin = 0; spin < SPIN_MAX && pend; ++spin) {
        COMPILER_FENCE();
        u32x4 t[9];
#pragma unroll
        for (int i = 0; i < 9; ++i) t[i] = ld16c(rs, (pend >> i & 1) ? (i < 8 ? o8 + i * 1024 : ol) : OOB);
#pragma unroll
        for (int i = 0; i < 9; ++i) {
          if (pend >> i & 1) g[i] = t[i];
          if (__all(nz8(t[i]))) pend &= ~(1u << i);
        }
      }
      qlast = g[8];
#pragma unroll
      for (int i = 0; i < 8; ++i)
        if (lane < 62) *reinterpret_cast<u32x4*>(scr + (w4 * 8 + i) * 1008 + 16 * lane) = g[i];
    }
    sbar4(cnt + 3, lane);
    STAMP(10);
    f32x16 sacc;
#pragma unroll
    for (int i = 0; i < 16; ++i) sacc[i] = 0.f;
#pragma unroll
    for (int jj = 0; jj < 32; ++jj) {
      const u32x4 af = *reinterpret_cast<const u32x4*>(smem + (w4 * 4 + (jj >> 3)) * 8192 + toff(r, 2 * (jj & 7) + hh));
      const u32x4 bf = jj < 31 ? *reinterpret_cast<const u32x4*>(scr + r * 1008 + 16 * (2 * (jj < 31 ? jj : 0) + hh)) : qlast;
      sacc = __builtin_amdgcn_mfma_f32_32x32x16_bf16(__builtin_bit_cast(bf16x8_t, af), __builtin_bit_cast(bf16x8_t, bf), sacc, 0, 0, 0);
    }
    float m = sacc[0];
#pragma unroll
    for (int i = 1; i < 16; ++i) m = fmaxf(m, sacc[i]);
    m = xhalf_max(m);
    if (hh == 0) *reinterpret_cast<float*>(scr + r * 1008 + 992 + 4 * w4) = m;
    sbar4(cnt + 4, lane);
    STAMP(11);
    {
      const f32x4 m4 = *reinterpret_cast<const f32x4*>(scr + r * 1008 + 992);
      m = fmaxf(fmaxf(m4[0], m4[1]), fmaxf(m4[2], m4[3]));
    }
    const float mc = m * C2;
    float p[16], ls = 0.f;
#pragma unroll
    for (int i = 0; i < 16; ++i) { p[i] = ex2(fmaf(sacc[i], C2, -mc)); ls += p[i]; }
    ls = xhalf_sum(ls);
    sbar4(cnt + 5, lane);
    u32x4* pf = reinterpret_cast<u32x4*>(scr);
    float* wl = reinterpret_cast<float*>(scr + 8192);
    if (hh == 0) wl[r * 4 + w4] = ls;
    pf[(2 * w4) * 64 + lane] = u32x4{pk_bf(p[0], p[1]), pk_bf(p[2], p[3]), pk_bf(p[4], p[5]), pk_bf(p[6], p[7])};
    pf[(2 * w4 + 1) * 64 + lane] = u32x4{pk_bf(p[8], p[9]), pk_bf(p[10], p[11]), pk_bf(p[12], p[13]), pk_bf(p[14], p[15])};
    sbar4(cnt + 6, lane);
    u32x4 pb8[8];
#pragma unroll
    for (int ks = 0; ks < 8; ++ks) pb8[ks] = pf[ks * 64 + lane];
    const f32x4 l4 = *reinterpret_cast<const f32x4*>(wl + r * 4);
    const int qq = (lane & 15) >> 2, pp = lane & 3, blk2 = (lane >> 4) & 1;
    char* ot = scr + 12288 + w4 * 4096;
#pragma unroll
    for (int c2 = 0; c2 < 2; ++c2) {
      f32x16 oacc[2];
#pragma unroll
      for (int ct = 0; ct < 2; ++ct)
#pragma unroll
        for (int i = 0; i < 16; ++i) oacc[ct][i] = 0.f;
#pragma unroll
      for (int ks = 0; ks < 8; ++ks)
#pragma unroll
        for (int ct = 0; ct < 2; ++ct) {
          typedef __attribute__((address_space(3))) s16x4* lp;
          const char* tile = smem + ((ks >> 1) * 4 + w4) * 8192;
          const int cc = 2 * c2 + ct;
          const s16x4 lo = __builtin_amdgcn_ds_read_tr16_b64_v4i16((lp)(tile + toff(16 * (ks & 1) + 4 * hh + qq, 4 * cc + 2 * blk2 + (pp >> 1)) + 8 * (pp & 1)));
          const s16x4 hi = __builtin_amdgcn_ds_read_tr16_b64_v4i16((lp)(tile + toff(16 * (ks & 1) + 8 + 4 * hh + qq, 4 * cc + 2 * blk2 + (pp >> 1)) + 8 * (pp & 1)));
          const s16x8 afr = __builtin_shufflevector(lo, hi, 0, 1, 2, 3, 4, 5, 6, 7);
          oacc[ct] = __builtin_amdgcn_mfma_f32_32x32x16_bf16(__builtin_bit_cast(bf16x8_t, afr), __builtin_bit_cast(bf16x8_t, pb8[ks]), oacc[ct], 0, 0, 0);
        }
#pragma unroll
      for (int ct = 0; ct < 2; ++ct)
#pragma unroll
        for (int g = 0; g < 4; ++g)
          *reinterpret_cast<u32x2*>(ot + r * 128 + 16 * ((4 * ct + g) ^ (r & 7)) + 8 * hh) = u32x2{pk_bf(oacc[ct][4 * g], oacc[ct][4 * g + 1]), pk_bf(oacc[ct][4 * g + 2], oacc[ct][4 * g + 3])};
      if (c2 == 0) lds_add(cnt + 8, 1u, lane);
      __builtin_amdgcn_wave_barrier();
#pragma unroll
      for (int i = 0; i < 4; ++i) {
        const int hd = 8 * i + (lane >> 3), pc = lane & 7;
        const u32x4 vv = *reinterpret_cast<const u32x4*>(ot + hd * 128 + 16 * (pc ^ (hd & 7)));
        st16c(rs, WS_APO + (((hd * 2 + (w4 >> 1)) * 64 + idx) * 256 + (w4 & 1) * 128 + 64 * c2 + 8 * pc) * 2, vv);
      }
      __builtin_amdgcn_wave_barrier();
    }
    lds_arrive(cnt + 1, lane);
    STAMP(1);
    asm volatile("s_waitcnt vmcnt(0)" ::: "memory");
    if (lds_arrive(cnt + 9 + (w4 >> 1), lane) == 1u && hh == 0)
      st16c(rs, WS_AML + ((r * 2 + (w4 >> 1)) * 64 + idx) * 16, u32x4{__float_as_uint(m), tg.hi, __float_as_uint((l4[0] + l4[1]) + (l4[2] + l4[3])), tg.hi});
    lds_add(cnt + 8, ~0u, lane);
    STAMP(2);
    if (idx == 0 && w4 == 0) {
      lds_wait(cnt + 0, 4u);
#pragma unroll 1
      for (int spin = 0; spin < SPIN_MAX; ++spin) {
        COMPILER_FENCE();
        const u32x4 t0 = ld16c(rs, WS_GZ + lane * 16), t1 = ld16c(rs, WS_GZ + 8 * 1024 + lane * 16);
        if (__all((t0[1] == tg.hi) & (t0[3] == tg.hi) & (t1[1] == tg.hi) & (t1[3] == tg.hi))) break;
        __builtin_amdgcn_s_sleep(1);
      }
      if (lane == 0) __hip_atomic_store((gu64*)(ws + WS_EP), ((u64)tg.hi << 32) | tlow, __ATOMIC_RELAXED, __HIP_MEMORY_SCOPE_AGENT);
    }
    STAMP_FLUSH(0);
    return;
  }
#pragma unroll
  for (int ip = 0; ip < 8; ++ip) {
#pragma unroll
    for (int k = 0; k < 4; ++k) { const unsigned lo = plo(Wb[2 * ip][k], Wb[2 * ip + 1][k]), hi = phi(Wb[2 * ip][k], Wb[2 * ip + 1][k]); Wb[2 * ip][k] = lo; Wb[2 * ip + 1][k] = hi; }
    pin2(Wb[2 * ip], Wb[2 * ip + 1]);
  }
  if (cls == 2) {
    float* red2 = red + 4 * 256;
    float* wsh = red + 3072;
    if (w4 == 0) {
      const unsigned fo = WS_AML + ((h * 2 + b) * 64 + lane) * 16;
#pragma unroll 1
      for (int spin = 0; spin < SPIN_MAX; ++spin) {
        COMPILER_FENCE();
        const u32x4 g = ld16c(rs, lane < 8 ? fo : OOB);
        if (__any(g[1] == tg.hi)) break;
        __builtin_amdgcn_s_sleep(8);
      }
      u32x4 g;
#pragma unroll 1
      for (int spin = 0; spin < SPIN_MAX; ++spin) {
        COMPILER_FENCE();
        g = ld16c(rs, fo);
        if (__all((g[1] == tg.hi) & (g[3] == tg.hi))) break;
      }
      lds_add(cnt + 7, 1u, lane);
      const float mu = __uint_as_float(g[0]), lu = __uint_as_float(g[2]);
      const float M = x16_max(row16_max(mu));
      const float wl = ex2((mu - M) * C2);
      wsh[lane] = wl / x16_sum(row16_sum(wl * lu));
      lds_arrive(cnt + 7, lane);
    } else lds_wait(cnt + 7, 1u);
    __builtin_amdgcn_fence(__ATOMIC_ACQUIRE, "wavefront");
    STAMP(3);
    u32x4 pp[8];
#pragma unroll
    for (int i = 0; i < 8; ++i) pp[i] = ld16c(rs, WS_APO + ((h * 2 + b) * 64 + w4 * 16 + 2 * i) * 512 + lane * 16);
    SCHED();
    lds_wait(cnt + 7, 2u);
    float a8[8];
#pragma unroll
    for (int k = 0; k < 8; ++k) a8[k] = 0.f;
#pragma unroll
    for (int i = 0; i < 8; ++i) {
      const float wt = wsh[w4 * 16 + 2 * i + (lane >> 5)];
#pragma unroll
      for (int k = 0; k < 4; ++k) { a8[2 * k] = fmaf(wt, bflo(pp[i][k]), a8[2 * k]); a8[2 * k + 1] = fmaf(wt, bfhi(pp[i][k]), a8[2 * k + 1]); }
    }
    *reinterpret_cast<f32x4*>(red + w4 * 256 + 8 * (lane & 31) + 4 * (lane >> 5)) = f32x4{rs32(a8[0], a8[4]), rs32(a8[1], a8[5]), rs32(a8[2], a8[6]), rs32(a8[3], a8[7])};
    sbar4(cnt + 3, lane);
    STAMP(4);
    float acc[2][8];
#pragma unroll
    for (int bb = 0; bb < 2; ++bb) {
      u32x4 cv[2];
#pragma unroll
      for (int e = 0; e < 2; ++e) {
        const float* src = red + bb * 512 + w4 * 64 + 16 * r4 + 8 * e;
        const f32x4 t0 = *reinterpret_cast<const f32x4*>(src) + *reinterpret_cast<const f32x4*>(src + 256), t1 = *reinterpret_cast<const f32x4*>(src + 4) + *reinterpret_cast<const f32x4*>(src + 260);
        cv[e] = u32x4{pk_bf(t0[0], t0[1]), pk_bf(t0[2], t0[3]), pk_bf(t1[0], t1[1]), pk_bf(t1[2], t1[3])};
      }
#pragma unroll
      for (int k = 0; k < 8; ++k) acc[bb][k] = 0.f;
#pragma unroll
      for (int ip = 0; ip < 8; ++ip)
#pragma unroll
        for (int k = 0; k < 4; ++k) { acc[bb][2 * k] = dot2(Wb[2 * ip][k], cv[ip >> 2][ip & 3], acc[bb][2 * k]); acc[bb][2 * k + 1] = dot2(Wb[2 * ip + 1][k], cv[ip >> 2][ip & 3], acc[bb][2 * k + 1]); }
    }
    {
      float v8[8], v4[4];
#pragma unroll
      for (int k = 0; k < 8; ++k) v8[k] = rs32(acc[0][k], acc[1][k]);
#pragma unroll
      for (int k = 0; k < 4; ++k) v4[k] = rs16(v8[k], v8[k + 4]);
      *reinterpret_cast<f32x4*>(red2 + (w4 * 2 + (lane >> 5)) * 128 + 8 * c16 + 4 * (r4 & 1)) = f32x4{v4[0], v4[1], v4[2], v4[3]};
    }
    {
      COMPILER_FENCE();
      const unsigned got = lds_arrive_nw(L_CNT + 16);
      f32x4 t4[4];
#pragma unroll
      for (int w = 0; w < 4; ++w) t4[w] = *reinterpret_cast<const f32x4*>(red2 + w * 256 + 4 * lane);
      SCHED();
      if (lds_done(got) == 3u) {
        const f32x4 t = (t4[0] + t4[1]) + (t4[2] + t4[3]);
        const unsigned co = WS_CX + ((h * 2 + b) * 256 + 64 * r4 + 32 * (c16 & 1) + 2 * (c16 >> 1)) * 8;
        st16c(rs, co, u32x4{__float_as_uint(t[0]), tg.hi, __float_as_uint(t[1]), tg.hi});
        st16c(rs, co + 128, u32x4{__float_as_uint(t[2]), tg.hi, __float_as_uint(t[3]), tg.hi});
      }
    }
    STAMP(12);
    STAMP_FLUSH(0);
    return;
  }
  {
    unsigned op;
    {
      u32x4 c;
      const unsigned co = WS_CX + ((h * 2 + ((lane >> 3) & 1)) * 256 + (lane >> 5) * 128 + b * 64 + w4 * 16 + 2 * (lane & 7)) * 8;
#pragma unroll 1
      for (int spin = 0; spin < SPIN_MAX; ++spin) {
        COMPILER_FENCE();
        c = ld16c(rs, co);
        if (__all((c[1] == tg.hi) & (c[3] == tg.hi))) break;
        __builtin_amdgcn_s_sleep(16);
      }
      float o0 = __uint_as_float(c[0]), o1 = __uint_as_float(c[2]);
      o0 += dpp<0x128>(o0); o1 += dpp<0x128>(o1);
      op = pk_bf(o0, o1);
    }
    STAMP(14);
    float* red3 = red + 4096;
    float a[2][8];
#pragma unroll
    for (int bb = 0; bb < 2; ++bb)
#pragma unroll
      for (int k = 0; k < 8; ++k) a[bb][k] = 0.f;
#pragma unroll
    for (int ip = 0; ip < 8; ++ip) {
      const unsigned v0 = __builtin_amdgcn_readlane(op, ip), v1 = __builtin_amdgcn_readlane(op, 32 + ip);
#pragma unroll
      for (int k = 0; k < 4; ++k) {
        a[0][2 * k] = dot2(Wb[2 * ip][k], v0, a[0][2 * k]); a[0][2 * k + 1] = dot2(Wb[2 * ip + 1][k], v0, a[0][2 * k + 1]);
        a[1][2 * k] = dot2(Wb[2 * ip][k], v1, a[1][2 * k]); a[1][2 * k + 1] = dot2(Wb[2 * ip + 1][k], v1, a[1][2 * k + 1]);
      }
    }
#pragma unroll
    for (int bb = 0; bb < 2; ++bb) {
      *reinterpret_cast<f32x4*>(red3 + (w4 * 2 + bb) * 512 + 8 * lane) = f32x4{a[bb][0], a[bb][1], a[bb][2], a[bb][3]};
      *reinterpret_cast<f32x4*>(red3 + (w4 * 2 + bb) * 512 + 8 * lane + 4) = f32x4{a[bb][4], a[bb][5], a[bb][6], a[bb][7]};
    }
    sbar4(cnt + 5, lane);
#pragma unroll
    for (int e = 0; e < 2; ++e) {
      f32x2 t = {0.f, 0.f};
#pragma unroll
      for (int w = 0; w < 4; ++w) t += *reinterpret_cast<const f32x2*>(red3 + w * 1024 + e * 512 + 2 * tid);
      st16c(rs, WS_GA + (idx * 1024 + e * 512 + 2 * tid) * 8, u32x4{g24(t[0], tg), tg.hi, g24(t[1], tg), tg.hi});
    }
    STAMP(5);
  }
  {
    const int fb = idx >> 5, fc = 16 * (idx & 31);
    const unsigned doff = WS_GA + ((w4 * 16 + (lane >> 3)) * 1024 + fb * 512 + fc + 2 * (lane & 7)) * 8;
    u32x4 d0, d1;
    bool ok = false;
#pragma unroll 1
    for (int spin = 0; spin < SPIN_MAX; ++spin) {
      COMPILER_FENCE();
      if (!ok) { d0 = ld16c(rs, doff); d1 = ld16c(rs, doff + 8 * 8192); }
      const unsigned eh = (d0[1] ^ tg.hi) | (d0[3] ^ tg.hi) | (d1[1] ^ tg.hi) | (d1[3] ^ tg.hi), el = (d0[0] ^ tg.lo8) | (d0[2] ^ tg.lo8) | (d1[0] ^ tg.lo8) | (d1[2] ^ tg.lo8);
      ok = (eh | (el & 0xff000000u)) == 0u;
      if (__all(ok)) break;
      __builtin_amdgcn_s_sleep(1);
    }
    fin(v24(d0[0]) + v24(d1[0]), v24(d0[2]) + v24(d1[2]), out + fb * 1024 + fc);
  }
  STAMP(6);
  STAMP_FLUSH(0);
}

__global__ __launch_bounds__(64) void k0(char* ws) { const u64 t = wall_clock64(); if (threadIdx.x == 0) *reinterpret_cast<u64*>(ws + 2048) = t; }
__global__ __launch_bounds__(64) void k2(char* ws) { const u64 tend = *reinterpret_cast<volatile u64*>(ws + 2048) + 6000; for (int sp_ = 0; sp_ < SPIN_MAX && wall_clock64() < tend; ++sp_) __builtin_amdgcn_s_sleep(2); }

extern "C" void kernel_launch(void* const* d_in, const int* in_sizes, int n_in, void* d_out, int out_size, void* d_ws, size_t ws_size, hipStream_t stream) {
  const u16* xA = (const u16*)d_in[0];
  const u16* ckv = (const u16*)d_in[1];
  const u16* w_q = (const u16*)d_in[2];
  const u16* w_uk = (const u16*)d_in[3];
  const u16* w_uv = (const u16*)d_in[4];
  const u16* w_o = (const u16*)d_in[5];
  const u16* xB = (const u16*)d_in[6];
  const u16* kc = (const u16*)d_in[7];
  const u16* vc = (const u16*)d_in[8];
  const u16* wq = (const u16*)d_in[9];
  const u16* wo = (const u16*)d_in[10];
  if (PROBE == 2 || PROBE == 4) k0<<<1, 64, 0, stream>>>((char*)d_ws);
  kmain<<<256, 512, 0, stream>>>(xA, ckv, w_q, w_uk, w_uv, w_o, xB, kc, vc, wq, wo, (char*)d_ws, (u16*)d_out);
  if (PROBE >= 3) k2<<<1, 64, 0, stream>>>((char*)d_ws);
}
```

```cpp
#include <hip/hip_runtime.h>
#include <hip/hip_bf16.h>
#include <stdint.h>

#define DI __device__ __forceinline__
typedef __attribute__((ext_vector_type(4))) unsigned u32x4;
typedef __attribute__((ext_vector_type(2))) unsigned u32x2;
typedef __attribute__((ext_vector_type(4))) float f32x4;
typedef __attribute__((ext_vector_type(2))) float f32x2;
typedef __attribute__((ext_vector_type(16))) float f32x16;
typedef __attribute__((ext_vector_type(8))) __bf16 bf16x8_t;
typedef __attribute__((ext_vector_type(2))) __bf16 bf16x2_t;
typedef __attribute__((ext_vector_type(4))) short s16x4;
typedef __attribute__((ext_vector_type(8))) short s16x8;
typedef unsigned short u16;
typedef unsigned long long u64;

#define EP 0
#define P4 0
#define QTAB 54,53,74,75, 55,53,76,72, 54,53,74,75, 55,49,76,76, 54,53,74,75, 55,49,76,76, 55,53,73,75, 55,53,72,76
constexpr int AHL = 1;
#define TS0 90
#define TS1 90
#define TS2 75
#define TS3 65
struct Tab { u64 lo[4], hi[128]; bool ok; };
constexpr int QX_[32] = {QTAB}, TSX[4] = {TS0, TS1, TS2, TS3};
constexpr int etime(int c, int n, int ns) { return n < 3 ? 4 * (10 + n * (TSX[c] - 10) / 3) + c : 4 * (TSX[c] + (n - 3) * (240 - TSX[c]) / (ns - 4)) + c; }
constexpr Tab make_tab() {
  Tab t = {};
  t.ok = true;
  const int nb[4] = {14, 14, 18, 18};
  u64 used = 0;
  for (int c = 0; c < 4; ++c)
    for (int n = 0; n < 10; ++n) {
      u64 rank = 0;
      for (int d = 0; d < 4; ++d)
        for (int m = 0; m < nb[d]; ++m) rank += etime(d, m, nb[d]) < etime(c, n, nb[c]);
      t.lo[c] |= rank << (6 * n); used |= 1ull << rank;
    }
  for (int r = 0; r < 8; ++r)
    for (int w = 0; w < 4; ++w) {
      int ns[4] = {}, first = 0, sum = 0;
      for (int c = 0; c < 4; ++c) { const int q = QX_[4 * r + c]; ns[c] = q / 4 + (((w - first) & 3) < q % 4); first += q % 4; sum += ns[c]; t.ok &= ns[c] >= 12 + P4 && ns[c] <= 20; }
      t.ok &= sum == 64;
      u64 seen = used;
      for (int c = 0; c < 4 && t.ok; ++c) {
        u64 v = (u64)(ns[c] - 10) << 60;
        for (int n = 10; n < ns[c]; ++n) {
          int rank = 0, k = 0;
          for (int d = 0; d < 4; ++d)
            for (int m = 10; m < ns[d]; ++m) rank += etime(d, m, ns[d]) < etime(c, n, ns[c]);
          for (;; ++k) if (!(used >> k & 1) && rank-- == 0) break;
          v |= (u64)k << (6 * (n - 10)); seen |= 1ull << k;
        }
        t.hi[(r * 4 + c) * 4 + w] = v;
      }
      t.ok &= seen == ~0ull;
    }
  return t;
}
constexpr Tab TB = make_tab();
static_assert(TB.ok, "every chunk once");
struct Hi { u64 v[128]; };
constexpr Hi make_hi() { Hi h = {}; for (int i = 0; i < 128; ++i) h.v[i] = TB.hi[i]; return h; }
__device__ constexpr Hi TH = make_hi();
#define PACE0 4
#define PACE1 1
#define ROT2 6
#define TQX 530
#define TAML 1800
#define QT 1
constexpr int SK = 4096;
constexpr float C2 = 0.08838834764831845f * 1.4426950408889634f;
constexpr u64 GOLD = 0x9E3779B97F4A7C15ull;

constexpr unsigned WS_EP = 0;
constexpr unsigned WS_QX = 4096;
constexpr unsigned WS_QA = WS_QX + 2 * 2 * 32 * 64 * 8;
constexpr unsigned WS_AML = WS_QA + 2 * 32 * 256 * 8;
constexpr unsigned WS_APO = WS_AML + 64 * 32 * 16;
constexpr unsigned WS_GB = WS_APO + 32 * 64 * 512 * 2;
constexpr unsigned WS_GA = WS_GB + 2 * 128 * 128 * 16;
constexpr unsigned WS_END = WS_GA + 64 * 2 * 128 * 16;
#define STAMP(i) do {} while (0)
#define STAMP_DECL do {} while (0)
#define STAMP_FLUSH(grp) do {} while (0)

#define TR_DECL do {} while (0)
#define TR() do {} while (0)
#define TR_FLUSH() do {} while (0)
DI float bflo(unsigned u) { return __uint_as_float(u << 16); }
DI float bfhi(unsigned u) { return __uint_as_float(u & 0xffff0000u); }
DI unsigned pk_bf(float a, float b) { const f32x2 v = {a, b}; return __builtin_bit_cast(unsigned, __builtin_convertvector(v, bf16x2_t)); }
DI float dot2(unsigned k, unsigned q, float acc) {
  return __builtin_amdgcn_fdot2_f32_bf16(__builtin_bit_cast(bf16x2_t, k), __builtin_bit_cast(bf16x2_t, q), acc, false);
}
DI unsigned plo(unsigned w0, unsigned w1) { return __builtin_amdgcn_perm(w1, w0, 0x05040100u); }
DI unsigned phi(unsigned w0, unsigned w1) { return __builtin_amdgcn_perm(w1, w0, 0x07060302u); }
template <int CTRL> DI float dpp(float x) {
  return __builtin_bit_cast(float, __builtin_amdgcn_mov_dpp(__builtin_bit_cast(int, x), CTRL, 0xf, 0xf, true));
}
DI float row16_sum(float x) { x += dpp<0xB1>(x); x += dpp<0x4E>(x); x += dpp<0x141>(x); x += dpp<0x128>(x); return x; }
DI float row16_max(float x) { x = fmaxf(x, dpp<0xB1>(x)); x = fmaxf(x, dpp<0x4E>(x)); x = fmaxf(x, dpp<0x141>(x)); x = fmaxf(x, dpp<0x128>(x)); return x; }
DI float x16_sum(float x) { auto s = __builtin_amdgcn_permlane16_swap(__float_as_uint(x), __float_as_uint(x), false, false); return __uint_as_float(s[0]) + __uint_as_float(s[1]); }
DI float x16_max(float x) { auto s = __builtin_amdgcn_permlane16_swap(__float_as_uint(x), __float_as_uint(x), false, false); return fmaxf(__uint_as_float(s[0]), __uint_as_float(s[1])); }
DI float xhalf_max(float x) { auto t = __builtin_amdgcn_permlane32_swap(__float_as_uint(x), __float_as_uint(x), false, false); return fmaxf(__uint_as_float(t[0]), __uint_as_float(t[1])); }
DI float xhalf_sum(float x) { auto t = __builtin_amdgcn_permlane32_swap(__float_as_uint(x), __float_as_uint(x), false, false); return __uint_as_float(t[0]) + __uint_as_float(t[1]); }
DI float xrow_max(float x) { return xhalf_max(x16_max(x)); }
DI float xrow_sum(float x) { return xhalf_sum(x16_sum(x)); }
DI u32x4 ld16(const void* p) { return *reinterpret_cast<const u32x4*>(p); }
DI u32x4 ld16nt(const void* p) { return __builtin_nontemporal_load(reinterpret_cast<const u32x4*>(p)); }
DI float ex2(float x) { return __builtin_amdgcn_exp2f(x); }
#define SCHED() __builtin_amdgcn_sched_barrier(0)

typedef __attribute__((address_space(1))) u64 gu64;
typedef __amdgpu_buffer_rsrc_t rsrc_t;
DI u32x4 ld16c(rsrc_t rs, unsigned off) { return __builtin_amdgcn_raw_buffer_load_b128(rs, (int)off, 0, 16); }
DI u32x2 ld8c(rsrc_t rs, unsigned off) { return __builtin_amdgcn_raw_buffer_load_b64(rs, (int)off, 0, 16); }
DI void st16c(rsrc_t rs, unsigned off, u32x4 v) { __builtin_amdgcn_raw_buffer_store_b128(v, rs, (int)off, 0, 16); }
DI void st8c(rsrc_t rs, unsigned off, u32x2 v) { __builtin_amdgcn_raw_buffer_store_b64(v, rs, (int)off, 0, 16); }
constexpr int SPIN_MAX = 1 << 20;
constexpr unsigned OOB = 0x7ffffff0u;
DI void rawbar() { __builtin_amdgcn_sched_barrier(0); __builtin_amdgcn_s_barrier(); __builtin_amdgcn_sched_barrier(0); }
struct Tag { unsigned hi, lo; };
#define COMPILER_FENCE() asm volatile("" ::: "memory")

template <int N> struct IC { static constexpr int v = N; };
template <int I, int N, class F> DI void sfor(F&& f) { if constexpr (I < N) { f(IC<I>{}); sfor<I + 1, N>(f); } }
typedef __attribute__((address_space(3))) unsigned lu32;
DI void lds_wait(unsigned* cnt, unsigned target) {
#pragma unroll 1
  for (int spin = 0; spin < SPIN_MAX; ++spin) {
    if (__builtin_amdgcn_readfirstlane(__hip_atomic_load((lu32*)cnt, __ATOMIC_RELAXED, __HIP_MEMORY_SCOPE_WORKGROUP)) >= target) break;
    __builtin_amdgcn_s_sleep(1);
  }
  COMPILER_FENCE();
}
DI void lds_hold(unsigned* flag) {
#pragma unroll 1
  for (int spin = 0; spin < SPIN_MAX; ++spin) {
    if (__builtin_amdgcn_readfirstlane(__hip_atomic_load((lu32*)flag, __ATOMIC_RELAXED, __HIP_MEMORY_SCOPE_WORKGROUP)) == 0u) break;
    __builtin_amdgcn_s_sleep(2);
  }
}
DI unsigned lds_arrive_nw(unsigned addr) {
  unsigned old, one = 1u;
  asm volatile("s_mov_b64 exec, 1\n\tds_add_rtn_u32 %0, %1, %2\n\ts_mov_b64 exec, -1" : "=v"(old) : "v"(addr), "v"(one) : "memory");
  return old;
}
DI unsigned lds_done(unsigned old) { asm volatile("s_waitcnt lgkmcnt(0)" : "+v"(old) :: "memory"); return __builtin_amdgcn_readfirstlane(old); }
DI unsigned lds_arrive(unsigned* cnt, int) { return lds_done(lds_arrive_nw((unsigned)(size_t)(lu32*)cnt)); }
DI void lds_add(unsigned* cnt, unsigned v, int) {
  asm volatile("s_mov_b64 exec, 1\n\tds_add_u32 %0, %1\n\ts_mov_b64 exec, -1" :: "v"((unsigned)(size_t)(lu32*)cnt), "v"(v) : "memory");
}
DI void sbar4(unsigned* cnt, int lane) { if (lds_arrive(cnt, lane) < 3u) lds_wait(cnt, 4u); }
DI float rs32(float a, float b) { auto t = __builtin_amdgcn_permlane32_swap(__float_as_uint(a), __float_as_uint(b), false, false); return __uint_as_float(t[0]) + __uint_as_float(t[1]); }
DI float rs16(float a, float b) { auto t = __builtin_amdgcn_permlane16_swap(__float_as_uint(a), __float_as_uint(b), false, false); return __uint_as_float(t[0]) + __uint_as_float(t[1]); }
template <int N> DI unsigned rowbcast(unsigned x) { return (unsigned)__builtin_amdgcn_mov_dpp((int)x, 0x150 + N, 0xf, 0xf, true); }
DI void pin2(u32x4& a, u32x4& b) { asm volatile("" : "+v"(a), "+v"(b)); }
DI float rdlane(float x, int l) { return __builtin_bit_cast(float, __builtin_amdgcn_readlane(__builtin_bit_cast(int, x), l)); }

DI unsigned toff(unsigned row, unsigned ch) { return 256u * row + 16u * (ch ^ (((row & 3) << 2) | ((row >> 2) & 3))); }
constexpr int SMEM = 160 * 1024;
constexpr int L_SCR = 131072;
constexpr int L_MRG = 65536;
constexpr int L_CNT = L_SCR + 32 * 1008;

struct KV { u32x4 k[4], v[4]; };

__global__ __launch_bounds__(512, 2) __attribute__((aligned(4096))) void kmain(const u16* __restrict__ kc, const u16* __restrict__ vc, char* __restrict__ ws, const u16* __restrict__ ckv, const u16* __restrict__ w_q, const u16* __restrict__ wq, const u16* __restrict__ w_uk,
                                                const u16* __restrict__ xA, const u16* __restrict__ xB,
                                                const u16* __restrict__ w_uv, const u16* __restrict__ w_o,
                                                const u16* __restrict__ wo,
                                                u16* __restrict__ out) {
  __shared__ __attribute__((aligned(16))) char smem[SMEM];
  const int tid = threadIdx.x, blk = blockIdx.x, lane = tid & 63;
  const int wave = __builtin_amdgcn_readfirstlane(tid >> 6), w4 = wave & 3;
  const int cls = (blk >> 3) & 3;
  int idx = 8 * (blk & 7) + (blk >> 5);
  if (wave < 4) idx = (idx & ~7) | ((idx + (cls == 3 ? 2 : (cls == 2 ? 4 : 0))) & 7);
  const int h = idx >> 1, b = idx & 1;
  const int idx2 = (idx & ~7) | ((idx + ROT2) & 7);
  const rsrc_t rs = __builtin_amdgcn_make_buffer_rsrc(ws, 0, (int)WS_END, 0x00020000);
  const int r4 = lane >> 4, c16 = lane & 15;
  unsigned* cnt = reinterpret_cast<unsigned*>(smem + L_CNT);
  STAMP_DECL;
  TR_DECL;
  STAMP(0);
  asm volatile("" :: "s"(xA), "s"(ckv), "s"(w_q), "s"(w_uk), "s"(w_uv), "s"(w_o), "s"(xB), "s"(kc), "s"(vc), "s"(wq), "s"(wo), "s"(ws), "s"(out));
  Tag tg;
  if (wave >= 4) {
    if (tid - 256 < 16) cnt[tid - 256] = 0u;
    asm volatile("s_waitcnt lgkmcnt(0)" ::: "memory");
    rawbar();
    const u64 thi = TH.v[((idx & 7) * 4 + cls) * 4 + w4];
    const size_t kvo = ((size_t)(b * 32 + h) * SK + w4 * 16) * 256;
    const char* kb = reinterpret_cast<const char*>(kc) + kvo;
    const char* vb = reinterpret_cast<const char*>(vc) + kvo;
    const unsigned lo = lane * 16u;
    u64 tlo = cls == 0 ? TB.lo[0] : (cls == 1 ? TB.lo[1] : (cls == 2 ? TB.lo[2] : TB.lo[3]));
    auto nexts = [&](unsigned) { const unsigned o = (((unsigned)tlo + (unsigned)idx) & 63u) << 14; tlo >>= 6; return o; };
    unsigned so = nexts(0u);
    KV A, B;
    u32x4 W[8];
    u32x4 T[4][8];
    auto ldp = [&](KV& S, auto P, unsigned o) {
      constexpr int p = P.v;
      constexpr int o0 = (p & 1) * 2048, o1 = o0 + 1024;
      if constexpr (p < 2) { const char* q = kb + o; S.k[2 * p] = ld16nt(q + lo + o0); S.k[2 * p + 1] = ld16nt(q + lo + o1); }
      else { const char* q = vb + o; S.v[2 * p - 4] = ld16nt(q + lo + o0); S.v[2 * p - 3] = ld16nt(q + lo + o1); }
    };
    auto landed = [&](KV& S, auto P) {
      constexpr int p = P.v;
      if constexpr (p < 2) asm volatile("" : "+v"(S.k[2 * p]), "+v"(S.k[2 * p + 1])); else asm volatile("" : "+v"(S.v[2 * p - 4]), "+v"(S.v[2 * p - 3]));
    };
    KV X, Y;
    const unsigned qo = WS_QX + (((2 + b) * 32 + h) * 64 + 4 * c16) * 8;
    u32x4 e0[2];
    u32x2 tw;
    {
      const unsigned so1 = nexts(so), so2 = nexts(so1), so3 = P4 ? nexts(so2) : so2;
      sfor<0, (P4 ? 16 : 12)>([&](auto I) {
        constexpr int i = I.v;
        if constexpr (i >= 4) { if constexpr (i < 8) landed(X, IC<i & 3>{}); else if constexpr (i < 12) landed(B, IC<i & 3>{}); else landed(A, IC<i & 3>{}); }
        if constexpr (i == 7 || i == 11 || i == 15) TR();
        if constexpr (i == 8) tw = ld8c(rs, lane == 0 ? WS_GB + (b * 128 + h * 4 + cls) * 2048 + 512 * w4 : OOB);
        if constexpr (EP && i == EP) { e0[0] = ld16c(rs, qo); e0[1] = ld16c(rs, qo + 16); }
        if constexpr (i < 4) ldp(X, IC<i & 3>{}, so); else if constexpr (i < 8) ldp(B, IC<i & 3>{}, so1); else if constexpr (i < 12) ldp(A, IC<i & 3>{}, so2); else ldp(Y, IC<i & 3>{}, so3);
        SCHED();
      });
      so = so3;
    }
    lds_wait(cnt + 15, 1u);
    tg.hi = __builtin_amdgcn_readfirstlane(cnt[13]); tg.lo = __builtin_amdgcn_readfirstlane(cnt[14]);
    asm volatile("" :: "v"(tw));
    const char* wp = reinterpret_cast<const char*>(wo) + ((size_t)h * 128 + r4 * 8) * 1024 + w4 * 256 + c16 * 16;
    u32x4 qpk;
    {
      auto qok = [&](const u32x4& g0, const u32x4& g1) {
        qpk = u32x4{g0[0], g0[2], g1[0], g1[2]};
        return __all((g0[1] == tg.hi) & (g0[3] == tg.hi) & (g1[1] == tg.hi) & (g1[3] == tg.hi));
      };
      if (!EP || !qok(e0[0], e0[1])) {
#pragma unroll 1
        for (int spin = 0; spin < SPIN_MAX; ++spin) {
          COMPILER_FENCE();
          const u32x4 g0 = ld16c(rs, qo), g1 = ld16c(rs, qo + 16);
          if (qok(g0, g1)) break;
        }
      }
    }
    STAMP(7);
    const bool holds = cls == 0;
    float m = -INFINITY, l = 0.f, acc[8];
#pragma unroll
    for (int k = 0; k < 8; ++k) acc[k] = 0.f;
    auto step = [&](KV& S, KV& N, auto REFILL, auto WR) {
      constexpr bool refill = REFILL.v;
      constexpr int wr = WR.v;
      unsigned sn = 0u;
      if constexpr (refill) sn = nexts(so);
      constexpr int ah = wr < 0 ? AHL : 0;
      auto point = [&](auto P) {
        if constexpr (refill) {
          if constexpr (P.v == 0 && ah > 0) { if (holds) lds_hold(cnt + 8); sfor<0, ah>([&](auto Q) { ldp(N, Q, sn); }); SCHED(); }
          landed(S, P);
          if constexpr (P.v + ah < 4) { if constexpr ((P.v & 1) == 0) { if (holds) lds_hold(cnt + 8); } ldp(N, IC<(P.v + ah) & 3>{}, sn); }
          if constexpr (wr >= 0 && wr < 8) W[(4 * wr + P.v) & 7] = ld16(wp + (((4 * wr + P.v) >> 3) * 32 + ((4 * wr + P.v) & 7)) * 1024);
          SCHED();
        }
      };
      point(IC<0>{});
      point(IC<1>{});
      float x[4];
#pragma unroll
      for (int t = 0; t < 4; ++t) {
        float d = 0.f;
#pragma unroll
        for (int k = 0; k < 4; ++k) d = dot2(S.k[t][k], qpk[k], d);
        x[t] = row16_sum(d);
      }
      const float mn = fmaxf(fmaxf(m, fmaxf(x[0], x[1])), fmaxf(x[2], x[3]));
      const float alpha = ex2((m - mn) * C2);
      m = mn;
      const float mc = mn * C2;
      unsigned p01 = pk_bf(ex2(fmaf(x[0], C2, -mc)), ex2(fmaf(x[1], C2, -mc))), p23 = pk_bf(ex2(fmaf(x[2], C2, -mc)), ex2(fmaf(x[3], C2, -mc)));
      l = dot2(p23, 0x3f803f80u, dot2(p01, 0x3f803f80u, l * alpha));
#pragma unroll
      for (int k = 0; k < 8; ++k) acc[k] *= alpha;
      asm volatile("" : "+v"(p01), "+v"(p23), "+v"(l));
      asm volatile("" : "+v"(acc[0]), "+v"(acc[1]), "+v"(acc[2]), "+v"(acc[3]), "+v"(acc[4]), "+v"(acc[5]), "+v"(acc[6]), "+v"(acc[7]));
      SCHED();
      point(IC<2>{});
#pragma unroll
      for (int k = 0; k < 4; ++k) { acc[2 * k] = dot2(plo(S.v[0][k], S.v[1][k]), p01, acc[2 * k]); acc[2 * k + 1] = dot2(phi(S.v[0][k], S.v[1][k]), p01, acc[2 * k + 1]); }
      asm volatile("" : "+v"(acc[0]), "+v"(acc[1]), "+v"(acc[2]), "+v"(acc[3]), "+v"(acc[4]), "+v"(acc[5]), "+v"(acc[6]), "+v"(acc[7]));
      SCHED();
      point(IC<3>{});
#pragma unroll
      for (int k = 0; k < 4; ++k) { acc[2 * k] = dot2(plo(S.v[2][k], S.v[3][k]), p23, acc[2 * k]); acc[2 * k + 1] = dot2(phi(S.v[2][k], S.v[3][k]), p23, acc[2 * k + 1]); }
      asm volatile("" : "+v"(acc[0]), "+v"(acc[1]), "+v"(acc[2]), "+v"(acc[3]), "+v"(acc[4]), "+v"(acc[5]), "+v"(acc[6]), "+v"(acc[7]));
      if constexpr (wr >= 1) {
        constexpr int t = (wr - 1) >> 1, j0 = ((wr - 1) & 1) * 2;
#pragma unroll
        for (int jj = 0; jj < 2; ++jj)
#pragma unroll
          for (int e = 0; e < 4; ++e) {
            const unsigned a_ = W[(4 * wr - 4 + 2 * jj) & 7][e], b_ = W[(4 * wr - 3 + 2 * jj) & 7][e];
            T[t][2 * e][j0 + jj] = plo(a_, b_); T[t][2 * e + 1][j0 + jj] = phi(a_, b_);
          }
#pragma unroll
        for (int e = 0; e < 8; e += 2) pin2(T[t][e], T[t][e + 1]);
      }
      if constexpr (refill) so = sn;
      SCHED();
      if constexpr (wr != -2) TR();
    };
    step(B, B, IC<0>{}, IC<-2>{});
    step(A, B, IC<1>{}, IC<0>{});
    step(X, X, IC<0>{}, IC<-2>{});
    sfor<0, 4>([&](auto I) {
      if constexpr (I.v == 3) tlo = thi;
      step(B, A, IC<1>{}, IC<2 * I.v + 1>{});
      step(A, B, IC<1>{}, IC<2 * I.v + 2>{});
    });
    unsigned yoff = (r4 & 1) ? OOB : WS_GB + (b * 128 + h * 4 + cls) * 2048 + 512 * w4 + 32 * c16 + 16 * (lane >> 5), zsh = 16 * (c16 & 1) + 8 * (lane >> 5);
    unsigned rwo = L_MRG + (w4 * 4 + r4) * 272 + c16 * 16, rro = L_MRG + c16 * 272 + r4 * 16, mwo = L_MRG + 16 * 272 + ((c16 & 1) * 16 + w4 * 4 + r4) * 4, mro = L_MRG + 16 * 272 + c16 * 4, m4o = L_MRG + 16 * 272 + r4 * 16;
    asm volatile("" : "+v"(zsh), "+v"(yoff), "+v"(rwo), "+v"(rro), "+v"(mwo), "+v"(mro), "+v"(m4o));
    const int ns = (int)(thi >> 60) + 10 - P4, nit = (ns - 12) >> 1;
#pragma unroll 1
    for (int i = 0; i < nit; ++i) {
      step(B, A, IC<1>{}, IC<-1>{});
      step(A, B, IC<1>{}, IC<-1>{});
    }
    if (cls == 0) lds_wait(cnt + 1, 4u);
    if (ns & 1) { step(B, A, IC<1>{}, IC<-1>{}); step(A, A, IC<0>{}, IC<-1>{}); }
    else step(B, B, IC<0>{}, IC<-1>{});
    STAMP(1);
    *reinterpret_cast<u32x4*>(smem + rwo) = u32x4{pk_bf(acc[0], acc[1]), pk_bf(acc[2], acc[3]), pk_bf(acc[4], acc[5]), pk_bf(acc[6], acc[7])};
    *reinterpret_cast<float*>(smem + mwo) = (c16 & 1) ? l : m;
    {
      u32x4 af[4];
      float mr, lr;
      f32x4 m4;
      unsigned got = lds_arrive_nw(L_CNT), need = 3u;
#pragma unroll 1
      for (int spin = 0; spin < SPIN_MAX; ++spin) {
#pragma unroll
        for (int t = 0; t < 4; ++t) af[t] = *reinterpret_cast<const u32x4*>(smem + rro + t * 64);
        mr = *reinterpret_cast<const float*>(smem + mro); lr = *reinterpret_cast<const float*>(smem + mro + 64);
        m4 = *reinterpret_cast<const f32x4*>(smem + m4o);
        SCHED();
        if (lds_done(got) >= need) break;
        need = 4u;
        __builtin_amdgcn_s_sleep(1);
        asm volatile("ds_read_b32 %0, %1" : "=v"(got) : "v"((unsigned)L_CNT) : "memory");
      }
      SCHED();
      f32x4 d[8];
#pragma unroll
      for (int e = 0; e < 8; ++e) {
        d[e] = f32x4{0.f, 0.f, 0.f, 0.f};
#pragma unroll
        for (int t = 0; t < 4; ++t) d[e] = __builtin_amdgcn_mfma_f32_16x16x32_bf16(__builtin_bit_cast(bf16x8_t, af[t]), __builtin_bit_cast(bf16x8_t, T[t][e]), d[e], 0, 0, 0);
      }
      const float M = row16_max(mr);
      const float L = row16_sum(ex2((mr - M) * C2) * lr);
      const float z = fmaf(M, C2, __builtin_amdgcn_logf(L));
      float wt[4], a[8];
#pragma unroll
      for (int i = 0; i < 4; ++i) wt[i] = ex2(fmaf(m4[i], C2, -z));
#pragma unroll
      for (int e = 0; e < 8; ++e) a[e] = fmaf(wt[3], d[e][3], fmaf(wt[2], d[e][2], fmaf(wt[1], d[e][1], wt[0] * d[e][0])));
      float v4[4];
#pragma unroll
      for (int k = 0; k < 4; ++k) v4[k] = x16_sum(rs32(a[k], a[k + 4]));
      st16c(rs, yoff, u32x4{pk_bf(v4[0], v4[1]), tg.hi, pk_bf(v4[2], v4[3]), (tg.lo & 0xffffff00u) | ((__float_as_uint(z) >> zsh) & 0xffu)});
    }
    STAMP(2);
    STAMP_FLUSH(1); TR_FLUSH();
    return;
  }
  const u64 tstart = wall_clock64();
  const u32x2 epw = __builtin_amdgcn_raw_buffer_load_b64(rs, (int)WS_EP, 0, 16);
  auto fin = [&](const float (&a)[4], u16* dst) {
    float y = rs16(rs32(a[0], a[2]), rs32(a[1], a[3]));
    y += dpp<0x124>(y); y += dpp<0x128>(y);
    float* fr = reinterpret_cast<float*>(smem + 32768);
    if ((lane & 12) == 0) fr[w4 * 16 + 4 * (lane & 3) + r4] = y;
    COMPILER_FENCE();
    const unsigned got = lds_arrive_nw(L_CNT + 24);
    f32x2 t[4];
#pragma unroll
    for (int w = 0; w < 4; ++w) t[w] = *reinterpret_cast<const f32x2*>(fr + w * 16 + 2 * (lane & 7));
    SCHED();
    if (lds_done(got) == 3u) {
      const f32x2 r = (t[0] + t[1]) + (t[2] + t[3]);
      if (lane < 8) *reinterpret_cast<unsigned*>(dst + 2 * lane) = pk_bf(r[0], r[1]);
    }
  };
  u32x4 Wa[16], Wb[16], xv[4];
  {
    const u16* pa; unsigned sa = 4096;
    if (cls & 1) pa = (cls == 1 ? w_q : wq) + (w4 * 128 + 16 * (lane >> 3)) * 4096 + h * 128 + b * 64 + 8 * (lane & 7);
    else if (cls == 2) pa = w_uk + ((b * 256 + w4 * 64 + 16 * r4) * 32 + h) * 128 + 8 * c16;
    else { pa = ckv + (idx * 128 + w4) * 512 + 8 * lane; sa = 2048; }
    const u16* xp = (cls == 1 ? xA : xB) + w4 * 128 + 16 * (lane >> 3);
#pragma unroll
    for (int i = 0; i < 4; ++i) xv[i] = ld16(xp + (i >> 1) * 512 + 8 * (i & 1));
    SCHED();
#pragma unroll
    for (int i = 0; i < 16; ++i) Wa[i] = ld16nt(pa + i * sa);
    SCHED();
    rawbar();
    const u64 t64 = (((u64)(unsigned)__builtin_amdgcn_readfirstlane(epw[1]) << 32) | (unsigned)__builtin_amdgcn_readfirstlane(epw[0])) + GOLD;
    tg.hi = (unsigned)(t64 >> 32); tg.lo = (unsigned)t64;
    asm volatile("" : "+s"(tg.hi), "+s"(tg.lo) :: "memory");
    if (w4 == 0) { cnt[13] = tg.hi; cnt[14] = tg.lo; COMPILER_FENCE(); lds_add(cnt + 15, 1u, lane); }
    STAMP(13);
  }
  float* red = reinterpret_cast<float*>(smem);
  auto gemv = [&](float* dst) {
    float acc[2][8];
#pragma unroll
    for (int bb = 0; bb < 2; ++bb)
#pragma unroll
      for (int k = 0; k < 8; ++k) acc[bb][k] = 0.f;
#pragma unroll
    for (int ip = 0; ip < 8; ++ip) {
      const unsigned xp0 = xv[ip >> 2][ip & 3], xp1 = xv[2 + (ip >> 2)][ip & 3];
#pragma unroll
      for (int k = 0; k < 4; ++k) {
        const unsigned lo = plo(Wa[2 * ip][k], Wa[2 * ip + 1][k]), hi = phi(Wa[2 * ip][k], Wa[2 * ip + 1][k]);
        acc[0][2 * k] = dot2(lo, xp0, acc[0][2 * k]); acc[0][2 * k + 1] = dot2(hi, xp0, acc[0][2 * k + 1]);
        acc[1][2 * k] = dot2(lo, xp1, acc[1][2 * k]); acc[1][2 * k + 1] = dot2(hi, xp1, acc[1][2 * k + 1]);
      }
    }
    float v8[8], v4[4];
#pragma unroll
    for (int k = 0; k < 8; ++k) v8[k] = rs32(acc[0][k], acc[1][k]);
#pragma unroll
    for (int k = 0; k < 4; ++k) { const float t = rs16(v8[k], v8[k + 4]); v4[k] = t + dpp<0x128>(t); }
    *reinterpret_cast<f32x4*>(dst + (lane >> 5) * 64 + 8 * (lane & 7) + 4 * (r4 & 1)) = f32x4{v4[0], v4[1], v4[2], v4[3]};
    COMPILER_FENCE();
  };
  if (cls & 1) {
    gemv(red + w4 * 128);
    const unsigned got = lds_arrive_nw(L_CNT + 8);
    f32x4 t4[4];
#pragma unroll
    for (int w = 0; w < 4; ++w) t4[w] = *reinterpret_cast<const f32x4*>(red + w * 128 + 4 * (lane & 31));
    SCHED();
    if (lds_done(got) == 3u) {
      const f32x4 t = (t4[0] + t4[1]) + (t4[2] + t4[3]);
      st16c(rs, lane < 32 ? WS_QX + ((((cls >> 1) * 2 + r4) * 32 + h) * 64 + b * 32 + 2 * c16) * 8 : OOB, u32x4{pk_bf(t[0], t[1]), tg.hi, pk_bf(t[2], t[3]), tg.hi});
      SCHED();
      lds_add(cnt + 2, 1u, lane);
    } else lds_wait(cnt + 2, 5u);
    STAMP(8);
  } else if (cls == 2) {
    while (wall_clock64() - tstart < TQX) __builtin_amdgcn_s_sleep(4);
    u32x4 q0, q1;
    const unsigned qo = WS_QX + (h * 64 + 4 * c16) * 8;
#pragma unroll 1
    for (int spin = 0; spin < SPIN_MAX; ++spin) {
      COMPILER_FENCE();
      const u32x4 g0 = ld16c(rs, qo), g1 = ld16c(rs, qo + 16), g2 = ld16c(rs, qo + 16384), g3 = ld16c(rs, qo + 16400);
      q0 = u32x4{g0[0], g0[2], g1[0], g1[2]}; q1 = u32x4{g2[0], g2[2], g3[0], g3[2]};
      if (__all((g0[1] == tg.hi) & (g0[3] == tg.hi) & (g1[1] == tg.hi) & (g1[3] == tg.hi) & (g2[1] == tg.hi) & (g2[3] == tg.hi) & (g3[1] == tg.hi) & (g3[3] == tg.hi))) break;
      __builtin_amdgcn_s_sleep(1);
    }
    STAMP(9);
    float v[4];
    {
      float u[16], t8[8];
#pragma unroll
      for (int i = 0; i < 16; ++i) {
        float a = 0.f, c = 0.f;
#pragma unroll
        for (int k = 0; k < 4; ++k) { a = dot2(Wa[i][k], q0[k], a); c = dot2(Wa[i][k], q1[k], c); }
        u[i] = (c16 & 4 ? c : a) + dpp<0x141>(c16 & 4 ? a : c);
        asm volatile("" : "+v"(u[i]));
      }
#pragma unroll
      for (int i = 0; i < 8; ++i) { t8[i] = (c16 & 2 ? u[i + 8] : u[i]) + dpp<0x4E>(c16 & 2 ? u[i] : u[i + 8]); asm volatile("" : "+v"(t8[i])); }
#pragma unroll
      for (int i = 0; i < 4; ++i) {
        float t = (c16 & 1 ? t8[i + 4] : t8[i]) + dpp<0xB1>(c16 & 1 ? t8[i] : t8[i + 4]);
        asm volatile("" : "+v"(t));
        v[i] = t + dpp<0x128>(t);
      }
    }
    st16c(rs, c16 < 8 ? WS_QA + ((c16 >> 2) * 32 + h) * 2048 + (b * 256 + w4 * 64 + 16 * r4 + 4 * (c16 & 3)) * 4 : OOB, u32x4{pk_bf(v[0], v[1]), tg.hi, pk_bf(v[2], v[3]), tg.hi});
    STAMP(10);
  }
  if (cls >= 2) {
    const unsigned tw = __builtin_nontemporal_load(reinterpret_cast<const unsigned*>(out + (idx >> 5) * 1024 + (cls & 1) * 512 + 16 * (idx & 31)));
    asm volatile("" :: "v"(tw));
  }
  asm volatile("" :: "v"(xv[0]), "v"(xv[1]), "v"(xv[2]), "v"(xv[3]));
  SCHED();
  if (cls == 3) {
    const int fb = idx >> 5;
    const unsigned doff = WS_GB + (fb * 128 + 32 * w4 + (lane >> 2)) * 2048 + (idx & 31) * 64 + (lane & 3) * 16;
    lds_wait(cnt + 0, 4u);
    STAMP(3);
    u32x4 d[2];
    bool ok = false;
#pragma unroll 1
    for (int spin = 0; spin < SPIN_MAX; ++spin) {
      COMPILER_FENCE();
      if (!ok) { d[0] = ld16c(rs, doff); d[1] = ld16c(rs, doff + 32768); }
      ok = (((d[0][1] ^ tg.hi) | (d[1][1] ^ tg.hi)) | (((d[0][3] ^ tg.lo) | (d[1][3] ^ tg.lo)) >> 8)) == 0u;
      if (__all(ok)) break;
    }
    STAMP(14);
    float a[4] = {0.f, 0.f, 0.f, 0.f};
#pragma unroll
    for (int j = 0; j < 2; ++j) {
      unsigned zb = (d[j][3] & 0xffu) << (8 * (lane & 3));
      zb |= __float_as_uint(dpp<0xB1>(__uint_as_float(zb))); zb |= __float_as_uint(dpp<0x4E>(__uint_as_float(zb)));
      const float z = __uint_as_float(zb);
      float Z = fmaxf(z, dpp<0x124>(z)); Z = fmaxf(Z, dpp<0x128>(Z));
      const float wt = ex2(z - Z);
      float L = wt + dpp<0x124>(wt); L += dpp<0x128>(L);
      const float w = wt * __builtin_amdgcn_rcpf(L);
      a[0] = fmaf(w, bflo(d[j][0]), a[0]); a[1] = fmaf(w, bfhi(d[j][0]), a[1]); a[2] = fmaf(w, bflo(d[j][2]), a[2]); a[3] = fmaf(w, bfhi(d[j][2]), a[3]);
    }
    fin(a, out + fb * 1024 + 512 + 16 * (idx & 31));
    STAMP(6);
    STAMP_FLUSH(0); TR_FLUSH();
    return;
  }
  if (cls == 0) {
    {
      const u16* pb = ckv + (idx * 128 + 64 + w4) * 512 + 8 * lane;
#pragma unroll
      for (int i = 0; i < 16; ++i) {
        Wb[i] = ld16nt(pb + i * 2048);
        if (i >= PACE0 && (i & 1)) asm volatile("" : "+v"(Wb[i - PACE0]));
        if (i == 9) TR();
        SCHED();
      }
    }
    char* scr = smem + L_SCR;
    const int ab = idx >> 5, r = lane & 31, hh = lane >> 5;
#pragma unroll
    for (int i = 0; i < 16; ++i) *reinterpret_cast<u32x4*>(smem + ((i >> 3) * 4 + r4) * 8192 + toff(4 * (i & 7) + w4, c16)) = Wa[i];
#pragma unroll
    for (int i = 0; i < 16; ++i) *reinterpret_cast<u32x4*>(smem + ((2 + (i >> 3)) * 4 + r4) * 8192 + toff(4 * (i & 7) + w4, c16)) = Wb[i];
    STAMP(8);
    TR();
    u32x4 qlast = {0u, 0u, 0u, 0u};
    {
      u32x4 g[18];
      const unsigned o8 = WS_QA + (ab * 32 + w4 * 8) * 2048 + lane * 16, ol = WS_QA + (ab * 32 + r) * 2048 + 1984 + 32 * hh;
      unsigned pend = 0x3ffffu;
#pragma unroll 1
      for (int spin = 0; spin < SPIN_MAX && pend; ++spin) {
        COMPILER_FENCE();
#pragma unroll
        for (int i = 0; i < 18; ++i) g[i] = ld16c(rs, (pend >> i & 1) ? (i < 16 ? o8 + i * 1024 : ol + 16 * (i - 16)) : OOB);
#pragma unroll
        for (int i = 0; i < 18; ++i) {
          if ((pend >> i & 1) && __all((g[i][1] == tg.hi) & (g[i][3] == tg.hi))) {
            pend &= ~(1u << i);
            if (i >= 16) { qlast[2 * (i - 16)] = g[i][0]; qlast[2 * (i - 16) + 1] = g[i][2]; }
            else if (!(i & 1) || lane < 60) *reinterpret_cast<u32x2*>(scr + (w4 * 8 + (i >> 1)) * 1008 + (i & 1) * 512 + 8 * lane) = u32x2{g[i][0], g[i][2]};
          }
        }
        if (pend) __builtin_amdgcn_s_sleep(8);
      }
    }
    sbar4(cnt + 3, lane);
    STAMP(10);
    f32x16 sacc;
#pragma unroll
    for (int i = 0; i < 16; ++i) sacc[i] = 0.f;
#pragma unroll
    for (int jj = 0; jj < 32; ++jj) {
      const u32x4 af = *reinterpret_cast<const u32x4*>(smem + (w4 * 4 + (jj >> 3)) * 8192 + toff(r, 2 * (jj & 7) + hh));
      const u32x4 bf = jj < 31 ? *reinterpret_cast<const u32x4*>(scr + r * 1008 + 16 * (2 * (jj < 31 ? jj : 0) + hh)) : qlast;
      sacc = __builtin_amdgcn_mfma_f32_32x32x16_bf16(__builtin_bit_cast(bf16x8_t, af), __builtin_bit_cast(bf16x8_t, bf), sacc, 0, 0, 0);
    }
    float m = sacc[0];
#pragma unroll
    for (int i = 1; i < 16; ++i) m = fmaxf(m, sacc[i]);
    m = xhalf_max(m);
    if (hh == 0) *reinterpret_cast<float*>(scr + r * 1008 + 992 + 4 * w4) = m;
    sbar4(cnt + 4, lane);
    STAMP(11);
    {
      const f32x4 m4 = *reinterpret_cast<const f32x4*>(scr + r * 1008 + 992);
      m = fmaxf(fmaxf(m4[0], m4[1]), fmaxf(m4[2], m4[3]));
    }
    const float mc = m * C2;
    float p[16], ls = 0.f;
#pragma unroll
    for (int i = 0; i < 16; ++i) { p[i] = ex2(fmaf(sacc[i], C2, -mc)); ls += p[i]; }
    ls = xhalf_sum(ls);
    sbar4(cnt + 5, lane);
    u32x4* pf = reinterpret_cast<u32x4*>(scr);
    float* wl = reinterpret_cast<float*>(scr + 8192);
    if (hh == 0) wl[r * 4 + w4] = ls;
    pf[(2 * w4) * 64 + lane] = u32x4{pk_bf(p[0], p[1]), pk_bf(p[2], p[3]), pk_bf(p[4], p[5]), pk_bf(p[6], p[7])};
    pf[(2 * w4 + 1) * 64 + lane] = u32x4{pk_bf(p[8], p[9]), pk_bf(p[10], p[11]), pk_bf(p[12], p[13]), pk_bf(p[14], p[15])};
    sbar4(cnt + 6, lane);
    u32x4 pb8[8];
#pragma unroll
    for (int ks = 0; ks < 8; ++ks) pb8[ks] = pf[ks * 64 + lane];
    const f32x4 l4 = *reinterpret_cast<const f32x4*>(wl + r * 4);
    const int qq = (lane & 15) >> 2, pp = lane & 3, blk2 = (lane >> 4) & 1;
    char* ot = scr + 12288 + w4 * 4096;
#pragma unroll
    for (int c2 = 0; c2 < 2; ++c2) {
      f32x16 oacc[2];
#pragma unroll
      for (int ct = 0; ct < 2; ++ct)
#pragma unroll
        for (int i = 0; i < 16; ++i) oacc[ct][i] = 0.f;
#pragma unroll
      for (int ks = 0; ks < 8; ++ks)
#pragma unroll
        for (int ct = 0; ct < 2; ++ct) {
          typedef __attribute__((address_space(3))) s16x4* lp;
          const char* tile = smem + ((ks >> 1) * 4 + w4) * 8192;
          const int cc = 2 * c2 + ct;
          const s16x4 lo = __builtin_amdgcn_ds_read_tr16_b64_v4i16((lp)(tile + toff(16 * (ks & 1) + 4 * hh + qq, 4 * cc + 2 * blk2 + (pp >> 1)) + 8 * (pp & 1)));
          const s16x4 hi = __builtin_amdgcn_ds_read_tr16_b64_v4i16((lp)(tile + toff(16 * (ks & 1) + 8 + 4 * hh + qq, 4 * cc + 2 * blk2 + (pp >> 1)) + 8 * (pp & 1)));
          const s16x8 afr = __builtin_shufflevector(lo, hi, 0, 1, 2, 3, 4, 5, 6, 7);
          oacc[ct] = __builtin_amdgcn_mfma_f32_32x32x16_bf16(__builtin_bit_cast(bf16x8_t, afr), __builtin_bit_cast(bf16x8_t, pb8[ks]), oacc[ct], 0, 0, 0);
        }
#pragma unroll
      for (int ct = 0; ct < 2; ++ct)
#pragma unroll
        for (int g = 0; g < 4; ++g)
          *reinterpret_cast<u32x2*>(ot + r * 128 + 16 * ((4 * ct + g) ^ (r & 7)) + 8 * hh) = u32x2{pk_bf(oacc[ct][4 * g], oacc[ct][4 * g + 1]), pk_bf(oacc[ct][4 * g + 2], oacc[ct][4 * g + 3])};
      if (c2 == 0) lds_add(cnt + 8, 1u, lane);
      __builtin_amdgcn_wave_barrier();
#pragma unroll
      for (int i = 0; i < 4; ++i) {
        const int hd = 8 * i + (lane >> 3), pc = lane & 7;
        const u32x4 vv = *reinterpret_cast<const u32x4*>(ot + hd * 128 + 16 * (pc ^ (hd & 7)));
        st16c(rs, WS_APO + ((hd * 64 + idx) * 512 + w4 * 128 + 64 * c2 + 8 * pc) * 2, vv);
      }
      __builtin_amdgcn_wave_barrier();
    }
    lds_arrive(cnt + 1, lane);
    STAMP(1);
    asm volatile("s_waitcnt vmcnt(0)" ::: "memory");
    if (lds_arrive(cnt + 9, lane) == 3u && hh == 0)
      st16c(rs, WS_AML + (idx * 32 + r) * 16, u32x4{__float_as_uint(m), tg.hi, __float_as_uint((l4[0] + l4[1]) + (l4[2] + l4[3])), tg.hi});
    lds_add(cnt + 8, ~0u, lane);
    STAMP(2);
    if (idx == 0 && w4 == 0) {
      lds_wait(cnt + 0, 4u);
#pragma unroll 1
      for (int spin = 0; spin < SPIN_MAX; ++spin) {
        COMPILER_FENCE();
        const u32x2 t0 = ld8c(rs, WS_AML + lane * 512), t1 = ld8c(rs, WS_QX + (64 * 64 + lane * 32) * 8);
        if (__all((t0[1] == tg.hi) & (t1[1] == tg.hi))) break;
        __builtin_amdgcn_s_sleep(8);
      }
      if (lane == 0) __hip_atomic_store((gu64*)(ws + WS_EP), ((u64)tg.hi << 32) | tg.lo, __ATOMIC_RELAXED, __HIP_MEMORY_SCOPE_AGENT);
    }
    STAMP_FLUSH(0); TR_FLUSH();
    return;
  }
  if (cls == 1) {
    const int idx = idx2, h = idx >> 1, b = idx & 1;
    {
      const u16* pa = w_uv + (w4 * 128 + 16 * (lane >> 3)) * 4096 + h * 128 + b * 64 + 8 * (lane & 7);
      const u16* pb = w_o + (h * 128 + b * 64 + w4 * 2) * 512 + 8 * lane;
#pragma unroll
      for (int i = 0; i < 16; ++i) {
        Wa[i] = ld16nt(pa + i * 4096);
        Wb[i] = ld16nt(pb + (i >> 1) * 4096 + (i & 1) * 512);
        if (i >= PACE1) asm volatile("" : "+v"(Wb[i - PACE1]));
        if ((i & 3) == 3) TR();
        SCHED();
      }
    }
    float* red2 = red + 2048;
    float* wsh = red + 3072;
    if (w4 == 0) {
      const unsigned fo = WS_AML + (lane * 32 + h) * 16;
      while (wall_clock64() - tstart < TAML) __builtin_amdgcn_s_sleep(8);
      u32x4 g;
      bool ok = false;
#pragma unroll 1
      for (int spin = 0; spin < SPIN_MAX; ++spin) {
        COMPILER_FENCE();
        if (!ok) g = ld16c(rs, fo);
        ok = (g[1] == tg.hi) & (g[3] == tg.hi);
        if (__all(ok)) break;
        __builtin_amdgcn_s_sleep(2);
      }
      lds_add(cnt + 7, 1u, lane);
      const float mu = __uint_as_float(g[0]), lu = __uint_as_float(g[2]);
      const float M = x16_max(row16_max(mu));
      const float wl = ex2((mu - M) * C2);
      wsh[lane] = wl / x16_sum(row16_sum(wl * lu));
      lds_arrive(cnt + 7, lane);
    } else lds_wait(cnt + 7, 1u);
    COMPILER_FENCE();
    STAMP(3);
    u32x4 pp[16];
#pragma unroll
    for (int i = 0; i < 16; ++i) pp[i] = ld16c(rs, WS_APO + (h * 64 + w4 * 16 + i) * 1024 + lane * 16);
    SCHED();
    lds_wait(cnt + 7, 2u);
    float a8[8];
#pragma unroll
    for (int k = 0; k < 8; ++k) a8[k] = 0.f;
    const float wv = wsh[w4 * 16 + c16];
#pragma unroll
    for (int i = 0; i < 16; ++i) {
      const float wt = rdlane(wv, i);
#pragma unroll
      for (int k = 0; k < 4; ++k) { a8[2 * k] = fmaf(wt, bflo(pp[i][k]), a8[2 * k]); a8[2 * k + 1] = fmaf(wt, bfhi(pp[i][k]), a8[2 * k + 1]); }
      asm volatile("" : "+v"(a8[0]), "+v"(a8[1]), "+v"(a8[2]), "+v"(a8[3]), "+v"(a8[4]), "+v"(a8[5]), "+v"(a8[6]), "+v"(a8[7]));
      SCHED();
    }
    *reinterpret_cast<f32x4*>(red + w4 * 512 + 8 * lane) = f32x4{a8[0], a8[1], a8[2], a8[3]};
    *reinterpret_cast<f32x4*>(red + w4 * 512 + 8 * lane + 4) = f32x4{a8[4], a8[5], a8[6], a8[7]};
    sbar4(cnt + 3, lane);
    STAMP(4);
#pragma unroll
    for (int i = 0; i < 4; ++i) {
      const float* src = red + (i >> 1) * 1024 + w4 * 128 + 16 * (lane >> 3) + 8 * (i & 1);
      const f32x4 t0 = *reinterpret_cast<const f32x4*>(src) + *reinterpret_cast<const f32x4*>(src + 512), t1 = *reinterpret_cast<const f32x4*>(src + 4) + *reinterpret_cast<const f32x4*>(src + 516);
      xv[i] = u32x4{pk_bf(t0[0], t0[1]), pk_bf(t0[2], t0[3]), pk_bf(t1[0], t1[1]), pk_bf(t1[2], t1[3])};
    }
    gemv(red2 + w4 * 128);
    sbar4(cnt + 4, lane);
    STAMP(12);
    unsigned op;
    {
      f32x2 t = {0.f, 0.f};
#pragma unroll
      for (int w = 0; w < 4; ++w) t += *reinterpret_cast<const f32x2*>(red2 + w * 128 + (lane >> 5) * 64 + 8 * (lane & 7) + 2 * w4);
      op = pk_bf(t[0], t[1]);
    }
    float* red3 = red + 4096;
    float a[2][8];
#pragma unroll
    for (int bb = 0; bb < 2; ++bb)
#pragma unroll
      for (int k = 0; k < 8; ++k) a[bb][k] = 0.f;
#pragma unroll
    for (int ip = 0; ip < 8; ++ip) {
      const unsigned v0 = __builtin_amdgcn_readlane(op, ip), v1 = __builtin_amdgcn_readlane(op, 32 + ip);
#pragma unroll
      for (int k = 0; k < 4; ++k) {
        const unsigned lo = plo(Wb[2 * ip][k], Wb[2 * ip + 1][k]), hi = phi(Wb[2 * ip][k], Wb[2 * ip + 1][k]);
        a[0][2 * k] = dot2(lo, v0, a[0][2 * k]); a[0][2 * k + 1] = dot2(hi, v0, a[0][2 * k + 1]);
        a[1][2 * k] = dot2(lo, v1, a[1][2 * k]); a[1][2 * k + 1] = dot2(hi, v1, a[1][2 * k + 1]);
      }
    }
#pragma unroll
    for (int bb = 0; bb < 2; ++bb) {
      *reinterpret_cast<f32x4*>(red3 + (w4 * 2 + bb) * 512 + 8 * lane) = f32x4{a[bb][0], a[bb][1], a[bb][2], a[bb][3]};
      *reinterpret_cast<f32x4*>(red3 + (w4 * 2 + bb) * 512 + 8 * lane + 4) = f32x4{a[bb][4], a[bb][5], a[bb][6], a[bb][7]};
    }
    sbar4(cnt + 5, lane);
    {
      f32x4 t = {0.f, 0.f, 0.f, 0.f};
#pragma unroll
      for (int w = 0; w < 4; ++w) t += *reinterpret_cast<const f32x4*>(red3 + w * 1024 + 4 * tid);
      st16c(rs, WS_GA + idx * 4096 + tid * 16, u32x4{pk_bf(t[0], t[1]), tg.hi, pk_bf(t[2], t[3]), tg.hi});
    }
    STAMP(5);
    STAMP_FLUSH(0); TR_FLUSH();
    return;
  }
  {
    const unsigned doff = WS_GA + (w4 * 16 + (lane >> 2)) * 4096 + idx * 64 + (lane & 3) * 16;
    lds_wait(cnt + 0, 4u);
    u32x4 d;
    bool ok = false;
#pragma unroll 1
    for (int spin = 0; spin < SPIN_MAX; ++spin) {
      COMPILER_FENCE();
      if (!ok) d = ld16c(rs, doff);
      ok = (d[1] == tg.hi) & (d[3] == tg.hi);
      if (__all(ok)) break;
      __builtin_amdgcn_s_sleep(1);
    }
    const float a[4] = {bflo(d[0]), bfhi(d[0]), bflo(d[2]), bfhi(d[2])};
    fin(a, out + (idx >> 5) * 1024 + 16 * (idx & 31));
  }
  STAMP(6);
  STAMP_FLUSH(0); TR_FLUSH();
}

extern "C" void kernel_launch(void* const* d_in, const int* in_sizes, int n_in, void* d_out, int out_size, void* d_ws, size_t ws_size, hipStream_t stream) {
  const u16* xA = (const u16*)d_in[0];
  const u16* ckv = (const u16*)d_in[1];
  const u16* w_q = (const u16*)d_in[2];
  const u16* w_uk = (const u16*)d_in[3];
  const u16* w_uv = (const u16*)d_in[4];
  const u16* w_o = (const u16*)d_in[5];
  const u16* xB = (const u16*)d_in[6];
  const u16* kc = (const u16*)d_in[7];
  const u16* vc = (const u16*)d_in[8];
  const u16* wq = (const u16*)d_in[9];
  const u16* wo = (const u16*)d_in[10];
  kmain<<<256, 512, 0, stream>>>(kc, vc, (char*)d_ws, ckv, w_q, wq, w_uk, xA, xB, w_uv, w_o, wo, (u16*)d_out);
}
```

```cpp
#define PMODE 0
#include <hip/hip_runtime.h>
#include <hip/hip_bf16.h>
#include <stdint.h>

#define DI __device__ __forceinline__
typedef __attribute__((ext_vector_type(4))) unsigned u32x4;
typedef __attribute__((ext_vector_type(2))) unsigned u32x2;
typedef __attribute__((ext_vector_type(4))) float f32x4;
typedef __attribute__((ext_vector_type(2))) float f32x2;
typedef __attribute__((ext_vector_type(16))) float f32x16;
typedef __attribute__((ext_vector_type(8))) __bf16 bf16x8_t;
typedef __attribute__((ext_vector_type(2))) __bf16 bf16x2_t;
typedef __attribute__((ext_vector_type(4))) short s16x4;
typedef __attribute__((ext_vector_type(8))) short s16x8;
typedef unsigned short u16;
typedef unsigned long long u64;

#define EP 0
#define P4 0
#define QTAB 54,53,74,75, 55,53,76,72, 54,53,74,75, 55,49,76,76, 54,53,74,75, 55,49,76,76, 55,53,73,75, 55,53,72,76
constexpr int AHL = 1;
#define TS0 90
#define TS1 90
#define TS2 75
#define TS3 65
struct Tab { u64 lo[4], hi[128]; bool ok; };
constexpr int QX_[32] = {QTAB}, TSX[4] = {TS0, TS1, TS2, TS3};
constexpr int etime(int c, int n, int ns) { return n < 3 ? 4 * (10 + n * (TSX[c] - 10) / 3) + c : 4 * (TSX[c] + (n - 3) * (240 - TSX[c]) / (ns - 4)) + c; }
constexpr Tab make_tab() {
  Tab t = {};
  t.ok = true;
  const int nb[4] = {14, 14, 18, 18};
  u64 used = 0;
  for (int c = 0; c < 4; ++c)
    for (int n = 0; n < 10; ++n) {
      u64 rank = 0;
      for (int d = 0; d < 4; ++d)
        for (int m = 0; m < nb[d]; ++m) rank += etime(d, m, nb[d]) < etime(c, n, nb[c]);
      t.lo[c] |= rank << (6 * n); used |= 1ull << rank;
    }
  for (int r = 0; r < 8; ++r)
    for (int w = 0; w < 4; ++w) {
      int ns[4] = {}, first = 0, sum = 0;
      for (int c = 0; c < 4; ++c) { const int q = QX_[4 * r + c]; ns[c] = q / 4 + (((w - first) & 3) < q % 4); first += q % 4; sum += ns[c]; t.ok &= ns[c] >= 12 + P4 && ns[c] <= 20; }
      t.ok &= sum == 64;
      u64 seen = used;
      for (int c = 0; c < 4 && t.ok; ++c) {
        u64 v = (u64)(ns[c] - 10) << 60;
        for (int n = 10; n < ns[c]; ++n) {
          int rank = 0, k = 0;
          for (int d = 0; d < 4; ++d)
            for (int m = 10; m < ns[d]; ++m) rank += etime(d, m, ns[d]) < etime(c, n, ns[c]);
          for (;; ++k) if (!(used >> k & 1) && rank-- == 0) break;
          v |= (u64)k << (6 * (n - 10)); seen |= 1ull << k;
        }
        t.hi[(r * 4 + c) * 4 + w] = v;
      }
      t.ok &= seen == ~0ull;
    }
  return t;
}
constexpr Tab TB = make_tab();
static_assert(TB.ok, "every chunk once");
struct Hi { u64 v[128]; };
constexpr Hi make_hi() { Hi h = {}; for (int i = 0; i < 128; ++i) h.v[i] = TB.hi[i]; return h; }
__device__ constexpr Hi TH = make_hi();
#define PACE0 4
#define PACE1 1
#define ROT2 6
#define TQX 530
#define TAML 1800
#define QT 1
constexpr int SK = 4096;
constexpr float C2 = 0.08838834764831845f * 1.4426950408889634f;
constexpr u64 GOLD = 0x9E3779B97F4A7C15ull;

constexpr unsigned WS_EP = 0;
constexpr unsigned WS_QX = 4096;
constexpr unsigned WS_QA = WS_QX + 2 * 2 * 32 * 64 * 8;
constexpr unsigned WS_AML = WS_QA + 2 * 32 * 256 * 8;
constexpr unsigned WS_APO = WS_AML + 64 * 32 * 16;
constexpr unsigned WS_GB = WS_APO + 32 * 64 * 512 * 2;
constexpr unsigned WS_GA = WS_GB + 2 * 128 * 128 * 16;
constexpr unsigned WS_END = WS_GA + 64 * 2 * 128 * 16;
#define STAMP(i) do {} while (0)
#define STAMP_DECL do {} while (0)
#define STAMP_FLUSH(grp) do {} while (0)

#define TR_DECL do {} while (0)
#define TR() do {} while (0)
#define TR_FLUSH() do {} while (0)
DI float bflo(unsigned u) { return __uint_as_float(u << 16); }
DI float bfhi(unsigned u) { return __uint_as_float(u & 0xffff0000u); }
DI unsigned pk_bf(float a, float b) { const f32x2 v = {a, b}; return __builtin_bit_cast(unsigned, __builtin_convertvector(v, bf16x2_t)); }
DI float dot2(unsigned k, unsigned q, float acc) {
  return __builtin_amdgcn_fdot2_f32_bf16(__builtin_bit_cast(bf16x2_t, k), __builtin_bit_cast(bf16x2_t, q), acc, false);
}
DI unsigned plo(unsigned w0, unsigned w1) { return __builtin_amdgcn_perm(w1, w0, 0x05040100u); }
DI unsigned phi(unsigned w0, unsigned w1) { return __builtin_amdgcn_perm(w1, w0, 0x07060302u); }
template <int CTRL> DI float dpp(float x) {
  return __builtin_bit_cast(float, __builtin_amdgcn_mov_dpp(__builtin_bit_cast(int, x), CTRL, 0xf, 0xf, true));
}
DI float row16_sum(float x) { x += dpp<0xB1>(x); x += dpp<0x4E>(x); x += dpp<0x141>(x); x += dpp<0x128>(x); return x; }
DI float row16_max(float x) { x = fmaxf(x, dpp<0xB1>(x)); x = fmaxf(x, dpp<0x4E>(x)); x = fmaxf(x, dpp<0x141>(x)); x = fmaxf(x, dpp<0x128>(x)); return x; }
DI float x16_sum(float x) { auto s = __builtin_amdgcn_permlane16_swap(__float_as_uint(x), __float_as_uint(x), false, false); return __uint_as_float(s[0]) + __uint_as_float(s[1]); }
DI float x16_max(float x) { auto s = __builtin_amdgcn_permlane16_swap(__float_as_uint(x), __float_as_uint(x), false, false); return fmaxf(__uint_as_float(s[0]), __uint_as_float(s[1])); }
DI float xhalf_max(float x) { auto t = __builtin_amdgcn_permlane32_swap(__float_as_uint(x), __float_as_uint(x), false, false); return fmaxf(__uint_as_float(t[0]), __uint_as_float(t[1])); }
DI float xhalf_sum(float x) { auto t = __builtin_amdgcn_permlane32_swap(__float_as_uint(x), __float_as_uint(x), false, false); return __uint_as_float(t[0]) + __uint_as_float(t[1]); }
DI float xrow_max(float x) { return xhalf_max(x16_max(x)); }
DI float xrow_sum(float x) { return xhalf_sum(x16_sum(x)); }
DI u32x4 ld16(const void* p) { return *reinterpret_cast<const u32x4*>(p); }
DI u32x4 ld16nt(const void* p) { return __builtin_nontemporal_load(reinterpret_cast<const u32x4*>(p)); }
DI float ex2(float x) { return __builtin_amdgcn_exp2f(x); }
#define SCHED() __builtin_amdgcn_sched_barrier(0)

typedef __attribute__((address_space(1))) u64 gu64;
typedef __amdgpu_buffer_rsrc_t rsrc_t;
DI u32x4 ld16c(rsrc_t rs, unsigned off) { return __builtin_amdgcn_raw_buffer_load_b128(rs, (int)off, 0, 16); }
DI u32x2 ld8c(rsrc_t rs, unsigned off) { return __builtin_amdgcn_raw_buffer_load_b64(rs, (int)off, 0, 16); }
DI void st16c(rsrc_t rs, unsigned off, u32x4 v) { __builtin_amdgcn_raw_buffer_store_b128(v, rs, (int)off, 0, 16); }
DI void st8c(rsrc_t rs, unsigned off, u32x2 v) { __builtin_amdgcn_raw_buffer_store_b64(v, rs, (int)off, 0, 16); }
constexpr int SPIN_MAX = 1 << 20;
constexpr unsigned OOB = 0x7ffffff0u;
DI void rawbar() { __builtin_amdgcn_sched_barrier(0); __builtin_amdgcn_s_barrier(); __builtin_amdgcn_sched_barrier(0); }
struct Tag { unsigned hi, lo; };
#define COMPILER_FENCE() asm volatile("" ::: "memory")

template <int N> struct IC { static constexpr int v = N; };
template <int I, int N, class F> DI void sfor(F&& f) { if constexpr (I < N) { f(IC<I>{}); sfor<I + 1, N>(f); } }
typedef __attribute__((address_space(3))) unsigned lu32;
DI void lds_wait(unsigned* cnt, unsigned target) {
#pragma unroll 1
  for (int spin = 0; spin < SPIN_MAX; ++spin) {
    if (__builtin_amdgcn_readfirstlane(__hip_atomic_load((lu32*)cnt, __ATOMIC_RELAXED, __HIP_MEMORY_SCOPE_WORKGROUP)) >= target) break;
    __builtin_amdgcn_s_sleep(1);
  }
  COMPILER_FENCE();
}
DI void lds_hold(unsigned* flag) {
#pragma unroll 1
  for (int spin = 0; spin < SPIN_MAX; ++spin) {
    if (__builtin_amdgcn_readfirstlane(__hip_atomic_load((lu32*)flag, __ATOMIC_RELAXED, __HIP_MEMORY_SCOPE_WORKGROUP)) == 0u) break;
    __builtin_amdgcn_s_sleep(2);
  }
}
DI unsigned lds_arrive_nw(unsigned addr) {
  unsigned old, one = 1u;
  asm volatile("s_mov_b64 exec, 1\n\tds_add_rtn_u32 %0, %1, %2\n\ts_mov_b64 exec, -1" : "=v"(old) : "v"(addr), "v"(one) : "memory");
  return old;
}
DI unsigned lds_done(unsigned old) { asm volatile("s_waitcnt lgkmcnt(0)" : "+v"(old) :: "memory"); return __builtin_amdgcn_readfirstlane(old); }
DI unsigned lds_arrive(unsigned* cnt, int) { return lds_done(lds_arrive_nw((unsigned)(size_t)(lu32*)cnt)); }
DI void lds_add(unsigned* cnt, unsigned v, int) {
  asm volatile("s_mov_b64 exec, 1\n\tds_add_u32 %0, %1\n\ts_mov_b64 exec, -1" :: "v"((unsigned)(size_t)(lu32*)cnt), "v"(v) : "memory");
}
DI void sbar4(unsigned* cnt, int lane) { if (lds_arrive(cnt, lane) < 3u) lds_wait(cnt, 4u); }
DI float rs32(float a, float b) { auto t = __builtin_amdgcn_permlane32_swap(__float_as_uint(a), __float_as_uint(b), false, false); return __uint_as_float(t[0]) + __uint_as_float(t[1]); }
DI float rs16(float a, float b) { auto t = __builtin_amdgcn_permlane16_swap(__float_as_uint(a), __float_as_uint(b), false, false); return __uint_as_float(t[0]) + __uint_as_float(t[1]); }
template <int N> DI unsigned rowbcast(unsigned x) { return (unsigned)__builtin_amdgcn_mov_dpp((int)x, 0x150 + N, 0xf, 0xf, true); }
DI void pin2(u32x4& a, u32x4& b) { asm volatile("" : "+v"(a), "+v"(b)); }
DI float rdlane(float x, int l) { return __builtin_bit_cast(float, __builtin_amdgcn_readlane(__builtin_bit_cast(int, x), l)); }

DI unsigned toff(unsigned row, unsigned ch) { return 256u * row + 16u * (ch ^ (((row & 3) << 2) | ((row >> 2) & 3))); }
constexpr int SMEM = 160 * 1024;
constexpr int L_SCR = 131072;
constexpr int L_MRG = 65536;
constexpr int L_CNT = L_SCR + 32 * 1008;

struct KV { u32x4 k[4], v[4]; };

__global__ __launch_bounds__(512, 2) __attribute__((aligned(4096))) void kmain(const u16* __restrict__ kc, const u16* __restrict__ vc, char* __restrict__ ws, const u16* __restrict__ ckv, const u16* __restrict__ w_q, const u16* __restrict__ wq, const u16* __restrict__ w_uk,
                                                const u16* __restrict__ xA, const u16* __restrict__ xB,
                                                const u16* __restrict__ w_uv, const u16* __restrict__ w_o,
                                                const u16* __restrict__ wo,
                                                u16* __restrict__ out) {
  __shared__ __attribute__((aligned(16))) char smem[SMEM];
  const int tid = threadIdx.x, blk = blockIdx.x, lane = tid & 63;
  const int wave = __builtin_amdgcn_readfirstlane(tid >> 6), w4 = wave & 3;
  const int cls = (blk >> 3) & 3;
  int idx = 8 * (blk & 7) + (blk >> 5);
  if (wave < 4) idx = (idx & ~7) | ((idx + (cls == 3 ? 2 : (cls == 2 ? 4 : 0))) & 7);
  const int h = idx >> 1, b = idx & 1;
  const int idx2 = (idx & ~7) | ((idx + ROT2) & 7);
  const rsrc_t rs = __builtin_amdgcn_make_buffer_rsrc(ws, 0, (int)WS_END, 0x00020000);
  const int r4 = lane >> 4, c16 = lane & 15;
  unsigned* cnt = reinterpret_cast<unsigned*>(smem + L_CNT);
  STAMP_DECL;
  TR_DECL;
  STAMP(0);
  asm volatile("" :: "s"(xA), "s"(ckv), "s"(w_q), "s"(w_uk), "s"(w_uv), "s"(w_o), "s"(xB), "s"(kc), "s"(vc), "s"(wq), "s"(wo), "s"(ws), "s"(out));
  Tag tg;
  if (wave >= 4) {
    if (tid - 256 < 16) cnt[tid - 256] = 0u;
    asm volatile("s_waitcnt lgkmcnt(0)" ::: "memory");
    rawbar();
    const u64 thi = TH.v[((idx & 7) * 4 + cls) * 4 + w4];
    const size_t kvo = ((size_t)(b * 32 + h) * SK + w4 * 16) * 256;
    const char* kb = reinterpret_cast<const char*>(kc) + kvo;
    const char* vb = reinterpret_cast<const char*>(vc) + kvo;
    const unsigned lo = lane * 16u;
    u64 tlo = cls == 0 ? TB.lo[0] : (cls == 1 ? TB.lo[1] : (cls == 2 ? TB.lo[2] : TB.lo[3]));
    auto nexts = [&](unsigned) { const unsigned o = (((unsigned)tlo + (unsigned)idx) & 63u) << 14; tlo >>= 6; return o; };
    unsigned so = nexts(0u);
    KV A, B;
    u32x4 W[8];
    u32x4 T[4][8];
    auto ldp = [&](KV& S, auto P, unsigned o) {
      constexpr int p = P.v;
      constexpr int o0 = (p & 1) * 2048, o1 = o0 + 1024;
      if constexpr (p < 2) { const char* q = kb + o; S.k[2 * p] = ld16nt(q + lo + o0); S.k[2 * p + 1] = ld16nt(q + lo + o1); }
      else { const char* q = vb + o; S.v[2 * p - 4] = ld16nt(q + lo + o0); S.v[2 * p - 3] = ld16nt(q + lo + o1); }
    };
    auto landed = [&](KV& S, auto P) {
      constexpr int p = P.v;
      if constexpr (p < 2) asm volatile("" : "+v"(S.k[2 * p]), "+v"(S.k[2 * p + 1])); else asm volatile("" : "+v"(S.v[2 * p - 4]), "+v"(S.v[2 * p - 3]));
    };
    KV X, Y;
    const unsigned qo = WS_QX + (((2 + b) * 32 + h) * 64 + 4 * c16) * 8;
    u32x4 e0[2];
    u32x2 tw;
    {
      const unsigned so1 = nexts(so), so2 = nexts(so1), so3 = P4 ? nexts(so2) : so2;
      sfor<0, (P4 ? 16 : 12)>([&](auto I) {
        constexpr int i = I.v;
        if constexpr (i >= 4) { if constexpr (i < 8) landed(X, IC<i & 3>{}); else if constexpr (i < 12) landed(B, IC<i & 3>{}); else landed(A, IC<i & 3>{}); }
        if constexpr (i == 7 || i == 11 || i == 15) TR();
        if constexpr (i == 8) tw = ld8c(rs, lane == 0 ? WS_GB + (b * 128 + h * 4 + cls) * 2048 + 512 * w4 : OOB);
        if constexpr (EP && i == EP) { e0[0] = ld16c(rs, qo); e0[1] = ld16c(rs, qo + 16); }
        if constexpr (i < 4) ldp(X, IC<i & 3>{}, so); else if constexpr (i < 8) ldp(B, IC<i & 3>{}, so1); else if constexpr (i < 12) ldp(A, IC<i & 3>{}, so2); else ldp(Y, IC<i & 3>{}, so3);
        SCHED();
      });
      so = so3;
    }
    lds_wait(cnt + 15, 1u);
    tg.hi = __builtin_amdgcn_readfirstlane(cnt[13]); tg.lo = __builtin_amdgcn_readfirstlane(cnt[14]);
    asm volatile("" :: "v"(tw));
    const char* wp = reinterpret_cast<const char*>(wo) + ((size_t)h * 128 + r4 * 8) * 1024 + w4 * 256 + c16 * 16;
    u32x4 qpk;
    {
      auto qok = [&](const u32x4& g0, const u32x4& g1) {
        qpk = u32x4{g0[0], g0[2], g1[0], g1[2]};
        return __all((g0[1] == tg.hi) & (g0[3] == tg.hi) & (g1[1] == tg.hi) & (g1[3] == tg.hi));
      };
      if (!EP || !qok(e0[0], e0[1])) {
#pragma unroll 1
        for (int spin = 0; spin < SPIN_MAX; ++spin) {
          COMPILER_FENCE();
          const u32x4 g0 = ld16c(rs, qo), g1 = ld16c(rs, qo + 16);
          if (qok(g0, g1)) break;
        }
      }
    }
    STAMP(7);
    const bool holds = cls == 0;
    float m = -INFINITY, l = 0.f, acc[8];
#pragma unroll
    for (int k = 0; k < 8; ++k) acc[k] = 0.f;
    auto step = [&](KV& S, KV& N, auto REFILL, auto WR) {
      constexpr bool refill = REFILL.v;
      constexpr int wr = WR.v;
      unsigned sn = 0u;
      if constexpr (refill) sn = nexts(so);
      constexpr int ah = wr < 0 ? AHL : 0;
      auto point = [&](auto P) {
        if constexpr (refill) {
          if constexpr (P.v == 0 && ah > 0) { if (holds) lds_hold(cnt + 8); sfor<0, ah>([&](auto Q) { ldp(N, Q, sn); }); SCHED(); }
          landed(S, P);
          if constexpr (P.v + ah < 4) { if constexpr ((P.v & 1) == 0) { if (holds) lds_hold(cnt + 8); } ldp(N, IC<(P.v + ah) & 3>{}, sn); }
          if constexpr (wr >= 0 && wr < 8) W[(4 * wr + P.v) & 7] = ld16(wp + (((4 * wr + P.v) >> 3) * 32 + ((4 * wr + P.v) & 7)) * 1024);
          SCHED();
        }
      };
      point(IC<0>{});
      point(IC<1>{});
      float x[4];
#pragma unroll
      for (int t = 0; t < 4; ++t) {
        float d = 0.f;
#pragma unroll
        for (int k = 0; k < 4; ++k) d = dot2(S.k[t][k], qpk[k], d);
        x[t] = row16_sum(d);
      }
      const float mn = fmaxf(fmaxf(m, fmaxf(x[0], x[1])), fmaxf(x[2], x[3]));
      const float alpha = ex2((m - mn) * C2);
      m = mn;
      const float mc = mn * C2;
      unsigned p01 = pk_bf(ex2(fmaf(x[0], C2, -mc)), ex2(fmaf(x[1], C2, -mc))), p23 = pk_bf(ex2(fmaf(x[2], C2, -mc)), ex2(fmaf(x[3], C2, -mc)));
      l = dot2(p23, 0x3f803f80u, dot2(p01, 0x3f803f80u, l * alpha));
#pragma unroll
      for (int k = 0; k < 8; ++k) acc[k] *= alpha;
      asm volatile("" : "+v"(p01), "+v"(p23), "+v"(l));
      asm volatile("" : "+v"(acc[0]), "+v"(acc[1]), "+v"(acc[2]), "+v"(acc[3]), "+v"(acc[4]), "+v"(acc[5]), "+v"(acc[6]), "+v"(acc[7]));
      SCHED();
      point(IC<2>{});
#pragma unroll
      for (int k = 0; k < 4; ++k) { acc[2 * k] = dot2(plo(S.v[0][k], S.v[1][k]), p01, acc[2 * k]); acc[2 * k + 1] = dot2(phi(S.v[0][k], S.v[1][k]), p01, acc[2 * k + 1]); }
      asm volatile("" : "+v"(acc[0]), "+v"(acc[1]), "+v"(acc[2]), "+v"(acc[3]), "+v"(acc[4]), "+v"(acc[5]), "+v"(acc[6]), "+v"(acc[7]));
      SCHED();
      point(IC<3>{});
#pragma unroll
      for (int k = 0; k < 4; ++k) { acc[2 * k] = dot2(plo(S.v[2][k], S.v[3][k]), p23, acc[2 * k]); acc[2 * k + 1] = dot2(phi(S.v[2][k], S.v[3][k]), p23, acc[2 * k + 1]); }
      asm volatile("" : "+v"(acc[0]), "+v"(acc[1]), "+v"(acc[2]), "+v"(acc[3]), "+v"(acc[4]), "+v"(acc[5]), "+v"(acc[6]), "+v"(acc[7]));
      if constexpr (wr >= 1) {
        constexpr int t = (wr - 1) >> 1, j0 = ((wr - 1) & 1) * 2;
#pragma unroll
        for (int jj = 0; jj < 2; ++jj)
#pragma unroll
          for (int e = 0; e < 4; ++e) {
            const unsigned a_ = W[(4 * wr - 4 + 2 * jj) & 7][e], b_ = W[(4 * wr - 3 + 2 * jj) & 7][e];
            T[t][2 * e][j0 + jj] = plo(a_, b_); T[t][2 * e + 1][j0 + jj] = phi(a_, b_);
          }
#pragma unroll
        for (int e = 0; e < 8; e += 2) pin2(T[t][e], T[t][e + 1]);
      }
      if constexpr (refill) so = sn;
      SCHED();
      if constexpr (wr != -2) TR();
    };
    step(B, B, IC<0>{}, IC<-2>{});
    step(A, B, IC<1>{}, IC<0>{});
    step(X, X, IC<0>{}, IC<-2>{});
    sfor<0, 4>([&](auto I) {
      if constexpr (I.v == 3) tlo = thi;
      step(B, A, IC<1>{}, IC<2 * I.v + 1>{});
      step(A, B, IC<1>{}, IC<2 * I.v + 2>{});
    });
    unsigned yoff = (r4 & 1) ? OOB : WS_GB + (b * 128 + h * 4 + cls) * 2048 + 512 * w4 + 32 * c16 + 16 * (lane >> 5), zsh = 16 * (c16 & 1) + 8 * (lane >> 5);
    unsigned rwo = L_MRG + (w4 * 4 + r4) * 272 + c16 * 16, rro = L_MRG + c16 * 272 + r4 * 16, mwo = L_MRG + 16 * 272 + ((c16 & 1) * 16 + w4 * 4 + r4) * 4, mro = L_MRG + 16 * 272 + c16 * 4, m4o = L_MRG + 16 * 272 + r4 * 16;
    asm volatile("" : "+v"(zsh), "+v"(yoff), "+v"(rwo), "+v"(rro), "+v"(mwo), "+v"(mro), "+v"(m4o));
    const int ns = (int)(thi >> 60) + 10 - P4, nit = (ns - 12) >> 1;
#pragma unroll 1
    for (int i = 0; i < nit; ++i) {
      step(B, A, IC<1>{}, IC<-1>{});
      step(A, B, IC<1>{}, IC<-1>{});
    }
    if (cls == 0) lds_wait(cnt + 1, 4u);
    if (ns & 1) { step(B, A, IC<1>{}, IC<-1>{}); step(A, A, IC<0>{}, IC<-1>{}); }
    else step(B, B, IC<0>{}, IC<-1>{});
    STAMP(1);
    *reinterpret_cast<u32x4*>(smem + rwo) = u32x4{pk_bf(acc[0], acc[1]), pk_bf(acc[2], acc[3]), pk_bf(acc[4], acc[5]), pk_bf(acc[6], acc[7])};
    *reinterpret_cast<float*>(smem + mwo) = (c16 & 1) ? l : m;
    {
      u32x4 af[4];
      float mr, lr;
      f32x4 m4;
      unsigned got = lds_arrive_nw(L_CNT), need = 3u;
#pragma unroll 1
      for (int spin = 0; spin < SPIN_MAX; ++spin) {
#pragma unroll
        for (int t = 0; t < 4; ++t) af[t] = *reinterpret_cast<const u32x4*>(smem + rro + t * 64);
        mr = *reinterpret_cast<const float*>(smem + mro); lr = *reinterpret_cast<const float*>(smem + mro + 64);
        m4 = *reinterpret_cast<const f32x4*>(smem + m4o);
        SCHED();
        if (lds_done(got) >= need) break;
        need = 4u;
        __builtin_amdgcn_s_sleep(1);
        asm volatile("ds_read_b32 %0, %1" : "=v"(got) : "v"((unsigned)L_CNT) : "memory");
      }
      SCHED();
      f32x4 d[8];
#pragma unroll
      for (int e = 0; e < 8; ++e) {
        d[e] = f32x4{0.f, 0.f, 0.f, 0.f};
#pragma unroll
        for (int t = 0; t < 4; ++t) d[e] = __builtin_amdgcn_mfma_f32_16x16x32_bf16(__builtin_bit_cast(bf16x8_t, af[t]), __builtin_bit_cast(bf16x8_t, T[t][e]), d[e], 0, 0, 0);
      }
      const float M = row16_max(mr);
      const float L = row16_sum(ex2((mr - M) * C2) * lr);
      const float z = fmaf(M, C2, __builtin_amdgcn_logf(L));
      float wt[4], a[8];
#pragma unroll
      for (int i = 0; i < 4; ++i) wt[i] = ex2(fmaf(m4[i], C2, -z));
#pragma unroll
      for (int e = 0; e < 8; ++e) a[e] = fmaf(wt[3], d[e][3], fmaf(wt[2], d[e][2], fmaf(wt[1], d[e][1], wt[0] * d[e][0])));
      float v4[4];
#pragma unroll
      for (int k = 0; k < 4; ++k) v4[k] = x16_sum(rs32(a[k], a[k + 4]));
      st16c(rs, yoff, u32x4{pk_bf(v4[0], v4[1]), tg.hi, pk_bf(v4[2], v4[3]), (tg.lo & 0xffffff00u) | ((__float_as_uint(z) >> zsh) & 0xffu)});
    }
    STAMP(2);
    STAMP_FLUSH(1); TR_FLUSH();
    return;
  }
  const u64 tstart = wall_clock64();
  const u32x2 epw = __builtin_amdgcn_raw_buffer_load_b64(rs, (int)WS_EP, 0, 16);
  auto fin = [&](const float (&a)[4], u16* dst) {
    float y = rs16(rs32(a[0], a[2]), rs32(a[1], a[3]));
    y += dpp<0x124>(y); y += dpp<0x128>(y);
    float* fr = reinterpret_cast<float*>(smem + 32768);
    if ((lane & 12) == 0) fr[w4 * 16 + 4 * (lane & 3) + r4] = y;
    COMPILER_FENCE();
    const unsigned got = lds_arrive_nw(L_CNT + 24);
    f32x2 t[4];
#pragma unroll
    for (int w = 0; w < 4; ++w) t[w] = *reinterpret_cast<const f32x2*>(fr + w * 16 + 2 * (lane & 7));
    SCHED();
    if (lds_done(got) == 3u) {
      const f32x2 r = (t[0] + t[1]) + (t[2] + t[3]);
      if (lane < 8) *reinterpret_cast<unsigned*>(dst + 2 * lane) = pk_bf(r[0], r[1]);
    }
  };
  u32x4 Wa[16], Wb[16], xv[4];
  {
    const u16* pa; unsigned sa = 4096;
    if (cls & 1) pa = (cls == 1 ? w_q : wq) + (w4 * 128 + 16 * (lane >> 3)) * 4096 + h * 128 + b * 64 + 8 * (lane & 7);
    else if (cls == 2) pa = w_uk + ((b * 256 + w4 * 64 + 16 * r4) * 32 + h) * 128 + 8 * c16;
    else { pa = ckv + (idx * 128 + w4) * 512 + 8 * lane; sa = 2048; }
    const u16* xp = (cls == 1 ? xA : xB) + w4 * 128 + 16 * (lane >> 3);
#pragma unroll
    for (int i = 0; i < 4; ++i) xv[i] = ld16(xp + (i >> 1) * 512 + 8 * (i & 1));
    SCHED();
#pragma unroll
    for (int i = 0; i < 16; ++i) Wa[i] = ld16nt(pa + i * sa);
    SCHED();
    rawbar();
    const u64 t64 = (((u64)(unsigned)__builtin_amdgcn_readfirstlane(epw[1]) << 32) | (unsigned)__builtin_amdgcn_readfirstlane(epw[0])) + GOLD;
    tg.hi = (unsigned)(t64 >> 32); tg.lo = (unsigned)t64;
    asm volatile("" : "+s"(tg.hi), "+s"(tg.lo) :: "memory");
    if (w4 == 0) { cnt[13] = tg.hi; cnt[14] = tg.lo; COMPILER_FENCE(); lds_add(cnt + 15, 1u, lane); }
    STAMP(13);
  }
  float* red = reinterpret_cast<float*>(smem);
  auto gemv = [&](float* dst) {
    float acc[2][8];
#pragma unroll
    for (int bb = 0; bb < 2; ++bb)
#pragma unroll
      for (int k = 0; k < 8; ++k) acc[bb][k] = 0.f;
#pragma unroll
    for (int ip = 0; ip < 8; ++ip) {
      const unsigned xp0 = xv[ip >> 2][ip & 3], xp1 = xv[2 + (ip >> 2)][ip & 3];
#pragma unroll
      for (int k = 0; k < 4; ++k) {
        const unsigned lo = plo(Wa[2 * ip][k], Wa[2 * ip + 1][k]), hi = phi(Wa[2 * ip][k], Wa[2 * ip + 1][k]);
        acc[0][2 * k] = dot2(lo, xp0, acc[0][2 * k]); acc[0][2 * k + 1] = dot2(hi, xp0, acc[0][2 * k + 1]);
        acc[1][2 * k] = dot2(lo, xp1, acc[1][2 * k]); acc[1][2 * k + 1] = dot2(hi, xp1, acc[1][2 * k + 1]);
      }
    }
    float v8[8], v4[4];
#pragma unroll
    for (int k = 0; k < 8; ++k) v8[k] = rs32(acc[0][k], acc[1][k]);
#pragma unroll
    for (int k = 0; k < 4; ++k) { const float t = rs16(v8[k], v8[k + 4]); v4[k] = t + dpp<0x128>(t); }
    *reinterpret_cast<f32x4*>(dst + (lane >> 5) * 64 + 8 * (lane & 7) + 4 * (r4 & 1)) = f32x4{v4[0], v4[1], v4[2], v4[3]};
    COMPILER_FENCE();
  };
  if (cls & 1) {
    gemv(red + w4 * 128);
    const unsigned got = lds_arrive_nw(L_CNT + 8);
    f32x4 t4[4];
#pragma unroll
    for (int w = 0; w < 4; ++w) t4[w] = *reinterpret_cast<const f32x4*>(red + w * 128 + 4 * (lane & 31));
    SCHED();
    if (lds_done(got) == 3u) {
      const f32x4 t = (t4[0] + t4[1]) + (t4[2] + t4[3]);
      st16c(rs, lane < 32 ? WS_QX + ((((cls >> 1) * 2 + r4) * 32 + h) * 64 + b * 32 + 2 * c16) * 8 : OOB, u32x4{pk_bf(t[0], t[1]), tg.hi, pk_bf(t[2], t[3]), tg.hi});
      SCHED();
      lds_add(cnt + 2, 1u, lane);
    } else lds_wait(cnt + 2, 5u);
    STAMP(8);
  } else if (cls == 2) {
    while (wall_clock64() - tstart < TQX) __builtin_amdgcn_s_sleep(4);
    u32x4 q0, q1;
    const unsigned qo = WS_QX + (h * 64 + 4 * c16) * 8;
#pragma unroll 1
    for (int spin = 0; spin < SPIN_MAX; ++spin) {
      COMPILER_FENCE();
      const u32x4 g0 = ld16c(rs, qo), g1 = ld16c(rs, qo + 16), g2 = ld16c(rs, qo + 16384), g3 = ld16c(rs, qo + 16400);
      q0 = u32x4{g0[0], g0[2], g1[0], g1[2]}; q1 = u32x4{g2[0], g2[2], g3[0], g3[2]};
      if (__all((g0[1] == tg.hi) & (g0[3] == tg.hi) & (g1[1] == tg.hi) & (g1[3] == tg.hi) & (g2[1] == tg.hi) & (g2[3] == tg.hi) & (g3[1] == tg.hi) & (g3[3] == tg.hi))) break;
      __builtin_amdgcn_s_sleep(1);
    }
    STAMP(9);
    float v[4];
    {
      float u[16], t8[8];
#pragma unroll
      for (int i = 0; i < 16; ++i) {
        float a = 0.f, c = 0.f;
#pragma unroll
        for (int k = 0; k < 4; ++k) { a = dot2(Wa[i][k], q0[k], a); c = dot2(Wa[i][k], q1[k], c); }
        u[i] = (c16 & 4 ? c : a) + dpp<0x141>(c16 & 4 ? a : c);
        asm volatile("" : "+v"(u[i]));
      }
#pragma unroll
      for (int i = 0; i < 8; ++i) { t8[i] = (c16 & 2 ? u[i + 8] : u[i]) + dpp<0x4E>(c16 & 2 ? u[i] : u[i + 8]); asm volatile("" : "+v"(t8[i])); }
#pragma unroll
      for (int i = 0; i < 4; ++i) {
        float t = (c16 & 1 ? t8[i + 4] : t8[i]) + dpp<0xB1>(c16 & 1 ? t8[i] : t8[i + 4]);
        asm volatile("" : "+v"(t));
        v[i] = t + dpp<0x128>(t);
      }
    }
    st16c(rs, c16 < 8 ? WS_QA + ((c16 >> 2) * 32 + h) * 2048 + (b * 256 + w4 * 64 + 16 * r4 + 4 * (c16 & 3)) * 4 : OOB, u32x4{pk_bf(v[0], v[1]), tg.hi, pk_bf(v[2], v[3]), tg.hi});
    STAMP(10);
  }
  if (cls >= 2) {
    const unsigned tw = __builtin_nontemporal_load(reinterpret_cast<const unsigned*>(out + (idx >> 5) * 1024 + (cls & 1) * 512 + 16 * (idx & 31)));
    asm volatile("" :: "v"(tw));
  }
  asm volatile("" :: "v"(xv[0]), "v"(xv[1]), "v"(xv[2]), "v"(xv[3]));
  SCHED();
  if (cls == 3) {
    const int fb = idx >> 5;
    const unsigned doff = WS_GB + (fb * 128 + 32 * w4 + (lane >> 2)) * 2048 + (idx & 31) * 64 + (lane & 3) * 16;
    lds_wait(cnt + 0, 4u);
    STAMP(3);
    u32x4 d[2];
    bool ok = false;
#pragma unroll 1
    for (int spin = 0; spin < SPIN_MAX; ++spin) {
      COMPILER_FENCE();
      if (!ok) { d[0] = ld16c(rs, doff); d[1] = ld16c(rs, doff + 32768); }
      ok = (((d[0][1] ^ tg.hi) | (d[1][1] ^ tg.hi)) | (((d[0][3] ^ tg.lo) | (d[1][3] ^ tg.lo)) >> 8)) == 0u;
      if (__all(ok)) break;
    }
    STAMP(14);
    float a[4] = {0.f, 0.f, 0.f, 0.f};
#pragma unroll
    for (int j = 0; j < 2; ++j) {
      unsigned zb = (d[j][3] & 0xffu) << (8 * (lane & 3));
      zb |= __float_as_uint(dpp<0xB1>(__uint_as_float(zb))); zb |= __float_as_uint(dpp<0x4E>(__uint_as_float(zb)));
      const float z = __uint_as_float(zb);
      float Z = fmaxf(z, dpp<0x124>(z)); Z = fmaxf(Z, dpp<0x128>(Z));
      const float wt = ex2(z - Z);
      float L = wt + dpp<0x124>(wt); L += dpp<0x128>(L);
      const float w = wt * __builtin_amdgcn_rcpf(L);
      a[0] = fmaf(w, bflo(d[j][0]), a[0]); a[1] = fmaf(w, bfhi(d[j][0]), a[1]); a[2] = fmaf(w, bflo(d[j][2]), a[2]); a[3] = fmaf(w, bfhi(d[j][2]), a[3]);
    }
    fin(a, out + fb * 1024 + 512 + 16 * (idx & 31));
    STAMP(6);
    STAMP_FLUSH(0); TR_FLUSH();
    return;
  }
  if (cls == 0) {
    {
      const u16* pb = ckv + (idx * 128 + 64 + w4) * 512 + 8 * lane;
#pragma unroll
      for (int i = 0; i < 16; ++i) {
        Wb[i] = ld16nt(pb + i * 2048);
        if (i >= PACE0 && (i & 1)) asm volatile("" : "+v"(Wb[i - PACE0]));
        if (i == 9) TR();
        SCHED();
      }
    }
    char* scr = smem + L_SCR;
    const int ab = idx >> 5, r = lane & 31, hh = lane >> 5;
#pragma unroll
    for (int i = 0; i < 16; ++i) *reinterpret_cast<u32x4*>(smem + ((i >> 3) * 4 + r4) * 8192 + toff(4 * (i & 7) + w4, c16)) = Wa[i];
#pragma unroll
    for (int i = 0; i < 16; ++i) *reinterpret_cast<u32x4*>(smem + ((2 + (i >> 3)) * 4 + r4) * 8192 + toff(4 * (i & 7) + w4, c16)) = Wb[i];
    STAMP(8);
    TR();
    u32x4 qlast = {0u, 0u, 0u, 0u};
    {
      u32x4 g[18];
      const unsigned o8 = WS_QA + (ab * 32 + w4 * 8) * 2048 + lane * 16, ol = WS_QA + (ab * 32 + r) * 2048 + 1984 + 32 * hh;
      unsigned pend = 0x3ffffu;
#pragma unroll 1
      for (int spin = 0; spin < SPIN_MAX && pend; ++spin) {
        COMPILER_FENCE();
#pragma unroll
        for (int i = 0; i < 18; ++i) g[i] = ld16c(rs, (pend >> i & 1) ? (i < 16 ? o8 + i * 1024 : ol + 16 * (i - 16)) : OOB);
#pragma unroll
        for (int i = 0; i < 18; ++i) {
          if ((pend >> i & 1) && __all((g[i][1] == tg.hi) & (g[i][3] == tg.hi))) {
            pend &= ~(1u << i);
            if (i >= 16) { qlast[2 * (i - 16)] = g[i][0]; qlast[2 * (i - 16) + 1] = g[i][2]; }
            else if (!(i & 1) || lane < 60) *reinterpret_cast<u32x2*>(scr + (w4 * 8 + (i >> 1)) * 1008 + (i & 1) * 512 + 8 * lane) = u32x2{g[i][0], g[i][2]};
          }
        }
        if (pend) __builtin_amdgcn_s_sleep(8);
      }
    }
    sbar4(cnt + 3, lane);
    STAMP(10);
    f32x16 sacc;
#pragma unroll
    for (int i = 0; i < 16; ++i) sacc[i] = 0.f;
#pragma unroll
    for (int jj = 0; jj < 32; ++jj) {
      const u32x4 af = *reinterpret_cast<const u32x4*>(smem + (w4 * 4 + (jj >> 3)) * 8192 + toff(r, 2 * (jj & 7) + hh));
      const u32x4 bf = jj < 31 ? *reinterpret_cast<const u32x4*>(scr + r * 1008 + 16 * (2 * (jj < 31 ? jj : 0) + hh)) : qlast;
      sacc = __builtin_amdgcn_mfma_f32_32x32x16_bf16(__builtin_bit_cast(bf16x8_t, af), __builtin_bit_cast(bf16x8_t, bf), sacc, 0, 0, 0);
    }
    float m = sacc[0];
#pragma unroll
    for (int i = 1; i < 16; ++i) m = fmaxf(m, sacc[i]);
    m = xhalf_max(m);
    if (hh == 0) *reinterpret_cast<float*>(scr + r * 1008 + 992 + 4 * w4) = m;
    sbar4(cnt + 4, lane);
    STAMP(11);
    {
      const f32x4 m4 = *reinterpret_cast<const f32x4*>(scr + r * 1008 + 992);
      m = fmaxf(fmaxf(m4[0], m4[1]), fmaxf(m4[2], m4[3]));
    }
    const float mc = m * C2;
    float p[16], ls = 0.f;
#pragma unroll
    for (int i = 0; i < 16; ++i) { p[i] = ex2(fmaf(sacc[i], C2, -mc)); ls += p[i]; }
    ls = xhalf_sum(ls);
    sbar4(cnt + 5, lane);
    u32x4* pf = reinterpret_cast<u32x4*>(scr);
    float* wl = reinterpret_cast<float*>(scr + 8192);
    if (hh == 0) wl[r * 4 + w4] = ls;
    pf[(2 * w4) * 64 + lane] = u32x4{pk_bf(p[0], p[1]), pk_bf(p[2], p[3]), pk_bf(p[4], p[5]), pk_bf(p[6], p[7])};
    pf[(2 * w4 + 1) * 64 + lane] = u32x4{pk_bf(p[8], p[9]), pk_bf(p[10], p[11]), pk_bf(p[12], p[13]), pk_bf(p[14], p[15])};
    sbar4(cnt + 6, lane);
    u32x4 pb8[8];
#pragma unroll
    for (int ks = 0; ks < 8; ++ks) pb8[ks] = pf[ks * 64 + lane];
    const f32x4 l4 = *reinterpret_cast<const f32x4*>(wl + r * 4);
    const int qq = (lane & 15) >> 2, pp = lane & 3, blk2 = (lane >> 4) & 1;
    char* ot = scr + 12288 + w4 * 4096;
#pragma unroll
    for (int c2 = 0; c2 < 2; ++c2) {
      f32x16 oacc[2];
#pragma unroll
      for (int ct = 0; ct < 2; ++ct)
#pragma unroll
        for (int i = 0; i < 16; ++i) oacc[ct][i] = 0.f;
#pragma unroll
      for (int ks = 0; ks < 8; ++ks)
#pragma unroll
        for (int ct = 0; ct < 2; ++ct) {
          typedef __attribute__((address_space(3))) s16x4* lp;
          const char* tile = smem + ((ks >> 1) * 4 + w4) * 8192;
          const int cc = 2 * c2 + ct;
          const s16x4 lo = __builtin_amdgcn_ds_read_tr16_b64_v4i16((lp)(tile + toff(16 * (ks & 1) + 4 * hh + qq, 4 * cc + 2 * blk2 + (pp >> 1)) + 8 * (pp & 1)));
          const s16x4 hi = __builtin_amdgcn_ds_read_tr16_b64_v4i16((lp)(tile + toff(16 * (ks & 1) + 8 + 4 * hh + qq, 4 * cc + 2 * blk2 + (pp >> 1)) + 8 * (pp & 1)));
          const s16x8 afr = __builtin_shufflevector(lo, hi, 0, 1, 2, 3, 4, 5, 6, 7);
          oacc[ct] = __builtin_amdgcn_mfma_f32_32x32x16_bf16(__builtin_bit_cast(bf16x8_t, afr), __builtin_bit_cast(bf16x8_t, pb8[ks]), oacc[ct], 0, 0, 0);
        }
#pragma unroll
      for (int ct = 0; ct < 2; ++ct)
#pragma unroll
        for (int g = 0; g < 4; ++g)
          *reinterpret_cast<u32x2*>(ot + r * 128 + 16 * ((4 * ct + g) ^ (r & 7)) + 8 * hh) = u32x2{pk_bf(oacc[ct][4 * g], oacc[ct][4 * g + 1]), pk_bf(oacc[ct][4 * g + 2], oacc[ct][4 * g + 3])};
      if (c2 == 0) lds_add(cnt + 8, 1u, lane);
      __builtin_amdgcn_wave_barrier();
#pragma unroll
      for (int i = 0; i < 4; ++i) {
        const int hd = 8 * i + (lane >> 3), pc = lane & 7;
        const u32x4 vv = *reinterpret_cast<const u32x4*>(ot + hd * 128 + 16 * (pc ^ (hd & 7)));
        st16c(rs, WS_APO + ((hd * 64 + idx) * 512 + w4 * 128 + 64 * c2 + 8 * pc) * 2, vv);
      }
      __builtin_amdgcn_wave_barrier();
    }
    lds_arrive(cnt + 1, lane);
    STAMP(1);
    asm volatile("s_waitcnt vmcnt(0)" ::: "memory");
    if (lds_arrive(cnt + 9, lane) == 3u && hh == 0)
      st16c(rs, WS_AML + (idx * 32 + r) * 16, u32x4{__float_as_uint(m), tg.hi, __float_as_uint((l4[0] + l4[1]) + (l4[2] + l4[3])), tg.hi});
    lds_add(cnt + 8, ~0u, lane);
    STAMP(2);
    if (idx == 0 && w4 == 0) {
      lds_wait(cnt + 0, 4u);
#pragma unroll 1
      for (int spin = 0; spin < SPIN_MAX; ++spin) {
        COMPILER_FENCE();
        const u32x2 t0 = ld8c(rs, WS_AML + lane * 512), t1 = ld8c(rs, WS_QX + (64 * 64 + lane * 32) * 8);
        if (__all((t0[1] == tg.hi) & (t1[1] == tg.hi))) break;
        __builtin_amdgcn_s_sleep(8);
      }
      if (lane == 0) __hip_atomic_store((gu64*)(ws + WS_EP), ((u64)tg.hi << 32) | tg.lo, __ATOMIC_RELAXED, __HIP_MEMORY_SCOPE_AGENT);
    }
    STAMP_FLUSH(0); TR_FLUSH();
    return;
  }
  if (cls == 1) {
    const int idx = idx2, h = idx >> 1, b = idx & 1;
    {
      const u16* pa = w_uv + (w4 * 128 + 16 * (lane >> 3)) * 4096 + h * 128 + b * 64 + 8 * (lane & 7);
      const u16* pb = w_o + (h * 128 + b * 64 + w4 * 2) * 512 + 8 * lane;
#pragma unroll
      for (int i = 0; i < 16; ++i) {
        Wa[i] = ld16nt(pa + i * 4096);
        Wb[i] = ld16nt(pb + (i >> 1) * 4096 + (i & 1) * 512);
        if (i >= PACE1) asm volatile("" : "+v"(Wb[i - PACE1]));
        if ((i & 3) == 3) TR();
        SCHED();
      }
    }
    float* red2 = red + 2048;
    float* wsh = red + 3072;
    if (w4 == 0) {
      const unsigned fo = WS_AML + (lane * 32 + h) * 16;
      while (wall_clock64() - tstart < TAML) __builtin_amdgcn_s_sleep(8);
      u32x4 g;
      bool ok = false;
#pragma unroll 1
      for (int spin = 0; spin < SPIN_MAX; ++spin) {
        COMPILER_FENCE();
        if (!ok) g = ld16c(rs, fo);
        ok = (g[1] == tg.hi) & (g[3] == tg.hi);
        if (__all(ok)) break;
        __builtin_amdgcn_s_sleep(2);
      }
      lds_add(cnt + 7, 1u, lane);
      const float mu = __uint_as_float(g[0]), lu = __uint_as_float(g[2]);
      const float M = x16_max(row16_max(mu));
      const float wl = ex2((mu - M) * C2);
      wsh[lane] = wl / x16_sum(row16_sum(wl * lu));
      lds_arrive(cnt + 7, lane);
    } else lds_wait(cnt + 7, 1u);
    COMPILER_FENCE();
    STAMP(3);
    u32x4 pp[16];
#pragma unroll
    for (int i = 0; i < 16; ++i) pp[i] = ld16c(rs, WS_APO + (h * 64 + w4 * 16 + i) * 1024 + lane * 16);
    SCHED();
    lds_wait(cnt + 7, 2u);
    float a8[8];
#pragma unroll
    for (int k = 0; k < 8; ++k) a8[k] = 0.f;
    const float wv = wsh[w4 * 16 + c16];
#pragma unroll
    for (int i = 0; i < 16; ++i) {
      const float wt = rdlane(wv, i);
#pragma unroll
      for (int k = 0; k < 4; ++k) { a8[2 * k] = fmaf(wt, bflo(pp[i][k]), a8[2 * k]); a8[2 * k + 1] = fmaf(wt, bfhi(pp[i][k]), a8[2 * k + 1]); }
      asm volatile("" : "+v"(a8[0]), "+v"(a8[1]), "+v"(a8[2]), "+v"(a8[3]), "+v"(a8[4]), "+v"(a8[5]), "+v"(a8[6]), "+v"(a8[7]));
      SCHED();
    }
    *reinterpret_cast<f32x4*>(red + w4 * 512 + 8 * lane) = f32x4{a8[0], a8[1], a8[2], a8[3]};
    *reinterpret_cast<f32x4*>(red + w4 * 512 + 8 * lane + 4) = f32x4{a8[4], a8[5], a8[6], a8[7]};
    sbar4(cnt + 3, lane);
    STAMP(4);
#pragma unroll
    for (int i = 0; i < 4; ++i) {
      const float* src = red + (i >> 1) * 1024 + w4 * 128 + 16 * (lane >> 3) + 8 * (i & 1);
      const f32x4 t0 = *reinterpret_cast<const f32x4*>(src) + *reinterpret_cast<const f32x4*>(src + 512), t1 = *reinterpret_cast<const f32x4*>(src + 4) + *reinterpret_cast<const f32x4*>(src + 516);
      xv[i] = u32x4{pk_bf(t0[0], t0[1]), pk_bf(t0[2], t0[3]), pk_bf(t1[0], t1[1]), pk_bf(t1[2], t1[3])};
    }
    gemv(red2 + w4 * 128);
    sbar4(cnt + 4, lane);
    STAMP(12);
    unsigned op;
    {
      f32x2 t = {0.f, 0.f};
#pragma unroll
      for (int w = 0; w < 4; ++w) t += *reinterpret_cast<const f32x2*>(red2 + w * 128 + (lane >> 5) * 64 + 8 * (lane & 7) + 2 * w4);
      op = pk_bf(t[0], t[1]);
    }
    float* red3 = red + 4096;
    float a[2][8];
#pragma unroll
    for (int bb = 0; bb < 2; ++bb)
#pragma unroll
      for (int k = 0; k < 8; ++k) a[bb][k] = 0.f;
#pragma unroll
    for (int ip = 0; ip < 8; ++ip) {
      const unsigned v0 = __builtin_amdgcn_readlane(op, ip), v1 = __builtin_amdgcn_readlane(op, 32 + ip);
#pragma unroll
      for (int k = 0; k < 4; ++k) {
        const unsigned lo = plo(Wb[2 * ip][k], Wb[2 * ip + 1][k]), hi = phi(Wb[2 * ip][k], Wb[2 * ip + 1][k]);
        a[0][2 * k] = dot2(lo, v0, a[0][2 * k]); a[0][2 * k + 1] = dot2(hi, v0, a[0][2 * k + 1]);
        a[1][2 * k] = dot2(lo, v1, a[1][2 * k]); a[1][2 * k + 1] = dot2(hi, v1, a[1][2 * k + 1]);
      }
    }
#pragma unroll
    for (int bb = 0; bb < 2; ++bb) {
      *reinterpret_cast<f32x4*>(red3 + (w4 * 2 + bb) * 512 + 8 * lane) = f32x4{a[bb][0], a[bb][1], a[bb][2], a[bb][3]};
      *reinterpret_cast<f32x4*>(red3 + (w4 * 2 + bb) * 512 + 8 * lane + 4) = f32x4{a[bb][4], a[bb][5], a[bb][6], a[bb][7]};
    }
    sbar4(cnt + 5, lane);
    {
      f32x4 t = {0.f, 0.f, 0.f, 0.f};
#pragma unroll
      for (int w = 0; w < 4; ++w) t += *reinterpret_cast<const f32x4*>(red3 + w * 1024 + 4 * tid);
      st16c(rs, WS_GA + idx * 4096 + tid * 16, u32x4{pk_bf(t[0], t[1]), tg.hi, pk_bf(t[2], t[3]), tg.hi});
    }
    STAMP(5);
    STAMP_FLUSH(0); TR_FLUSH();
    return;
  }
  {
    const unsigned doff = WS_GA + (w4 * 16 + (lane >> 2)) * 4096 + idx * 64 + (lane & 3) * 16;
    lds_wait(cnt + 0, 4u);
    u32x4 d;
    bool ok = false;
#pragma unroll 1
    for (int spin = 0; spin < SPIN_MAX; ++spin) {
      COMPILER_FENCE();
      if (!ok) d = ld16c(rs, doff);
      ok = (d[1] == tg.hi) & (d[3] == tg.hi);
      if (__all(ok)) break;
      __builtin_amdgcn_s_sleep(1);
    }
    const float a[4] = {bflo(d[0]), bfhi(d[0]), bflo(d[2]), bfhi(d[2])};
    fin(a, out + (idx >> 5) * 1024 + 16 * (idx & 31));
  }
  STAMP(6);
  STAMP_FLUSH(0); TR_FLUSH();
}

struct KV8 { u32x4 r[8]; };
__global__ __launch_bounds__(512, 2) void sweep(const char* kp, const char* vp, char* ws, int kind) {
  __shared__ char pad[100000];
  const int tid = threadIdx.x, lane = tid & 63, wave = __builtin_amdgcn_readfirstlane(tid >> 6), blk = blockIdx.x;
  if (kind == 77) pad[tid] = 1;
  const u64 t0 = wall_clock64();
  unsigned acc = 0;
  if (wave >= 4) return;
  const unsigned cls = (blk >> 3) & 3, idx = 8 * (blk & 7) + (blk >> 5), bh = (idx & 1) * 32 + (idx >> 1);
  const char* K = kp + ((size_t)bh << 20) + lane * 16; const char* V = vp + ((size_t)bh << 20) + lane * 16;
  KV8 A, B;
  auto addr = [&](int n, const char*& p1, const char*& p2) { n = n < 16 ? n : 15;
    if (kind == 0) { unsigned o = (((n * 4 + cls + idx) & 63) << 14) + wave * 4096; p1 = K + o; p2 = V + o; }
    else { int m = n & 7; const char* base = n >= 8 ? V : K; unsigned o = (((m * 4 + cls + (idx >> 1)) & 31) << 15) + wave * 8192; p1 = base + o; p2 = p1 + 4096; } };
  auto ldp = [&](KV8& S, int p, const char* p1, const char* p2) { const char* q = (p < 2 ? p1 : p2) + (p & 1) * 2048; S.r[2 * p] = ld16nt(q); S.r[2 * p + 1] = ld16nt(q + 1024); };
  auto step = [&](KV8& S, KV8& N, int n, bool rf) { const char* p1; const char* p2; addr(n, p1, p2);
#pragma unroll
    for (int p = 0; p < 4; p++) { asm volatile("" : "+v"(S.r[2 * p]), "+v"(S.r[2 * p + 1])); if (rf) ldp(N, p, p1, p2); SCHED(); acc += S.r[2 * p][0] ^ S.r[2 * p + 1][3]; SCHED(); } };
  { const char* p1; const char* p2; addr(0, p1, p2);
#pragma unroll
    for (int p = 0; p < 4; p++) { ldp(B, p, p1, p2); SCHED(); } }
#pragma unroll 1
  for (int n = 0; n < 14; n += 2) { step(B, A, n + 1, true); step(A, B, n + 2, true); }
  step(B, A, 15, true); step(A, B, 16, false);
  asm volatile("" :: "v"(acc));
  const u64 t1 = wall_clock64();
  if (kind == 77) acc += pad[tid ^ 1];
  if (lane == 0) reinterpret_cast<unsigned*>(ws + (32u << 20))[kind * 1024 + blk * 4 + wave] = (unsigned)(t1 - t0) + (acc == 0x12345u);
}
__global__ __launch_bounds__(64) void k0(char* ws) { const u64 t = wall_clock64(); if (threadIdx.x == 0) *reinterpret_cast<u64*>(ws + 2048) = t; }
__global__ __launch_bounds__(64) void k2(char* ws) {
  const unsigned* d = reinterpret_cast<const unsigned*>(ws + (32u << 20));
  int s0 = 0, s1 = 0;
  for (int i = 0; i < 1024; ++i) { s0 += d[i]; s1 += d[1024 + i]; }
  long long ex = PMODE == 0 ? 20ll * (s0 - s1) / 1024 + 10000 : 10ll * s0 / 1024;
  if (ex < 0) ex = 0; if (ex > 40000) ex = 40000;
  const u64 tend = *reinterpret_cast<volatile u64*>(ws + 2048) + 20000 + ex;
  for (int sp_ = 0; sp_ < SPIN_MAX && wall_clock64() < tend; ++sp_) __builtin_amdgcn_s_sleep(2);
}

extern "C" void kernel_launch(void* const* d_in, const int* in_sizes, int n_in, void* d_out, int out_size, void* d_ws, size_t ws_size, hipStream_t stream) {
  const u16* xA = (const u16*)d_in[0];
  const u16* ckv = (const u16*)d_in[1];
  const u16* w_q = (const u16*)d_in[2];
  const u16* w_uk = (const u16*)d_in[3];
  const u16* w_uv = (const u16*)d_in[4];
  const u16* w_o = (const u16*)d_in[5];
  const u16* xB = (const u16*)d_in[6];
  const u16* kc = (const u16*)d_in[7];
  const u16* vc = (const u16*)d_in[8];
  const u16* wq = (const u16*)d_in[9];
  const u16* wo = (const u16*)d_in[10];
  k0<<<1, 64, 0, stream>>>((char*)d_ws);
  sweep<<<256, 512, 0, stream>>>((const char*)kc, (const char*)vc, (char*)d_ws, 1);
  sweep<<<256, 512, 0, stream>>>((const char*)kc, (const char*)vc, (char*)d_ws, 0);
  sweep<<<256, 512, 0, stream>>>((const char*)kc, (const char*)vc, (char*)d_ws, 1);
  kmain<<<256, 512, 0, stream>>>(kc, vc, (char*)d_ws, ckv, w_q, wq, w_uk, xA, xB, w_uv, w_o, wo, (u16*)d_out);
  k2<<<1, 64, 0, stream>>>((char*)d_ws);
}
```

```cpp
#include <hip/hip_runtime.h>
#include <hip/hip_bf16.h>
#include <stdint.h>

#define DI __device__ __forceinline__
typedef __attribute__((ext_vector_type(4))) unsigned u32x4;
typedef __attribute__((ext_vector_type(2))) unsigned u32x2;
typedef __attribute__((ext_vector_type(4))) float f32x4;
typedef __attribute__((ext_vector_type(2))) float f32x2;
typedef __attribute__((ext_vector_type(16))) float f32x16;
typedef __attribute__((ext_vector_type(8))) __bf16 bf16x8_t;
typedef __attribute__((ext_vector_type(2))) __bf16 bf16x2_t;
typedef __attribute__((ext_vector_type(4))) short s16x4;
typedef __attribute__((ext_vector_type(8))) short s16x8;
typedef unsigned short u16;
typedef unsigned long long u64;

#define QTAB 53,53,75,75, 54,54,77,71, 53,53,75,75, 54,48,77,77, 53,53,75,75, 54,46,78,78, 53,53,74,76, 53,53,73,77
#define AHK 1
#define AHV 1
#define WROT 1
#define WS 0
#define N8 3
#define NRD(c) ((N8 >> (c) & 1) ? 8 : 10)
constexpr int QX_[32] = {QTAB};
constexpr u64 qpair(int r) { u64 v = 0; for (int i = 0; i < 8; ++i) v |= (u64)QX_[8 * r + i] << (8 * i); return v; }
constexpr bool qok() { bool ok = true; for (int r = 0; r < 8; ++r) { int s = 0; for (int c = 0; c < 4; ++c) { s += QX_[4 * r + c]; ok &= QX_[4 * r + c] >= 10 && QX_[4 * r + c] <= 8 * NRD(c); } ok &= s == 256; } return ok; }
static_assert(qok(), "every key once");
#define PACE0 4
#define PACE1 3
#define ROT2 6
#define TQX 530
#define TAML 1800
#define QT 1
constexpr int SK = 4096;
constexpr float C2 = 0.08838834764831845f * 1.4426950408889634f;
constexpr u64 GOLD = 0x9E3779B97F4A7C15ull;

constexpr unsigned WS_EP = 0;
constexpr unsigned WS_QX = 4096;
constexpr unsigned WS_QA = WS_QX + 2 * 2 * 32 * 64 * 8;
constexpr unsigned WS_AML = WS_QA + 2 * 32 * 256 * 8;
constexpr unsigned WS_APO = WS_AML + 64 * 32 * 16;
constexpr unsigned WS_GB = WS_APO + 32 * 64 * 512 * 2;
constexpr unsigned WS_GA = WS_GB + 2 * 128 * 128 * 16;
constexpr unsigned WS_END = WS_GA + 64 * 2 * 128 * 16;
#define STAMP(i) do {} while (0)
#define STAMP_DECL do {} while (0)
#define STAMP_FLUSH(grp) do {} while (0)

#define TR_DECL do {} while (0)
#define TR() do {} while (0)
#define TR_FLUSH() do {} while (0)
DI float bflo(unsigned u) { return __uint_as_float(u << 16); }
DI float bfhi(unsigned u) { return __uint_as_float(u & 0xffff0000u); }
DI unsigned pk_bf(float a, float b) { const f32x2 v = {a, b}; return __builtin_bit_cast(unsigned, __builtin_convertvector(v, bf16x2_t)); }
DI float dot2(unsigned k, unsigned q, float acc) {
  return __builtin_amdgcn_fdot2_f32_bf16(__builtin_bit_cast(bf16x2_t, k), __builtin_bit_cast(bf16x2_t, q), acc, false);
}
DI unsigned plo(unsigned w0, unsigned w1) { return __builtin_amdgcn_perm(w1, w0, 0x05040100u); }
DI unsigned phi(unsigned w0, unsigned w1) { return __builtin_amdgcn_perm(w1, w0, 0x07060302u); }
template <int CTRL> DI float dpp(float x) {
  return __builtin_bit_cast(float, __builtin_amdgcn_mov_dpp(__builtin_bit_cast(int, x), CTRL, 0xf, 0xf, true));
}
DI float row16_sum(float x) { x += dpp<0xB1>(x); x += dpp<0x4E>(x); x += dpp<0x141>(x); x += dpp<0x128>(x); return x; }
DI float row16_max(float x) { x = fmaxf(x, dpp<0xB1>(x)); x = fmaxf(x, dpp<0x4E>(x)); x = fmaxf(x, dpp<0x141>(x)); x = fmaxf(x, dpp<0x128>(x)); return x; }
DI float x16_sum(float x) { auto s = __builtin_amdgcn_permlane16_swap(__float_as_uint(x), __float_as_uint(x), false, false); return __uint_as_float(s[0]) + __uint_as_float(s[1]); }
DI float x16_max(float x) { auto s = __builtin_amdgcn_permlane16_swap(__float_as_uint(x), __float_as_uint(x), false, false); return fmaxf(__uint_as_float(s[0]), __uint_as_float(s[1])); }
DI float xhalf_max(float x) { auto t = __builtin_amdgcn_permlane32_swap(__float_as_uint(x), __float_as_uint(x), false, false); return fmaxf(__uint_as_float(t[0]), __uint_as_float(t[1])); }
DI float xhalf_sum(float x) { auto t = __builtin_amdgcn_permlane32_swap(__float_as_uint(x), __float_as_uint(x), false, false); return __uint_as_float(t[0]) + __uint_as_float(t[1]); }
DI float xrow_max(float x) { return xhalf_max(x16_max(x)); }
DI float xrow_sum(float x) { return xhalf_sum(x16_sum(x)); }
DI u32x4 ld16(const void* p) { return *reinterpret_cast<const u32x4*>(p); }
DI u32x4 ld16nt(const void* p) { return __builtin_nontemporal_load(reinterpret_cast<const u32x4*>(p)); }
DI float ex2(float x) { return __builtin_amdgcn_exp2f(x); }
#define SCHED() __builtin_amdgcn_sched_barrier(0)

typedef __attribute__((address_space(1))) u64 gu64;
typedef __amdgpu_buffer_rsrc_t rsrc_t;
DI u32x4 ld16c(rsrc_t rs, unsigned off) { return __builtin_amdgcn_raw_buffer_load_b128(rs, (int)off, 0, 16); }
DI u32x2 ld8c(rsrc_t rs, unsigned off) { return __builtin_amdgcn_raw_buffer_load_b64(rs, (int)off, 0, 16); }
DI void st16c(rsrc_t rs, unsigned off, u32x4 v) { __builtin_amdgcn_raw_buffer_store_b128(v, rs, (int)off, 0, 16); }
DI void st8c(rsrc_t rs, unsigned off, u32x2 v) { __builtin_amdgcn_raw_buffer_store_b64(v, rs, (int)off, 0, 16); }
constexpr int SPIN_MAX = 1 << 20;
constexpr unsigned OOB = 0x7ffffff0u;
DI void rawbar() { __builtin_amdgcn_sched_barrier(0); __builtin_amdgcn_s_barrier(); __builtin_amdgcn_sched_barrier(0); }
struct Tag { unsigned hi, lo; };
#define COMPILER_FENCE() asm volatile("" ::: "memory")

template <int N> struct IC { static constexpr int v = N; };
template <int I, int N, class F> DI void sfor(F&& f) { if constexpr (I < N) { f(IC<I>{}); sfor<I + 1, N>(f); } }
typedef __attribute__((address_space(3))) unsigned lu32;
DI void lds_wait(unsigned* cnt, unsigned target) {
#pragma unroll 1
  for (int spin = 0; spin < SPIN_MAX; ++spin) {
    if (__builtin_amdgcn_readfirstlane(__hip_atomic_load((lu32*)cnt, __ATOMIC_RELAXED, __HIP_MEMORY_SCOPE_WORKGROUP)) >= target) break;
    __builtin_amdgcn_s_sleep(1);
  }
  COMPILER_FENCE();
}
DI void lds_hold(unsigned* flag) {
#pragma unroll 1
  for (int spin = 0; spin < SPIN_MAX; ++spin) {
    if (__builtin_amdgcn_readfirstlane(__hip_atomic_load((lu32*)flag, __ATOMIC_RELAXED, __HIP_MEMORY_SCOPE_WORKGROUP)) == 0u) break;
    __builtin_amdgcn_s_sleep(2);
  }
}
DI unsigned lds_arrive_nw(unsigned addr) {
  unsigned old, one = 1u;
  asm volatile("s_mov_b64 exec, 1\n\tds_add_rtn_u32 %0, %1, %2\n\ts_mov_b64 exec, -1" : "=v"(old) : "v"(addr), "v"(one) : "memory");
  return old;
}
DI unsigned lds_done(unsigned old) { asm volatile("s_waitcnt lgkmcnt(0)" : "+v"(old) :: "memory"); return __builtin_amdgcn_readfirstlane(old); }
DI unsigned lds_arrive(unsigned* cnt, int) { return lds_done(lds_arrive_nw((unsigned)(size_t)(lu32*)cnt)); }
DI void lds_add(unsigned* cnt, unsigned v, int) {
  asm volatile("s_mov_b64 exec, 1\n\tds_add_u32 %0, %1\n\ts_mov_b64 exec, -1" :: "v"((unsigned)(size_t)(lu32*)cnt), "v"(v) : "memory");
}
DI void sbar4(unsigned* cnt, int lane) { if (lds_arrive(cnt, lane) < 3u) lds_wait(cnt, 4u); }
DI float rs32(float a, float b) { auto t = __builtin_amdgcn_permlane32_swap(__float_as_uint(a), __float_as_uint(b), false, false); return __uint_as_float(t[0]) + __uint_as_float(t[1]); }
DI float rs16(float a, float b) { auto t = __builtin_amdgcn_permlane16_swap(__float_as_uint(a), __float_as_uint(b), false, false); return __uint_as_float(t[0]) + __uint_as_float(t[1]); }
template <int N> DI unsigned rowbcast(unsigned x) { return (unsigned)__builtin_amdgcn_mov_dpp((int)x, 0x150 + N, 0xf, 0xf, true); }
DI void pin2(u32x4& a, u32x4& b) { asm volatile("" : "+v"(a), "+v"(b)); }
DI float rdlane(float x, int l) { return __builtin_bit_cast(float, __builtin_amdgcn_readlane(__builtin_bit_cast(int, x), l)); }

DI unsigned toff(unsigned row, unsigned ch) { return 256u * row + 16u * (ch ^ (((row & 3) << 2) | ((row >> 2) & 3))); }
constexpr int SMEM = 160 * 1024;
constexpr int L_SCR = 131072;
constexpr int L_MRG = 65536;
constexpr int L_CNT = L_SCR + 32 * 1008;

struct KV { u32x4 r[8]; };

__global__ __launch_bounds__(512, 2) __attribute__((aligned(4096))) void kmain(const u16* __restrict__ kc, const u16* __restrict__ vc, char* __restrict__ ws, const u16* __restrict__ ckv, const u16* __restrict__ w_q, const u16* __restrict__ wq, const u16* __restrict__ w_uk,
                                                const u16* __restrict__ xA, const u16* __restrict__ xB,
                                                const u16* __restrict__ w_uv, const u16* __restrict__ w_o,
                                                const u16* __restrict__ wo,
                                                u16* __restrict__ out) {
  __shared__ __attribute__((aligned(16))) char smem[SMEM];
  const int tid = threadIdx.x, blk = blockIdx.x, lane = tid & 63;
  const int wave = __builtin_amdgcn_readfirstlane(tid >> 6), w4 = wave & 3;
  const int cls = (blk >> 3) & 3;
  int idx = 8 * (blk & 7) + (blk >> 5);
  if (wave < 4) idx = (idx & ~7) | ((idx + (cls == 3 ? 2 : (cls == 2 ? 4 : 0))) & 7);
  const int h = idx >> 1, b = idx & 1;
  const int idx2 = (idx & ~7) | ((idx + ROT2) & 7);
  const rsrc_t rs = __builtin_amdgcn_make_buffer_rsrc(ws, 0, (int)WS_END, 0x00020000);
  const int r4 = lane >> 4, c16 = lane & 15;
  unsigned* cnt = reinterpret_cast<unsigned*>(smem + L_CNT);
  STAMP_DECL;
  TR_DECL;
  STAMP(0);
  asm volatile("" :: "s"(xA), "s"(ckv), "s"(w_q), "s"(w_uk), "s"(w_uv), "s"(w_o), "s"(xB), "s"(kc), "s"(vc), "s"(wq), "s"(wo), "s"(ws), "s"(out));
  Tag tg;
  if (wave >= 4) {
    if (tid - 256 < 16) cnt[tid - 256] = 0u;
    const int nr = NRD(cls);
    unsigned tab;
    {
      const int row = idx & 7;
      const u64 q2 = row < 4 ? (row < 2 ? qpair(0) : qpair(1)) : (row < 6 ? qpair(2) : qpair(3));
      const unsigned q4 = (unsigned)(q2 >> (32 * (row & 1)));
      int j = 2 * nr - 1 - lane;
      j = lane < nr ? lane : (j > 2 ? j : (j == 2 ? 0 : j + 1));
      int n = j + (h * nr >> 5);
      n = n >= nr ? n - nr : n;
      int off = 0, wd = 0;
#pragma unroll
      for (int d = 0; d < 4; ++d) {
        const int q = (q4 >> (8 * d)) & 255, bs = NRD(d) == 8 ? q >> 3 : q * 205 >> 11, rm = q - NRD(d) * bs;
        const int k = ((2 * n + 1) * NRD(d) + (d < cls) - 1 + nr) * (nr == 8 ? 4096 : 3277) >> 16;
        off += k * bs + min(k, rm);
        wd = d == cls ? bs + (n < rm) : wd;
      }
      tab = lane < 2 * nr ? (unsigned)(4 * off + w4 * wd) << 10 | (unsigned)wd : 0u;
    }
    const char* kb = reinterpret_cast<const char*>(kc) + ((size_t)(b * 32 + h) << 20);
    const char* vb = reinterpret_cast<const char*>(vc) + ((size_t)(b * 32 + h) << 20);
    const unsigned lo = lane * 16u, lo2 = lo + 4096u;
    int nx = 0;
    auto nexts = [&]() {
      const unsigned e = __builtin_amdgcn_readlane(tab, nx);
      const rsrc_t r = __builtin_amdgcn_make_buffer_rsrc(const_cast<char*>((nx < nr ? kb : vb) + (e & ~1023u)), 0, (int)((e & 15u) << 10), 0x00020000);
      ++nx;
      return r;
    };
    const rsrc_t s0 = nexts();
    const int rot = WROT ? cls : 0;
    const char* wb = reinterpret_cast<const char*>(wo) + h * 131072;
    unsigned wl = r4 * 8192 + w4 * 256 + c16 * 16;
    asm volatile("s_waitcnt lgkmcnt(0)" : "+v"(wl), "+v"(tab) :: "memory");
    rawbar();
    KV A, B, X;
    u32x4 W[8][4];
    u32x4 T[4][8];
    auto ldp = [&](KV& S, auto P, rsrc_t q) {
      constexpr int p = P.v;
      S.r[2 * p] = __builtin_amdgcn_raw_buffer_load_b128(q, (int)((p < 2 ? lo : lo2) + (p & 1) * 2048), 0, 2);
      S.r[2 * p + 1] = __builtin_amdgcn_raw_buffer_load_b128(q, (int)((p < 2 ? lo : lo2) + (p & 1) * 2048 + 1024), 0, 2);
    };
    auto landed = [&](KV& S, auto P) { asm volatile("" : "+v"(S.r[2 * P.v]), "+v"(S.r[2 * P.v + 1])); };
    auto ldw = [&](auto G, auto J) { W[G.v][J.v] = ld16(wb + ((((G.v >> 1) + rot) & 3) * 32768 + (G.v & 1) * 4096) + wl + J.v * 1024); };
    auto conv = [&](auto G) {
      constexpr int t = G.v >> 1, j0 = (G.v & 1) * 2;
#pragma unroll
      for (int jj = 0; jj < 2; ++jj)
#pragma unroll
        for (int e = 0; e < 4; ++e) {
          const unsigned a_ = W[G.v][2 * jj][e], b_ = W[G.v][2 * jj + 1][e];
          T[t][2 * e][j0 + jj] = plo(a_, b_); T[t][2 * e + 1][j0 + jj] = phi(a_, b_);
        }
#pragma unroll
      for (int e = 0; e < 8; e += 2) pin2(T[t][e], T[t][e + 1]);
    };
    u32x2 tw;
    {
      const rsrc_t s1 = nexts(), s2 = nexts();
      sfor<0, 12>([&](auto I) {
        constexpr int i = I.v;
        if constexpr (i >= 4) { if constexpr (i < 8) landed(X, IC<i & 3>{}); else landed(B, IC<i & 3>{}); }
        if constexpr (i == 7 || i == 11) TR();
        if constexpr (i == 8) tw = ld8c(rs, lane == 0 ? WS_GB + (b * 128 + h * 4 + cls) * 2048 + 512 * w4 : OOB);
        if constexpr (i < 4) { ldp(X, IC<i & 3>{}, s0); ldw(IC<0>{}, IC<i & 3>{}); }
        else if constexpr (i < 8) { ldp(B, IC<i & 3>{}, s1); ldw(IC<1>{}, IC<i & 3>{}); if constexpr (WS) ldw(IC<2>{}, IC<i & 3>{}); }
        else { ldp(A, IC<i & 3>{}, s2); ldw(IC<2 + WS>{}, IC<i & 3>{}); if constexpr (WS) ldw(IC<4>{}, IC<i & 3>{}); }
        SCHED();
        if constexpr (i == 8) { conv(IC<0>{}); SCHED(); }
      });
    }
    lds_wait(cnt + 15, 1u);
    tg.hi = __builtin_amdgcn_readfirstlane(cnt[13]); tg.lo = __builtin_amdgcn_readfirstlane(cnt[14]);
    asm volatile("" :: "v"(tw));
    u32x4 qpk;
    {
      const unsigned qo = WS_QX + (((2 + b) * 32 + h) * 64 + 4 * c16) * 8;
      u32x4 g0 = ld16c(rs, qo), g1 = ld16c(rs, qo + 16);
      SCHED();
      conv(IC<1>{}); if constexpr (WS) conv(IC<2>{});
      SCHED();
#pragma unroll 1
      for (int spin = 0; spin < SPIN_MAX; ++spin) {
        if (__all((g0[1] == tg.hi) & (g0[3] == tg.hi) & (g1[1] == tg.hi) & (g1[3] == tg.hi))) break;
        COMPILER_FENCE();
        g0 = ld16c(rs, qo); g1 = ld16c(rs, qo + 16);
      }
      qpk = u32x4{g0[0], g0[2], g1[0], g1[2]};
    }
    STAMP(7);
    const bool holds = cls == 0;
    float sc[10], acc[8];
#pragma unroll
    for (int k = 0; k < 10; ++k) sc[k] = 0.f;
    auto point = [&](KV& S, KV& N, rsrc_t sn, auto P, auto REFILL, auto AH, auto WG) {
      constexpr int ah = AH.v;
      if constexpr (REFILL.v) {
        if constexpr (P.v == 0 && ah > 0) { if (holds) lds_hold(cnt + 8); sfor<0, ah>([&](auto Q) { ldp(N, Q, sn); }); SCHED(); }
        landed(S, P);
        if constexpr (P.v + ah < 4) { if constexpr ((P.v & 1) == 0) { if (holds) lds_hold(cnt + 8); } ldp(N, IC<(P.v + ah) & 3>{}, sn); }
        if constexpr (WG.v >= 0) ldw(WG, P);
        SCHED();
      }
    };
    auto step = [&](KV& S, KV& N, auto REFILL, auto WG, auto ISV) {
      rsrc_t sn = s0;
      if constexpr (REFILL.v) sn = nexts();
      sfor<0, 4>([&](auto P) {
        point(S, N, sn, P, REFILL, IC<(ISV.v ? AHV : (WG.v < 0 ? AHK : 0))>{}, WG);
        if constexpr (!ISV.v) {
#pragma unroll
          for (int t = 2 * P.v; t < 2 * P.v + 2; ++t) {
            acc[t] = 0.f;
#pragma unroll
            for (int k = 0; k < 4; ++k) acc[t] = dot2(S.r[t][k], qpk[k], acc[t]);
          }
          asm volatile("" : "+v"(acc[2 * P.v]), "+v"(acc[2 * P.v + 1]));
        } else {
          const unsigned pp = rowbcast<4 * P.v>(__float_as_uint(sc[0]));
#pragma unroll
          for (int k = 0; k < 4; ++k) { acc[2 * k] = dot2(plo(S.r[2 * P.v][k], S.r[2 * P.v + 1][k]), pp, acc[2 * k]); acc[2 * k + 1] = dot2(phi(S.r[2 * P.v][k], S.r[2 * P.v + 1][k]), pp, acc[2 * k + 1]); }
          asm volatile("" : "+v"(acc[0]), "+v"(acc[1]), "+v"(acc[2]), "+v"(acc[3]), "+v"(acc[4]), "+v"(acc[5]), "+v"(acc[6]), "+v"(acc[7]));
        }
        SCHED();
      });
      if constexpr (!ISV.v) {
        float u[4], v[2];
#pragma unroll
        for (int i = 0; i < 4; ++i) u[i] = (c16 & 8 ? acc[i + 4] : acc[i]) + dpp<0x128>(c16 & 8 ? acc[i] : acc[i + 4]);
#pragma unroll
        for (int i = 0; i < 2; ++i) v[i] = (c16 & 4 ? u[i + 2] : u[i]) + dpp<0x141>(c16 & 4 ? u[i] : u[i + 2]);
        float x = (c16 & 2 ? v[1] : v[0]) + dpp<0x4E>(c16 & 2 ? v[0] : v[1]);
        x += dpp<0xB1>(x);
#pragma unroll
        for (int k = 9; k > 0; --k) sc[k] = sc[k - 1];
        sc[0] = x;
      } else {
#pragma unroll
        for (int k = 0; k < 9; ++k) sc[k] = sc[k + 1];
      }
      SCHED();
      if constexpr (REFILL.v || ISV.v) TR();
    };
    step(B, B, IC<0>{}, IC<-1>{}, IC<0>{});
    step(A, B, IC<1>{}, IC<3 + 2 * WS>{}, IC<0>{});
    step(X, X, IC<0>{}, IC<-1>{}, IC<0>{});
    conv(IC<2 + WS>{}); if constexpr (WS) conv(IC<4>{});
    sfor<3, 8>([&](auto I) {
      constexpr int g = I.v + 1 + 2 * WS;
      if constexpr (I.v & 1) step(B, A, IC<1>{}, IC<(g < 8 ? g : -1)>{}, IC<0>{}); else step(A, B, IC<1>{}, IC<(g < 8 ? g : -1)>{}, IC<0>{});
      if constexpr (g - 1 < 8) conv(IC<(g - 1 < 8 ? g - 1 : 0)>{});
    });
#pragma unroll 1
    for (int i = 8; i < nr; i += 2) {
      step(A, B, IC<1>{}, IC<-1>{}, IC<0>{});
      step(B, A, IC<1>{}, IC<-1>{}, IC<0>{});
    }
    float m = -INFINITY, l = 0.f;
#pragma unroll
    for (int k = 0; k < 10; ++k) { sc[k] = (unsigned)(c16 >> 1) < (__builtin_amdgcn_readlane(tab, nr + k) & 15u) ? sc[k] : -INFINITY; m = fmaxf(m, sc[k]); }
    m = row16_max(m);
    {
      const float mc = m * C2;
#pragma unroll
      for (int k = 0; k < 10; ++k) {
        const float p = ex2(fmaf(sc[k], C2, -mc));
        const unsigned pk = pk_bf(dpp<0x00>(p), dpp<0xAA>(p));
        l = dot2(pk, 0x3f803f80u, l);
        sc[k] = __uint_as_float(pk);
      }
      l += dpp<0x124>(l); l += dpp<0x128>(l);
    }
#pragma unroll
    for (int k = 0; k < 8; ++k) acc[k] = 0.f;
    SCHED();
    STAMP(8);
    unsigned yoff = (r4 & 1) ? OOB : WS_GB + (b * 128 + h * 4 + cls) * 2048 + 512 * w4 + 32 * c16 + 16 * (lane >> 5), zsh = 16 * (c16 & 1) + 8 * (lane >> 5);
    unsigned rwo = L_MRG + (w4 * 4 + r4) * 272 + ((c16 - 4 * rot) & 15) * 16, rro = L_MRG + c16 * 272 + r4 * 16, mwo = L_MRG + 16 * 272 + ((c16 & 1) * 16 + w4 * 4 + r4) * 4, mro = L_MRG + 16 * 272 + c16 * 4, m4o = L_MRG + 16 * 272 + r4 * 16;
    asm volatile("" : "+v"(zsh), "+v"(yoff), "+v"(rwo), "+v"(rro), "+v"(mwo), "+v"(mro), "+v"(m4o));
#pragma unroll 1
    for (int i = 2; i < nr; i += 2) {
      step(A, B, IC<1>{}, IC<-1>{}, IC<1>{});
      step(B, A, IC<1>{}, IC<-1>{}, IC<1>{});
    }
    step(A, B, IC<1>{}, IC<-1>{}, IC<1>{});
    if (cls == 0) lds_wait(cnt + 1, 4u);
    step(B, B, IC<0>{}, IC<-1>{}, IC<1>{});
    STAMP(1);
    *reinterpret_cast<u32x4*>(smem + rwo) = u32x4{pk_bf(acc[0], acc[1]), pk_bf(acc[2], acc[3]), pk_bf(acc[4], acc[5]), pk_bf(acc[6], acc[7])};
    *reinterpret_cast<float*>(smem + mwo) = (c16 & 1) ? l : m;
    {
      u32x4 af[4];
      float mr, lr;
      f32x4 m4;
      unsigned got = lds_arrive_nw(L_CNT), need = 3u;
#pragma unroll 1
      for (int spin = 0; spin < SPIN_MAX; ++spin) {
#pragma unroll
        for (int t = 0; t < 4; ++t) af[t] = *reinterpret_cast<const u32x4*>(smem + rro + t * 64);
        mr = *reinterpret_cast<const float*>(smem + mro); lr = *reinterpret_cast<const float*>(smem + mro + 64);
        m4 = *reinterpret_cast<const f32x4*>(smem + m4o);
        SCHED();
        if (lds_done(got) >= need) break;
        need = 4u;
        __builtin_amdgcn_s_sleep(1);
        asm volatile("ds_read_b32 %0, %1" : "=v"(got) : "v"((unsigned)L_CNT) : "memory");
      }
      SCHED();
      f32x4 d[8];
#pragma unroll
      for (int e = 0; e < 8; ++e) {
        d[e] = f32x4{0.f, 0.f, 0.f, 0.f};
#pragma unroll
        for (int t = 0; t < 4; ++t) d[e] = __builtin_amdgcn_mfma_f32_16x16x32_bf16(__builtin_bit_cast(bf16x8_t, af[t]), __builtin_bit_cast(bf16x8_t, T[t][e]), d[e], 0, 0, 0);
      }
      const float M = row16_max(mr);
      const float L = row16_sum(ex2((mr - M) * C2) * lr);
      const float z = fmaf(M, C2, __builtin_amdgcn_logf(L));
      float wt[4], a[8];
#pragma unroll
      for (int i = 0; i < 4; ++i) wt[i] = ex2(fmaf(m4[i], C2, -z));
#pragma unroll
      for (int e = 0; e < 8; ++e) a[e] = fmaf(wt[3], d[e][3], fmaf(wt[2], d[e][2], fmaf(wt[1], d[e][1], wt[0] * d[e][0])));
      float v4[4];
#pragma unroll
      for (int k = 0; k < 4; ++k) v4[k] = x16_sum(rs32(a[k], a[k + 4]));
      st16c(rs, yoff, u32x4{pk_bf(v4[0], v4[1]), tg.hi, pk_bf(v4[2], v4[3]), (tg.lo & 0xffffff00u) | ((__float_as_uint(z) >> zsh) & 0xffu)});
    }
    STAMP(2);
    STAMP_FLUSH(1); TR_FLUSH();
    return;
  }
  const u64 tstart = wall_clock64();
  const u32x2 epw = __builtin_amdgcn_raw_buffer_load_b64(rs, (int)WS_EP, 0, 16);
  auto fin = [&](const float (&a)[4], u16* dst) {
    float y = rs16(rs32(a[0], a[2]), rs32(a[1], a[3]));
    y += dpp<0x124>(y); y += dpp<0x128>(y);
    float* fr = reinterpret_cast<float*>(smem + 32768);
    if ((lane & 12) == 0) fr[w4 * 16 + 4 * (lane & 3) + r4] = y;
    COMPILER_FENCE();
    const unsigned got = lds_arrive_nw(L_CNT + 24);
    f32x2 t[4];
#pragma unroll
    for (int w = 0; w < 4; ++w) t[w] = *reinterpret_cast<const f32x2*>(fr + w * 16 + 2 * (lane & 7));
    SCHED();
    if (lds_done(got) == 3u) {
      const f32x2 r = (t[0] + t[1]) + (t[2] + t[3]);
      if (lane < 8) *reinterpret_cast<unsigned*>(dst + 2 * lane) = pk_bf(r[0], r[1]);
    }
  };
  u32x4 Wa[16], Wb[16], xv[4];
  {
    const u16* pa; unsigned sa = 4096;
    if (cls & 1) pa = (cls == 1 ? w_q : wq) + (w4 * 128 + 16 * (lane >> 3)) * 4096 + h * 128 + b * 64 + 8 * (lane & 7);
    else if (cls == 2) pa = w_uk + ((b * 256 + w4 * 64 + 16 * r4) * 32 + h) * 128 + 8 * c16;
    else { pa = ckv + (idx * 128 + w4) * 512 + 8 * lane; sa = 2048; }
    const u16* xp = (cls == 1 ? xA : xB) + w4 * 128 + 16 * (lane >> 3);
#pragma unroll
    for (int i = 0; i < 4; ++i) xv[i] = ld16(xp + (i >> 1) * 512 + 8 * (i & 1));
    SCHED();
#pragma unroll
    for (int i = 0; i < 16; ++i) Wa[i] = ld16nt(pa + i * sa);
    SCHED();
    rawbar();
    const u64 t64 = (((u64)(unsigned)__builtin_amdgcn_readfirstlane(epw[1]) << 32) | (unsigned)__builtin_amdgcn_readfirstlane(epw[0])) + GOLD;
    tg.hi = (unsigned)(t64 >> 32); tg.lo = (unsigned)t64;
    asm volatile("" : "+s"(tg.hi), "+s"(tg.lo) :: "memory");
    if (w4 == 0) { cnt[13] = tg.hi; cnt[14] = tg.lo; COMPILER_FENCE(); lds_add(cnt + 15, 1u, lane); }
    STAMP(13);
  }
  float* red = reinterpret_cast<float*>(smem);
  auto gemv = [&](float* dst) {
    float acc[2][8];
#pragma unroll
    for (int bb = 0; bb < 2; ++bb)
#pragma unroll
      for (int k = 0; k < 8; ++k) acc[bb][k] = 0.f;
#pragma unroll
    for (int ip = 0; ip < 8; ++ip) {
      const unsigned xp0 = xv[ip >> 2][ip & 3], xp1 = xv[2 + (ip >> 2)][ip & 3];
#pragma unroll
      for (int k = 0; k < 4; ++k) {
        const unsigned lo = plo(Wa[2 * ip][k], Wa[2 * ip + 1][k]), hi = phi(Wa[2 * ip][k], Wa[2 * ip + 1][k]);
        acc[0][2 * k] = dot2(lo, xp0, acc[0][2 * k]); acc[0][2 * k + 1] = dot2(hi, xp0, acc[0][2 * k + 1]);
        acc[1][2 * k] = dot2(lo, xp1, acc[1][2 * k]); acc[1][2 * k + 1] = dot2(hi, xp1, acc[1][2 * k + 1]);
      }
    }
    float v8[8], v4[4];
#pragma unroll
    for (int k = 0; k < 8; ++k) v8[k] = rs32(acc[0][k], acc[1][k]);
#pragma unroll
    for (int k = 0; k < 4; ++k) { const float t = rs16(v8[k], v8[k + 4]); v4[k] = t + dpp<0x128>(t); }
    *reinterpret_cast<f32x4*>(dst + (lane >> 5) * 64 + 8 * (lane & 7) + 4 * (r4 & 1)) = f32x4{v4[0], v4[1], v4[2], v4[3]};
    COMPILER_FENCE();
  };
  if (cls & 1) {
    gemv(red + w4 * 128);
    const unsigned got = lds_arrive_nw(L_CNT + 8);
    f32x4 t4[4];
#pragma unroll
    for (int w = 0; w < 4; ++w) t4[w] = *reinterpret_cast<const f32x4*>(red + w * 128 + 4 * (lane & 31));
    SCHED();
    if (lds_done(got) == 3u) {
      const f32x4 t = (t4[0] + t4[1]) + (t4[2] + t4[3]);
      st16c(rs, lane < 32 ? WS_QX + ((((cls >> 1) * 2 + r4) * 32 + h) * 64 + b * 32 + 2 * c16) * 8 : OOB, u32x4{pk_bf(t[0], t[1]), tg.hi, pk_bf(t[2], t[3]), tg.hi});
      SCHED();
      lds_add(cnt + 2, 1u, lane);
    } else lds_wait(cnt + 2, 5u);
    STAMP(8);
  } else if (cls == 2) {
    while (wall_clock64() - tstart < TQX) __builtin_amdgcn_s_sleep(4);
    u32x4 q0, q1;
    const unsigned qo = WS_QX + (h * 64 + 4 * c16) * 8;
#pragma unroll 1
    for (int spin = 0; spin < SPIN_MAX; ++spin) {
      COMPILER_FENCE();
      const u32x4 g0 = ld16c(rs, qo), g1 = ld16c(rs, qo + 16), g2 = ld16c(rs, qo + 16384), g3 = ld16c(rs, qo + 16400);
      q0 = u32x4{g0[0], g0[2], g1[0], g1[2]}; q1 = u32x4{g2[0], g2[2], g3[0], g3[2]};
      if (__all((g0[1] == tg.hi) & (g0[3] == tg.hi) & (g1[1] == tg.hi) & (g1[3] == tg.hi) & (g2[1] == tg.hi) & (g2[3] == tg.hi) & (g3[1] == tg.hi) & (g3[3] == tg.hi))) break;
      __builtin_amdgcn_s_sleep(1);
    }
    STAMP(9);
    float v[4];
    {
      float u[16], t8[8];
#pragma unroll
      for (int i = 0; i < 16; ++i) {
        float a = 0.f, c = 0.f;
#pragma unroll
        for (int k = 0; k < 4; ++k) { a = dot2(Wa[i][k], q0[k], a); c = dot2(Wa[i][k], q1[k], c); }
        u[i] = (c16 & 4 ? c : a) + dpp<0x141>(c16 & 4 ? a : c);
        asm volatile("" : "+v"(u[i]));
      }
#pragma unroll
      for (int i = 0; i < 8; ++i) { t8[i] = (c16 & 2 ? u[i + 8] : u[i]) + dpp<0x4E>(c16 & 2 ? u[i] : u[i + 8]); asm volatile("" : "+v"(t8[i])); }
#pragma unroll
      for (int i = 0; i < 4; ++i) {
        float t = (c16 & 1 ? t8[i + 4] : t8[i]) + dpp<0xB1>(c16 & 1 ? t8[i] : t8[i + 4]);
        asm volatile("" : "+v"(t));
        v[i] = t + dpp<0x128>(t);
      }
    }
    st16c(rs, c16 < 8 ? WS_QA + ((c16 >> 2) * 32 + h) * 2048 + (b * 256 + w4 * 64 + 16 * r4 + 4 * (c16 & 3)) * 4 : OOB, u32x4{pk_bf(v[0], v[1]), tg.hi, pk_bf(v[2], v[3]), tg.hi});
    STAMP(10);
  }
  if (cls >= 2) {
    const unsigned tw = __builtin_nontemporal_load(reinterpret_cast<const unsigned*>(out + (idx >> 5) * 1024 + (cls & 1) * 512 + 16 * (idx & 31)));
    asm volatile("" :: "v"(tw));
  }
  asm volatile("" :: "v"(xv[0]), "v"(xv[1]), "v"(xv[2]), "v"(xv[3]));
  SCHED();
  if (cls == 3) {
    const int fb = idx >> 5;
    const unsigned doff = WS_GB + (fb * 128 + 32 * w4 + (lane >> 2)) * 2048 + (idx & 31) * 64 + (lane & 3) * 16;
    lds_wait(cnt + 0, 4u);
    STAMP(3);
    u32x4 d[2];
    bool ok = false;
#pragma unroll 1
    for (int spin = 0; spin < SPIN_MAX; ++spin) {
      COMPILER_FENCE();
      if (!ok) { d[0] = ld16c(rs, doff); d[1] = ld16c(rs, doff + 32768); }
      ok = (((d[0][1] ^ tg.hi) | (d[1][1] ^ tg.hi)) | (((d[0][3] ^ tg.lo) | (d[1][3] ^ tg.lo)) >> 8)) == 0u;
      if (__all(ok)) break;
    }
    STAMP(14);
    float a[4] = {0.f, 0.f, 0.f, 0.f};
#pragma unroll
    for (int j = 0; j < 2; ++j) {
      unsigned zb = (d[j][3] & 0xffu) << (8 * (lane & 3));
      zb |= __float_as_uint(dpp<0xB1>(__uint_as_float(zb))); zb |= __float_as_uint(dpp<0x4E>(__uint_as_float(zb)));
      const float z = __uint_as_float(zb);
      float Z = fmaxf(z, dpp<0x124>(z)); Z = fmaxf(Z, dpp<0x128>(Z));
      const float wt = ex2(z - Z);
      float L = wt + dpp<0x124>(wt); L += dpp<0x128>(L);
      const float w = wt * __builtin_amdgcn_rcpf(L);
      a[0] = fmaf(w, bflo(d[j][0]), a[0]); a[1] = fmaf(w, bfhi(d[j][0]), a[1]); a[2] = fmaf(w, bflo(d[j][2]), a[2]); a[3] = fmaf(w, bfhi(d[j][2]), a[3]);
    }
    fin(a, out + fb * 1024 + 512 + 16 * (idx & 31));
    STAMP(6);
    STAMP_FLUSH(0); TR_FLUSH();
    return;
  }
  if (cls == 0) {
    {
      const u16* pb = ckv + (idx * 128 + 64 + w4) * 512 + 8 * lane;
#pragma unroll
      for (int i = 0; i < 16; ++i) {
        Wb[i] = ld16nt(pb + i * 2048);
        if (i >= PACE0 && (i & 1)) asm volatile("" : "+v"(Wb[i - PACE0]));
        if (i == 9) TR();
        SCHED();
      }
    }
    char* scr = smem + L_SCR;
    const int ab = idx >> 5, r = lane & 31, hh = lane >> 5;
#pragma unroll
    for (int i = 0; i < 16; ++i) *reinterpret_cast<u32x4*>(smem + ((i >> 3) * 4 + r4) * 8192 + toff(4 * (i & 7) + w4, c16)) = Wa[i];
#pragma unroll
    for (int i = 0; i < 16; ++i) *reinterpret_cast<u32x4*>(smem + ((2 + (i >> 3)) * 4 + r4) * 8192 + toff(4 * (i & 7) + w4, c16)) = Wb[i];
    STAMP(8);
    TR();
    u32x4 qlast = {0u, 0u, 0u, 0u};
    {
      u32x4 g[18];
      const int fl = (idx & 1) * 8;
      const unsigned o8 = WS_QA + (ab * 32 + w4 * 8) * 2048 + lane * 16, ol = WS_QA + (ab * 32 + r) * 2048 + 1984 + 32 * hh;
      unsigned pend = 0x3ffffu;
#pragma unroll 1
      for (int spin = 0; spin < SPIN_MAX && pend; ++spin) {
        COMPILER_FENCE();
#pragma unroll
        for (int i = 0; i < 18; ++i) g[i] = ld16c(rs, (pend >> i & 1) ? (i < 16 ? o8 + (i ^ fl) * 1024 : ol + 16 * (i - 16)) : OOB);
#pragma unroll
        for (int i = 0; i < 18; ++i) {
          if ((pend >> i & 1) && __all((g[i][1] == tg.hi) & (g[i][3] == tg.hi))) {
            pend &= ~(1u << i);
            if (i >= 16) { qlast[2 * (i - 16)] = g[i][0]; qlast[2 * (i - 16) + 1] = g[i][2]; }
            else if (!(i & 1) || lane < 60) *reinterpret_cast<u32x2*>(scr + (w4 * 8 + ((i ^ fl) >> 1)) * 1008 + (i & 1) * 512 + 8 * lane) = u32x2{g[i][0], g[i][2]};
          }
        }
        if (pend) __builtin_amdgcn_s_sleep(8);
      }
    }
    sbar4(cnt + 3, lane);
    STAMP(10);
    f32x16 sacc;
#pragma unroll
    for (int i = 0; i < 16; ++i) sacc[i] = 0.f;
#pragma unroll
    for (int jj = 0; jj < 32; ++jj) {
      const u32x4 af = *reinterpret_cast<const u32x4*>(smem + (w4 * 4 + (jj >> 3)) * 8192 + toff(r, 2 * (jj & 7) + hh));
      const u32x4 bf = jj < 31 ? *reinterpret_cast<const u32x4*>(scr + r * 1008 + 16 * (2 * (jj < 31 ? jj : 0) + hh)) : qlast;
      sacc = __builtin_amdgcn_mfma_f32_32x32x16_bf16(__builtin_bit_cast(bf16x8_t, af), __builtin_bit_cast(bf16x8_t, bf), sacc, 0, 0, 0);
    }
    float m = sacc[0];
#pragma unroll
    for (int i = 1; i < 16; ++i) m = fmaxf(m, sacc[i]);
    m = xhalf_max(m);
    if (hh == 0) *reinterpret_cast<float*>(scr + r * 1008 + 992 + 4 * w4) = m;
    sbar4(cnt + 4, lane);
    STAMP(11);
    {
      const f32x4 m4 = *reinterpret_cast<const f32x4*>(scr + r * 1008 + 992);
      m = fmaxf(fmaxf(m4[0], m4[1]), fmaxf(m4[2], m4[3]));
    }
    const float mc = m * C2;
    float p[16], ls = 0.f;
#pragma unroll
    for (int i = 0; i < 16; ++i) { p[i] = ex2(fmaf(sacc[i], C2, -mc)); ls += p[i]; }
    ls = xhalf_sum(ls);
    sbar4(cnt + 5, lane);
    u32x4* pf = reinterpret_cast<u32x4*>(scr);
    float* wl = reinterpret_cast<float*>(scr + 8192);
    if (hh == 0) wl[r * 4 + w4] = ls;
    pf[(2 * w4) * 64 + lane] = u32x4{pk_bf(p[0], p[1]), pk_bf(p[2], p[3]), pk_bf(p[4], p[5]), pk_bf(p[6], p[7])};
    pf[(2 * w4 + 1) * 64 + lane] = u32x4{pk_bf(p[8], p[9]), pk_bf(p[10], p[11]), pk_bf(p[12], p[13]), pk_bf(p[14], p[15])};
    sbar4(cnt + 6, lane);
    u32x4 pb8[8];
#pragma unroll
    for (int ks = 0; ks < 8; ++ks) pb8[ks] = pf[ks * 64 + lane];
    const f32x4 l4 = *reinterpret_cast<const f32x4*>(wl + r * 4);
    const int qq = (lane & 15) >> 2, pp = lane & 3, blk2 = (lane >> 4) & 1;
    char* ot = scr + 12288 + w4 * 4096;
#pragma unroll
    for (int c2 = 0; c2 < 2; ++c2) {
      f32x16 oacc[2];
#pragma unroll
      for (int ct = 0; ct < 2; ++ct)
#pragma unroll
        for (int i = 0; i < 16; ++i) oacc[ct][i] = 0.f;
#pragma unroll
      for (int ks = 0; ks < 8; ++ks)
#pragma unroll
        for (int ct = 0; ct < 2; ++ct) {
          typedef __attribute__((address_space(3))) s16x4* lp;
          const char* tile = smem + ((ks >> 1) * 4 + w4) * 8192;
          const int cc = 2 * c2 + ct;
          const s16x4 lo = __builtin_amdgcn_ds_read_tr16_b64_v4i16((lp)(tile + toff(16 * (ks & 1) + 4 * hh + qq, 4 * cc + 2 * blk2 + (pp >> 1)) + 8 * (pp & 1)));
          const s16x4 hi = __builtin_amdgcn_ds_read_tr16_b64_v4i16((lp)(tile + toff(16 * (ks & 1) + 8 + 4 * hh + qq, 4 * cc + 2 * blk2 + (pp >> 1)) + 8 * (pp & 1)));
          const s16x8 afr = __builtin_shufflevector(lo, hi, 0, 1, 2, 3, 4, 5, 6, 7);
          oacc[ct] = __builtin_amdgcn_mfma_f32_32x32x16_bf16(__builtin_bit_cast(bf16x8_t, afr), __builtin_bit_cast(bf16x8_t, pb8[ks]), oacc[ct], 0, 0, 0);
        }
#pragma unroll
      for (int ct = 0; ct < 2; ++ct)
#pragma unroll
        for (int g = 0; g < 4; ++g)
          *reinterpret_cast<u32x2*>(ot + r * 128 + 16 * ((4 * ct + g) ^ (r & 7)) + 8 * hh) = u32x2{pk_bf(oacc[ct][4 * g], oacc[ct][4 * g + 1]), pk_bf(oacc[ct][4 * g + 2], oacc[ct][4 * g + 3])};
      if (c2 == 0) lds_add(cnt + 8, 1u, lane);
      __builtin_amdgcn_wave_barrier();
#pragma unroll
      for (int i = 0; i < 4; ++i) {
        const int hd = 8 * i + (lane >> 3), pc = lane & 7;
        const u32x4 vv = *reinterpret_cast<const u32x4*>(ot + hd * 128 + 16 * (pc ^ (hd & 7)));
        st16c(rs, WS_APO + ((hd * 64 + idx) * 512 + w4 * 128 + 64 * c2 + 8 * pc) * 2, vv);
      }
      __builtin_amdgcn_wave_barrier();
    }
    lds_arrive(cnt + 1, lane);
    STAMP(1);
    asm volatile("s_waitcnt vmcnt(0)" ::: "memory");
    if (lds_arrive(cnt + 9, lane) == 3u && hh == 0)
      st16c(rs, WS_AML + (idx * 32 + r) * 16, u32x4{__float_as_uint(m), tg.hi, __float_as_uint((l4[0] + l4[1]) + (l4[2] + l4[3])), tg.hi});
    lds_add(cnt + 8, ~0u, lane);
    STAMP(2);
    if (idx == 0 && w4 == 0) {
      lds_wait(cnt + 0, 4u);
#pragma unroll 1
      for (int spin = 0; spin < SPIN_MAX; ++spin) {
        COMPILER_FENCE();
        const u32x2 t0 = ld8c(rs, WS_AML + lane * 512), t1 = ld8c(rs, WS_QX + (64 * 64 + lane * 32) * 8);
        if (__all((t0[1] == tg.hi) & (t1[1] == tg.hi))) break;
        __builtin_amdgcn_s_sleep(8);
      }
      if (lane == 0) __hip_atomic_store((gu64*)(ws + WS_EP), ((u64)tg.hi << 32) | tg.lo, __ATOMIC_RELAXED, __HIP_MEMORY_SCOPE_AGENT);
    }
    STAMP_FLUSH(0); TR_FLUSH();
    return;
  }
  if (cls == 1) {
    const int idx = idx2, h = idx >> 1, b = idx & 1;
    {
      const u16* pa = w_uv + (w4 * 128 + 16 * (lane >> 3)) * 4096 + h * 128 + b * 64 + 8 * (lane & 7);
      const u16* pb = w_o + (h * 128 + b * 64 + w4 * 2) * 512 + 8 * lane;
#pragma unroll
      for (int i = 0; i < 16; ++i) {
        Wa[i] = ld16nt(pa + i * 4096);
        Wb[i] = ld16nt(pb + (i >> 1) * 4096 + (i & 1) * 512);
        if (i >= PACE1) asm volatile("" : "+v"(Wb[i - PACE1]));
        if ((i & 3) == 3) TR();
        SCHED();
      }
    }
    float* red2 = red + 2048;
    float* wsh = red + 3072;
    if (w4 == 0) {
      const unsigned fo = WS_AML + (lane * 32 + h) * 16;
      while (wall_clock64() - tstart < TAML) __builtin_amdgcn_s_sleep(8);
      u32x4 g;
      bool ok = false;
#pragma unroll 1
      for (int spin = 0; spin < SPIN_MAX; ++spin) {
        COMPILER_FENCE();
        if (!ok) g = ld16c(rs, fo);
        ok = (g[1] == tg.hi) & (g[3] == tg.hi);
        if (__all(ok)) break;
        __builtin_amdgcn_s_sleep(2);
      }
      lds_add(cnt + 7, 1u, lane);
      const float mu = __uint_as_float(g[0]), lu = __uint_as_float(g[2]);
      const float M = x16_max(row16_max(mu));
      const float wl = ex2((mu - M) * C2);
      wsh[lane] = wl / x16_sum(row16_sum(wl * lu));
      lds_arrive(cnt + 7, lane);
    } else lds_wait(cnt + 7, 1u);
    COMPILER_FENCE();
    STAMP(3);
    u32x4 pp[16];
#pragma unroll
    for (int i = 0; i < 16; ++i) pp[i] = ld16c(rs, WS_APO + (h * 64 + w4 * 16 + i) * 1024 + lane * 16);
    SCHED();
    lds_wait(cnt + 7, 2u);
    float a8[8];
#pragma unroll
    for (int k = 0; k < 8; ++k) a8[k] = 0.f;
    const float wv = wsh[w4 * 16 + c16];
#pragma unroll
    for (int i = 0; i < 16; ++i) {
      const float wt = rdlane(wv, i);
#pragma unroll
      for (int k = 0; k < 4; ++k) { a8[2 * k] = fmaf(wt, bflo(pp[i][k]), a8[2 * k]); a8[2 * k + 1] = fmaf(wt, bfhi(pp[i][k]), a8[2 * k + 1]); }
      asm volatile("" : "+v"(a8[0]), "+v"(a8[1]), "+v"(a8[2]), "+v"(a8[3]), "+v"(a8[4]), "+v"(a8[5]), "+v"(a8[6]), "+v"(a8[7]));
      SCHED();
    }
    *reinterpret_cast<f32x4*>(red + w4 * 512 + 8 * lane) = f32x4{a8[0], a8[1], a8[2], a8[3]};
    *reinterpret_cast<f32x4*>(red + w4 * 512 + 8 * lane + 4) = f32x4{a8[4], a8[5], a8[6], a8[7]};
    sbar4(cnt + 3, lane);
    STAMP(4);
#pragma unroll
    for (int i = 0; i < 4; ++i) {
      const float* src = red + (i >> 1) * 1024 + w4 * 128 + 16 * (lane >> 3) + 8 * (i & 1);
      const f32x4 t0 = *reinterpret_cast<const f32x4*>(src) + *reinterpret_cast<const f32x4*>(src + 512), t1 = *reinterpret_cast<const f32x4*>(src + 4) + *reinterpret_cast<const f32x4*>(src + 516);
      xv[i] = u32x4{pk_bf(t0[0], t0[1]), pk_bf(t0[2], t0[3]), pk_bf(t1[0], t1[1]), pk_bf(t1[2], t1[3])};
    }
    gemv(red2 + w4 * 128);
    sbar4(cnt + 4, lane);
    STAMP(12);
    unsigned op;
    {
      f32x2 t = {0.f, 0.f};
#pragma unroll
      for (int w = 0; w < 4; ++w) t += *reinterpret_cast<const f32x2*>(red2 + w * 128 + (lane >> 5) * 64 + 8 * (lane & 7) + 2 * w4);
      op = pk_bf(t[0], t[1]);
    }
    float* red3 = red + 4096;
    float a[2][8];
#pragma unroll
    for (int bb = 0; bb < 2; ++bb)
#pragma unroll
      for (int k = 0; k < 8; ++k) a[bb][k] = 0.f;
#pragma unroll
    for (int ip = 0; ip < 8; ++ip) {
      const unsigned v0 = __builtin_amdgcn_readlane(op, ip), v1 = __builtin_amdgcn_readlane(op, 32 + ip);
#pragma unroll
      for (int k = 0; k < 4; ++k) {
        const unsigned lo = plo(Wb[2 * ip][k], Wb[2 * ip + 1][k]), hi = phi(Wb[2 * ip][k], Wb[2 * ip + 1][k]);
        a[0][2 * k] = dot2(lo, v0, a[0][2 * k]); a[0][2 * k + 1] = dot2(hi, v0, a[0][2 * k + 1]);
        a[1][2 * k] = dot2(lo, v1, a[1][2 * k]); a[1][2 * k + 1] = dot2(hi, v1, a[1][2 * k + 1]);
      }
    }
#pragma unroll
    for (int bb = 0; bb < 2; ++bb) {
      *reinterpret_cast<f32x4*>(red3 + (w4 * 2 + bb) * 512 + 8 * lane) = f32x4{a[bb][0], a[bb][1], a[bb][2], a[bb][3]};
      *reinterpret_cast<f32x4*>(red3 + (w4 * 2 + bb) * 512 + 8 * lane + 4) = f32x4{a[bb][4], a[bb][5], a[bb][6], a[bb][7]};
    }
    sbar4(cnt + 5, lane);
    {
      f32x4 t = {0.f, 0.f, 0.f, 0.f};
#pragma unroll
      for (int w = 0; w < 4; ++w) t += *reinterpret_cast<const f32x4*>(red3 + w * 1024 + 4 * tid);
      st16c(rs, WS_GA + idx * 4096 + tid * 16, u32x4{pk_bf(t[0], t[1]), tg.hi, pk_bf(t[2], t[3]), tg.hi});
    }
    STAMP(5);
    STAMP_FLUSH(0); TR_FLUSH();
    return;
  }
  {
    const unsigned doff = WS_GA + (w4 * 16 + (lane >> 2)) * 4096 + idx * 64 + (lane & 3) * 16;
    lds_wait(cnt + 0, 4u);
    u32x4 d;
    bool ok = false;
#pragma unroll 1
    for (int spin = 0; spin < SPIN_MAX; ++spin) {
      COMPILER_FENCE();
      if (!ok) d = ld16c(rs, doff);
      ok = (d[1] == tg.hi) & (d[3] == tg.hi);
      if (__all(ok)) break;
      __builtin_amdgcn_s_sleep(1);
    }
    const float a[4] = {bflo(d[0]), bfhi(d[0]), bflo(d[2]), bfhi(d[2])};
    fin(a, out + (idx >> 5) * 1024 + 16 * (idx & 31));
  }
  STAMP(6);
  STAMP_FLUSH(0); TR_FLUSH();
}

extern "C" void kernel_launch(void* const* d_in, const int* in_sizes, int n_in, void* d_out, int out_size, void* d_ws, size_t ws_size, hipStream_t stream) {
  const u16* xA = (const u16*)d_in[0];
  const u16* ckv = (const u16*)d_in[1];
  const u16* w_q = (const u16*)d_in[2];
  const u16* w_uk = (const u16*)d_in[3];
  const u16* w_uv = (const u16*)d_in[4];
  const u16* w_o = (const u16*)d_in[5];
  const u16* xB = (const u16*)d_in[6];
  const u16* kc = (const u16*)d_in[7];
  const u16* vc = (const u16*)d_in[8];
  const u16* wq = (const u16*)d_in[9];
  const u16* wo = (const u16*)d_in[10];
  kmain<<<256, 512, 0, stream>>>(kc, vc, (char*)d_ws, ckv, w_q, wq, w_uk, xA, xB, w_uv, w_o, wo, (u16*)d_out);
}
```

```cpp
#include <hip/hip_runtime.h>
#include <hip/hip_bf16.h>
#include <stdint.h>

#define DI __device__ __forceinline__
typedef __attribute__((ext_vector_type(4))) unsigned u32x4;
typedef __attribute__((ext_vector_type(2))) unsigned u32x2;
typedef __attribute__((ext_vector_type(4))) float f32x4;
typedef __attribute__((ext_vector_type(2))) float f32x2;
typedef __attribute__((ext_vector_type(16))) float f32x16;
typedef __attribute__((ext_vector_type(8))) __bf16 bf16x8_t;
typedef __attribute__((ext_vector_type(2))) __bf16 bf16x2_t;
typedef __attribute__((ext_vector_type(4))) short s16x4;
typedef __attribute__((ext_vector_type(8))) short s16x8;
typedef unsigned short u16;
typedef unsigned long long u64;

#define QTAB 53,53,74,76, 53,52,73,78, 53,53,74,76, 53,55,73,75, 53,53,74,76, 53,52,76,75, 53,52,76,75, 53,53,74,76
#define QTAB7 QTAB
#define AHK 1
#define AHV 1
#define WROT 1
#define WS 0
DI constexpr int rounds(int q) { return q <= 48 ? 6 : (q <= 64 ? 8 : 10); }
constexpr int QX_[64] = {QTAB, QTAB7};
constexpr u64 qpair(int r) { u64 v = 0; for (int i = 0; i < 8; ++i) v |= (u64)QX_[8 * r + i] << (8 * i); return v; }
constexpr bool qok() { bool ok = true; for (int r = 0; r < 16; ++r) { int s = 0; for (int c = 0; c < 4; ++c) { s += QX_[4 * r + c]; ok &= QX_[4 * r + c] >= 10 && QX_[4 * r + c] <= 80; } ok &= s == 256; } return ok; }
static_assert(qok(), "every key once");
#define PACE0 4
#define PACE1 3
#define ROT2 0
#define TQX 500
#define TAML 1670
#define XK 0
#define TFIN 0
#define HG 1
#define PG 16
#define H1 0
#define TE 0
#define TD 95
#define TE1 400
#define PF 1
#define BURST 1
#define TPUB 0
#define FSL 0
#define QT 1
constexpr int SK = 4096;
constexpr float C2 = 0.08838834764831845f * 1.4426950408889634f;
constexpr u64 GOLD = 0x9E3779B97F4A7C15ull;

constexpr unsigned WS_EP = 0;
constexpr unsigned WS_QX = 4096;
constexpr unsigned WS_QA = WS_QX + 2 * 2 * 32 * 64 * 8;
constexpr unsigned WS_AML = WS_QA + 2 * 32 * 256 * 8;
constexpr unsigned WS_APO = WS_AML + 64 * 32 * 16;
constexpr unsigned WS_GB = WS_APO + 32 * 64 * 512 * 2;
constexpr unsigned WS_GA = WS_GB + 2 * 128 * 128 * 16;
constexpr unsigned WS_END = WS_GA + 64 * 2 * 128 * 16;
#define STAMP(i) do {} while (0)
#define STAMP_DECL do {} while (0)
#define STAMP_FLUSH(grp) do {} while (0)

#define TR_DECL do {} while (0)
#define TR() do {} while (0)
#define TR_FLUSH() do {} while (0)
DI float bflo(unsigned u) { return __uint_as_float(u << 16); }
DI float bfhi(unsigned u) { return __uint_as_float(u & 0xffff0000u); }
DI unsigned pk_bf(float a, float b) { const f32x2 v = {a, b}; return __builtin_bit_cast(unsigned, __builtin_convertvector(v, bf16x2_t)); }
DI float dot2(unsigned k, unsigned q, float acc) {
  return __builtin_amdgcn_fdot2_f32_bf16(__builtin_bit_cast(bf16x2_t, k), __builtin_bit_cast(bf16x2_t, q), acc, false);
}
DI unsigned plo(unsigned w0, unsigned w1) { return __builtin_amdgcn_perm(w1, w0, 0x05040100u); }
DI unsigned phi(unsigned w0, unsigned w1) { return __builtin_amdgcn_perm(w1, w0, 0x07060302u); }
template <int CTRL> DI float dpp(float x) {
  return __builtin_bit_cast(float, __builtin_amdgcn_mov_dpp(__builtin_bit_cast(int, x), CTRL, 0xf, 0xf, true));
}
DI float row16_sum(float x) { x += dpp<0xB1>(x); x += dpp<0x4E>(x); x += dpp<0x141>(x); x += dpp<0x128>(x); return x; }
DI float row16_max(float x) { x = fmaxf(x, dpp<0xB1>(x)); x = fmaxf(x, dpp<0x4E>(x)); x = fmaxf(x, dpp<0x141>(x)); x = fmaxf(x, dpp<0x128>(x)); return x; }
DI float x16_sum(float x) { auto s = __builtin_amdgcn_permlane16_swap(__float_as_uint(x), __float_as_uint(x), false, false); return __uint_as_float(s[0]) + __uint_as_float(s[1]); }
DI float x16_max(float x) { auto s = __builtin_amdgcn_permlane16_swap(__float_as_uint(x), __float_as_uint(x), false, false); return fmaxf(__uint_as_float(s[0]), __uint_as_float(s[1])); }
DI float xhalf_max(float x) { auto t = __builtin_amdgcn_permlane32_swap(__float_as_uint(x), __float_as_uint(x), false, false); return fmaxf(__uint_as_float(t[0]), __uint_as_float(t[1])); }
DI float xhalf_sum(float x) { auto t = __builtin_amdgcn_permlane32_swap(__float_as_uint(x), __float_as_uint(x), false, false); return __uint_as_float(t[0]) + __uint_as_float(t[1]); }
DI float xrow_max(float x) { return xhalf_max(x16_max(x)); }
DI float xrow_sum(float x) { return xhalf_sum(x16_sum(x)); }
DI u32x4 ld16(const void* p) { return *reinterpret_cast<const u32x4*>(p); }
DI u32x4 ld16nt(const void* p) { return __builtin_nontemporal_load(reinterpret_cast<const u32x4*>(p)); }
DI float ex2(float x) { return __builtin_amdgcn_exp2f(x); }
#define SCHED() __builtin_amdgcn_sched_barrier(0)

typedef __attribute__((address_space(1))) u64 gu64;
typedef __amdgpu_buffer_rsrc_t rsrc_t;
DI u32x4 ld16c(rsrc_t rs, unsigned off) { return __builtin_amdgcn_raw_buffer_load_b128(rs, (int)off, 0, 16); }
DI u32x2 ld8c(rsrc_t rs, unsigned off) { return __builtin_amdgcn_raw_buffer_load_b64(rs, (int)off, 0, 16); }
DI void st16c(rsrc_t rs, unsigned off, u32x4 v) { __builtin_amdgcn_raw_buffer_store_b128(v, rs, (int)off, 0, 16); }
DI void st8c(rsrc_t rs, unsigned off, u32x2 v) { __builtin_amdgcn_raw_buffer_store_b64(v, rs, (int)off, 0, 16); }
constexpr int SPIN_MAX = 1 << 20;
constexpr unsigned OOB = 0x7ffffff0u;
DI void rawbar() { __builtin_amdgcn_sched_barrier(0); __builtin_amdgcn_s_barrier(); __builtin_amdgcn_sched_barrier(0); }
struct Tag { unsigned hi, lo; };
#define COMPILER_FENCE() asm volatile("" ::: "memory")

template <int N> struct IC { static constexpr int v = N; };
template <int I, int N, class F> DI void sfor(F&& f) { if constexpr (I < N) { f(IC<I>{}); sfor<I + 1, N>(f); } }
typedef __attribute__((address_space(3))) unsigned lu32;
DI void lds_wait(unsigned* cnt, unsigned target) {
#pragma unroll 1
  for (int spin = 0; spin < SPIN_MAX; ++spin) {
    if (__builtin_amdgcn_readfirstlane(__hip_atomic_load((lu32*)cnt, __ATOMIC_RELAXED, __HIP_MEMORY_SCOPE_WORKGROUP)) >= target) break;
    __builtin_amdgcn_s_sleep(1);
  }
  COMPILER_FENCE();
}
DI void lds_hold(unsigned* flag) {
#pragma unroll 1
  for (int spin = 0; spin < SPIN_MAX; ++spin) {
    if (__builtin_amdgcn_readfirstlane(__hip_atomic_load((lu32*)flag, __ATOMIC_RELAXED, __HIP_MEMORY_SCOPE_WORKGROUP)) == 0u) break;
    __builtin_amdgcn_s_sleep(2);
  }
}
DI unsigned lds_arrive_nw(unsigned addr) {
  unsigned old, one = 1u;
  asm volatile("s_mov_b64 exec, 1\n\tds_add_rtn_u32 %0, %1, %2\n\ts_mov_b64 exec, -1" : "=v"(old) : "v"(addr), "v"(one) : "memory");
  return old;
}
DI unsigned lds_done(unsigned old) { asm volatile("s_waitcnt lgkmcnt(0)" : "+v"(old) :: "memory"); return __builtin_amdgcn_readfirstlane(old); }
DI unsigned lds_arrive(unsigned* cnt, int) { return lds_done(lds_arrive_nw((unsigned)(size_t)(lu32*)cnt)); }
DI void lds_add(unsigned* cnt, unsigned v, int) {
  asm volatile("s_mov_b64 exec, 1\n\tds_add_u32 %0, %1\n\ts_mov_b64 exec, -1" :: "v"((unsigned)(size_t)(lu32*)cnt), "v"(v) : "memory");
}
DI void sbar4(unsigned* cnt, int lane) { if (lds_arrive(cnt, lane) < 3u) lds_wait(cnt, 4u); }
DI float rs32(float a, float b) { auto t = __builtin_amdgcn_permlane32_swap(__float_as_uint(a), __float_as_uint(b), false, false); return __uint_as_float(t[0]) + __uint_as_float(t[1]); }
DI float rs16(float a, float b) { auto t = __builtin_amdgcn_permlane16_swap(__float_as_uint(a), __float_as_uint(b), false, false); return __uint_as_float(t[0]) + __uint_as_float(t[1]); }
template <int N> DI unsigned rowbcast(unsigned x) { return (unsigned)__builtin_amdgcn_mov_dpp((int)x, 0x150 + N, 0xf, 0xf, true); }
DI void pin2(u32x4& a, u32x4& b) { asm volatile("" : "+v"(a), "+v"(b)); }
DI void pin16(u32x4* W) {
#pragma unroll
  for (int i = 0; i < 16; i += 2) asm volatile("" : "+v"(W[i]), "+v"(W[i + 1]));
}
DI float rdlane(float x, int l) { return __builtin_bit_cast(float, __builtin_amdgcn_readlane(__builtin_bit_cast(int, x), l)); }

DI unsigned toff(unsigned row, unsigned ch) { return 256u * row + 16u * (ch ^ (((row & 3) << 2) | ((row >> 2) & 3))); }
constexpr int SMEM = 160 * 1024;
constexpr int L_SCR = 131072;
constexpr int L_MRG = 65536;
constexpr int L_CNT = L_SCR + 32 * 1008;

struct KV { u32x4 r[8]; };

typedef const __attribute__((address_space(1))) char* gptr;
struct Args { gptr t[5][4]; gptr w_o; gptr out; };

__global__ __launch_bounds__(512, 2) __attribute__((aligned(4096))) void kmain(Args a) {
  __shared__ __attribute__((aligned(16))) char smem[SMEM];
  const int tid = threadIdx.x, blk = blockIdx.x, lane = tid & 63;
  const int wave = __builtin_amdgcn_readfirstlane(tid >> 6), w4 = wave & 3;
  const int cls = (blk >> 3) & 3;
  const int arow = wave < 4 ? cls : 4;
  gptr p0 = a.t[arow][0], p1 = a.t[arow][1], p2 = a.t[arow][2], p3 = a.t[arow][3], p4 = a.w_o, p5 = a.out;
  const u64 tstart = wall_clock64();
  SCHED();
  int idx = 8 * (blk & 7) + (blk >> 5);
  if (wave < 4) idx = (idx & ~7) | ((idx + (cls == 3 ? 2 : (cls == 2 ? 5 : 0))) & 7);
  const int h = idx >> 1, b = idx & 1;
  const int idx2 = (idx & ~7) | ((idx + ROT2) & 7);
  const int r4 = lane >> 4, c16 = lane & 15;
  unsigned* cnt = reinterpret_cast<unsigned*>(smem + L_CNT);
  STAMP_DECL;
  TR_DECL;
  STAMP(0);
  Tag tg;
  if (wave >= 4) {
    if (tid - 256 < 16) cnt[tid - 256] = 0u;
    const unsigned t0s = TE ? (unsigned)tstart - (cls == 1 ? TE1 : 0) : 0u;
    int nr;
    unsigned tab;
    {
      const int row = idx & 7;
      const u64 q2 = idx < 56 ? (row < 4 ? (row < 2 ? qpair(0) : qpair(1)) : (row < 6 ? qpair(2) : qpair(3))) : (row < 4 ? (row < 2 ? qpair(4) : qpair(5)) : (row < 6 ? qpair(6) : qpair(7)));
      const unsigned q4 = (unsigned)(q2 >> (32 * (row & 1)));
      nr = rounds((q4 >> (8 * cls)) & 255);
      int j = 2 * nr - 1 - lane;
      j = lane < nr ? lane : (j > 2 ? j : (j == 2 ? 0 : j + 1));
      int n = j + (h * nr >> 5);
      n = n >= nr ? n - nr : n;
      int off = 0, wd = 0;
#pragma unroll
      for (int d = 0; d < 4; ++d) {
        const int q = (q4 >> (8 * d)) & 255, nd = rounds(q), bs = q * (nd == 6 ? 342 : (nd == 8 ? 256 : 205)) >> 11, rm = q - nd * bs;
        const int k = ((2 * n + 1) * nd + (d < cls) - 1 + nr) * (nr == 6 ? 5462 : (nr == 8 ? 4096 : 3277)) >> 16;
        off += k * bs + min(k, rm);
        wd = d == cls ? bs + (n < rm) : wd;
      }
      tab = lane < 2 * nr ? (unsigned)(4 * off + w4 * wd) << 10 | (unsigned)wd : 0u;
    }
    SCHED();
    char* ws = (char*)p3;
    const rsrc_t rs = __builtin_amdgcn_make_buffer_rsrc(ws, 0, (int)WS_END, 0x00020000);
    const char* kb = (const char*)p0 + ((size_t)(b * 32 + h) << 20);
    const char* vb = (const char*)p1 + ((size_t)(b * 32 + h) << 20);
    const unsigned lo = lane * 16u, lo2 = lo + 4096u;
    int nx = 0;
    auto nexts = [&]() {
      const unsigned e = __builtin_amdgcn_readlane(tab, nx);
      const rsrc_t r = __builtin_amdgcn_make_buffer_rsrc(const_cast<char*>((nx < nr ? kb : vb) + (e & ~1023u)), 0, (int)((e & 15u) << 10), 0x00020000);
      ++nx;
      return r;
    };
    const rsrc_t s0 = nexts();
    const int rot = WROT ? cls : 0;
    const char* wb = (const char*)p2 + h * 131072;
    unsigned wl = r4 * 8192 + w4 * 256 + c16 * 16;
    asm volatile("s_waitcnt lgkmcnt(0)" : "+v"(wl), "+v"(tab) :: "memory");
    rawbar();
    KV A, B, X;
    u32x4 W[8][4];
    u32x4 T[4][8];
    auto ldp = [&](KV& S, auto P, rsrc_t q) {
      constexpr int p = P.v;
      S.r[2 * p] = __builtin_amdgcn_raw_buffer_load_b128(q, (int)((p < 2 ? lo : lo2) + (p & 1) * 2048), 0, 2);
      S.r[2 * p + 1] = __builtin_amdgcn_raw_buffer_load_b128(q, (int)((p < 2 ? lo : lo2) + (p & 1) * 2048 + 1024), 0, 2);
    };
    auto landed = [&](KV& S, auto P) { asm volatile("" : "+v"(S.r[2 * P.v]), "+v"(S.r[2 * P.v + 1])); };
    auto ldw = [&](auto G, auto J) { W[G.v][J.v] = (XK & 4) ? u32x4{wl, wl, wl, wl} : ld16(wb + ((((G.v >> 1) + rot) & 3) * 32768 + (G.v & 1) * 4096) + wl + J.v * 1024); };
    auto conv = [&](auto G) {
      constexpr int t = G.v >> 1, j0 = (G.v & 1) * 2;
#pragma unroll
      for (int jj = 0; jj < 2; ++jj)
#pragma unroll
        for (int e = 0; e < 4; ++e) {
          const unsigned a_ = W[G.v][2 * jj][e], b_ = W[G.v][2 * jj + 1][e];
          T[t][2 * e][j0 + jj] = plo(a_, b_); T[t][2 * e + 1][j0 + jj] = phi(a_, b_);
        }
#pragma unroll
      for (int e = 0; e < 8; e += 2) pin2(T[t][e], T[t][e + 1]);
    };
    u32x2 tw;
    {
      const rsrc_t s1 = nexts(), s2 = nexts();
      sfor<0, 12>([&](auto I) {
        constexpr int i = I.v;
        if constexpr (i >= 4) { if constexpr (i < 8) landed(X, IC<i & 3>{}); else landed(B, IC<i & 3>{}); }
        if constexpr (i == 7 || i == 11) TR();
        if constexpr (i == 8) tw = ld8c(rs, lane == 0 ? WS_GB + (b * 128 + h * 4 + cls) * 2048 + 512 * w4 : OOB);
        if constexpr (i < 4) { ldp(X, IC<i & 3>{}, s0); ldw(IC<0>{}, IC<i & 3>{}); }
        else if constexpr (i < 8) { ldp(B, IC<i & 3>{}, s1); ldw(IC<1>{}, IC<i & 3>{}); if constexpr (WS) ldw(IC<2>{}, IC<i & 3>{}); }
        else { ldp(A, IC<i & 3>{}, s2); ldw(IC<2 + WS>{}, IC<i & 3>{}); if constexpr (WS) ldw(IC<4>{}, IC<i & 3>{}); }
        SCHED();
        if constexpr (i == 8) { conv(IC<0>{}); SCHED(); }
      });
    }
    lds_wait(cnt + 15, 1u);
    tg.hi = __builtin_amdgcn_readfirstlane(cnt[13]); tg.lo = __builtin_amdgcn_readfirstlane(cnt[14]);
    asm volatile("" :: "v"(tw));
    u32x4 qpk;
    {
      const unsigned qo = WS_QX + (((2 + b) * 32 + h) * 64 + 4 * c16) * 8;
      u32x4 g0 = ld16c(rs, qo), g1 = ld16c(rs, qo + 16);
      SCHED();
      conv(IC<1>{}); if constexpr (WS) conv(IC<2>{});
      SCHED();
#pragma unroll 1
      for (int spin = 0; spin < SPIN_MAX; ++spin) {
        if ((XK & 1) || __all((g0[1] == tg.hi) & (g0[3] == tg.hi) & (g1[1] == tg.hi) & (g1[3] == tg.hi))) break;
        COMPILER_FENCE();
        g0 = ld16c(rs, qo); g1 = ld16c(rs, qo + 16);
      }
      qpk = (XK & 1) ? u32x4{lo, lo, lo, lo} : u32x4{g0[0], g0[2], g1[0], g1[2]};
    }
    STAMP(7);
    const bool holds = cls == 0 || (H1 && cls == 1);
    float sc[10], acc[8];
#pragma unroll
    for (int k = 0; k < 10; ++k) sc[k] = 0.f;
    auto point = [&](KV& S, KV& N, rsrc_t sn, auto P, auto REFILL, auto AH, auto WG) {
      constexpr int ah = AH.v;
      if constexpr (REFILL.v) {
        if constexpr (P.v == 0 && ah > 0) { if (holds) lds_hold(cnt + 8); sfor<0, ah>([&](auto Q) { ldp(N, Q, sn); }); SCHED(); }
        landed(S, P);
        if constexpr (P.v + ah < 4) { if constexpr ((P.v & 1) == 0) { if (holds) lds_hold(cnt + 8); } ldp(N, IC<(P.v + ah) & 3>{}, sn); }
        if constexpr (WG.v >= 0) ldw(WG, P);
        SCHED();
      }
    };
    auto step = [&](KV& S, KV& N, auto REFILL, auto WG, auto ISV) {
      rsrc_t sn = s0;
      if constexpr (TE && REFILL.v && ISV.v) {
        const unsigned due = t0s + TE - (2 * nr - nx) * TD;
#pragma unroll 1
        while ((int)((unsigned)wall_clock64() - due) < 0) __builtin_amdgcn_s_sleep(1);
      }
      if constexpr (REFILL.v) sn = nexts();
      sfor<0, 4>([&](auto P) {
        point(S, N, sn, P, REFILL, IC<(ISV.v ? AHV : (WG.v < 0 ? AHK : 0))>{}, WG);
        if constexpr (!ISV.v) {
#pragma unroll
          for (int t = 2 * P.v; t < 2 * P.v + 2; ++t) {
            acc[t] = 0.f;
#pragma unroll
            for (int k = 0; k < 4; ++k) acc[t] = dot2(S.r[t][k], qpk[k], acc[t]);
          }
          asm volatile("" : "+v"(acc[2 * P.v]), "+v"(acc[2 * P.v + 1]));
        } else {
          const unsigned pp = rowbcast<4 * P.v>(__float_as_uint(sc[0]));
#pragma unroll
          for (int k = 0; k < 4; ++k) { acc[2 * k] = dot2(plo(S.r[2 * P.v][k], S.r[2 * P.v + 1][k]), pp, acc[2 * k]); acc[2 * k + 1] = dot2(phi(S.r[2 * P.v][k], S.r[2 * P.v + 1][k]), pp, acc[2 * k + 1]); }
          asm volatile("" : "+v"(acc[0]), "+v"(acc[1]), "+v"(acc[2]), "+v"(acc[3]), "+v"(acc[4]), "+v"(acc[5]), "+v"(acc[6]), "+v"(acc[7]));
        }
        SCHED();
      });
      if constexpr (!ISV.v) {
        float u[4], v[2];
#pragma unroll
        for (int i = 0; i < 4; ++i) u[i] = (c16 & 8 ? acc[i + 4] : acc[i]) + dpp<0x128>(c16 & 8 ? acc[i] : acc[i + 4]);
#pragma unroll
        for (int i = 0; i < 2; ++i) v[i] = (c16 & 4 ? u[i + 2] : u[i]) + dpp<0x141>(c16 & 4 ? u[i] : u[i + 2]);
        float x = (c16 & 2 ? v[1] : v[0]) + dpp<0x4E>(c16 & 2 ? v[0] : v[1]);
        x += dpp<0xB1>(x);
#pragma unroll
        for (int k = 9; k > 0; --k) sc[k] = sc[k - 1];
        sc[0] = x;
      } else {
#pragma unroll
        for (int k = 0; k < 9; ++k) sc[k] = sc[k + 1];
      }
      SCHED();
      if constexpr (REFILL.v || ISV.v) TR();
    };
    step(B, B, IC<0>{}, IC<-1>{}, IC<0>{});
    step(A, B, IC<1>{}, IC<3 + 2 * WS>{}, IC<0>{});
    step(X, X, IC<0>{}, IC<-1>{}, IC<0>{});
    conv(IC<2 + WS>{}); if constexpr (WS) conv(IC<4>{});
    sfor<3, 6>([&](auto I) {
      if constexpr (I.v & 1) step(B, A, IC<1>{}, IC<I.v + 1>{}, IC<0>{}); else step(A, B, IC<1>{}, IC<I.v + 1>{}, IC<0>{});
      conv(I);
    });
#pragma unroll 1
    for (int i = 6; i < nr; i += 2) {
      step(A, B, IC<1>{}, IC<-1>{}, IC<0>{});
      step(B, A, IC<1>{}, IC<-1>{}, IC<0>{});
    }
    float m = -INFINITY, l = 0.f;
#pragma unroll
    for (int k = 0; k < 10; ++k) { sc[k] = (unsigned)(c16 >> 1) < (__builtin_amdgcn_readlane(tab, nr + k) & 15u) ? sc[k] : -INFINITY; m = fmaxf(m, sc[k]); }
    m = row16_max(m);
    {
      const float mc = m * C2;
#pragma unroll
      for (int k = 0; k < 10; ++k) {
        const float p = ex2(fmaf(sc[k], C2, -mc));
        const unsigned pk = pk_bf(dpp<0x00>(p), dpp<0xAA>(p));
        l = dot2(pk, 0x3f803f80u, l);
        sc[k] = __uint_as_float(pk);
      }
      l += dpp<0x124>(l); l += dpp<0x128>(l);
    }
#pragma unroll
    for (int k = 0; k < 8; ++k) acc[k] = 0.f;
    SCHED();
    STAMP(8);
    unsigned yoff = (r4 & 1) ? OOB : WS_GB + (b * 128 + h * 4 + cls) * 2048 + 512 * w4 + 32 * c16 + 16 * (lane >> 5), zsh = 16 * (c16 & 1) + 8 * (lane >> 5);
    unsigned rwo = L_MRG + (w4 * 4 + r4) * 272 + ((c16 - 4 * rot) & 15) * 16, rro = L_MRG + c16 * 272 + r4 * 16, mwo = L_MRG + 16 * 272 + ((c16 & 1) * 16 + w4 * 4 + r4) * 4, mro = L_MRG + 16 * 272 + c16 * 4, m4o = L_MRG + 16 * 272 + r4 * 16;
    asm volatile("" : "+v"(zsh), "+v"(yoff), "+v"(rwo), "+v"(rro), "+v"(mwo), "+v"(mro), "+v"(m4o));
    step(A, B, IC<1>{}, IC<7>{}, IC<1>{});
    conv(IC<6>{});
#pragma unroll 1
    for (int i = 2;;) {
      step(B, A, IC<1>{}, IC<-1>{}, IC<1>{});
      step(A, B, IC<1>{}, IC<-1>{}, IC<1>{});
      i += 2;
      if (i >= nr) break;
    }
    conv(IC<7>{});
    if (cls == 0) lds_wait(cnt + 1, 4u);
    step(B, B, IC<0>{}, IC<-1>{}, IC<1>{});
    STAMP(1);
    *reinterpret_cast<u32x4*>(smem + rwo) = u32x4{pk_bf(acc[0], acc[1]), pk_bf(acc[2], acc[3]), pk_bf(acc[4], acc[5]), pk_bf(acc[6], acc[7])};
    *reinterpret_cast<float*>(smem + mwo) = (c16 & 1) ? l : m;
    {
      u32x4 af[4];
      float mr, lr;
      f32x4 m4;
      unsigned got = lds_arrive_nw(L_CNT), need = 3u;
#pragma unroll 1
      for (int spin = 0; spin < SPIN_MAX; ++spin) {
#pragma unroll
        for (int t = 0; t < 4; ++t) af[t] = *reinterpret_cast<const u32x4*>(smem + rro + t * 64);
        mr = *reinterpret_cast<const float*>(smem + mro); lr = *reinterpret_cast<const float*>(smem + mro + 64);
        m4 = *reinterpret_cast<const f32x4*>(smem + m4o);
        SCHED();
        if (lds_done(got) >= need) break;
        need = 4u;
        __builtin_amdgcn_s_sleep(1);
        asm volatile("ds_read_b32 %0, %1" : "=v"(got) : "v"((unsigned)L_CNT) : "memory");
      }
      SCHED();
      u64 tq = TPUB ? wall_clock64() : 0;
      f32x4 d[8];
#pragma unroll
      for (int e = 0; e < 8; ++e) {
        d[e] = f32x4{0.f, 0.f, 0.f, 0.f};
#pragma unroll
        for (int t = 0; t < 4; ++t) d[e] = __builtin_amdgcn_mfma_f32_16x16x32_bf16(__builtin_bit_cast(bf16x8_t, af[t]), __builtin_bit_cast(bf16x8_t, T[t][e]), d[e], 0, 0, 0);
      }
      const float M = row16_max(mr);
      const float L = row16_sum(ex2((mr - M) * C2) * lr);
      const float z = fmaf(M, C2, __builtin_amdgcn_logf(L));
      float wt[4], a[8];
#pragma unroll
      for (int i = 0; i < 4; ++i) wt[i] = ex2(fmaf(m4[i], C2, -z));
#pragma unroll
      for (int e = 0; e < 8; ++e) a[e] = fmaf(wt[3], d[e][3], fmaf(wt[2], d[e][2], fmaf(wt[1], d[e][1], wt[0] * d[e][0])));
      float v4[4];
#pragma unroll
      for (int k = 0; k < 4; ++k) v4[k] = x16_sum(rs32(a[k], a[k + 4]));
      if (TPUB) { while (tq - tstart < TPUB) { __builtin_amdgcn_s_sleep(1); tq = wall_clock64(); } }
      st16c(rs, yoff, u32x4{pk_bf(v4[0], v4[1]), tg.hi, pk_bf(v4[2], v4[3]), (tg.lo & 0xffffff00u) | ((__float_as_uint(z) >> zsh) & 0xffu)});
    }
    STAMP(2);
    STAMP_FLUSH(1); TR_FLUSH();
    return;
  }
  u32x4 Wa[16], Wb[16], xv[4];
  unsigned pf;
  u64 epw;
  {
    unsigned oa[16];
    oa[0] = (cls & 1) ? (w4 * 128 + 16 * (lane >> 3)) * 8192 + idx * 128 + 16 * (lane & 7) : (cls == 2 ? (b * 256 + w4 * 64 + 16 * r4) * 8192 + h * 256 + 16 * c16 : (idx * 128 + w4) * 1024 + 16 * lane);
#pragma unroll
    for (int i = 1; i < 16; ++i) oa[i] = oa[i - 1] + (cls ? 8192 : 4096);
    unsigned op = (((blk >> 5) * 4 + w4) * 16 + c16) * 8192 + ((idx & 56) + 3 + (cls == 0 ? -1 : (idx >= 56 ? 4 : 0)) * (lane >> 5)) * 128 + (r4 & 1) * 64;
    unsigned ox = w4 * 256 + 32 * (lane >> 3);
#pragma unroll
    for (int i = 0; i < 16; i += 8) asm volatile("" : "+v"(oa[i]), "+v"(oa[i + 1]), "+v"(oa[i + 2]), "+v"(oa[i + 3]), "+v"(oa[i + 4]), "+v"(oa[i + 5]), "+v"(oa[i + 6]), "+v"(oa[i + 7]));
    asm volatile("" : "+v"(op), "+v"(ox));
    SCHED();
    pf = *reinterpret_cast<const unsigned*>((const char*)p1 + op);
    epw = __hip_atomic_load((gu64*)p3, __ATOMIC_RELAXED, __HIP_MEMORY_SCOPE_AGENT);
#pragma unroll
    for (int i = 0; i < 4; ++i) xv[i] = ld16((const char*)p2 + ox + (i >> 1) * 1024 + 16 * (i & 1));
    SCHED();
#pragma unroll
    for (int i = 0; i < 16; ++i) { Wa[i] = ld16nt((const char*)p0 + oa[i]); SCHED(); }
  }
  char* ws = (char*)p3;
  const rsrc_t rs = __builtin_amdgcn_make_buffer_rsrc(ws, 0, (int)WS_END, 0x00020000);
  u16* out = (u16*)p5;
  auto fin = [&](const float (&a)[4], u16* dst) {
    float y = rs16(rs32(a[0], a[2]), rs32(a[1], a[3]));
    y += dpp<0x124>(y); y += dpp<0x128>(y);
    float* fr = reinterpret_cast<float*>(smem + 32768);
    if ((lane & 12) == 0) fr[w4 * 16 + 4 * (lane & 3) + r4] = y;
    COMPILER_FENCE();
    const unsigned got = lds_arrive_nw(L_CNT + 24);
    f32x2 t[4];
#pragma unroll
    for (int w = 0; w < 4; ++w) t[w] = *reinterpret_cast<const f32x2*>(fr + w * 16 + 2 * (lane & 7));
    SCHED();
    if (lds_done(got) == 3u) {
      const f32x2 r = (t[0] + t[1]) + (t[2] + t[3]);
      if (lane < 8) *reinterpret_cast<unsigned*>(dst + 2 * lane) = pk_bf(r[0], r[1]);
    }
  };
  {
    rawbar();
    const u64 t64 = (((u64)(unsigned)__builtin_amdgcn_readfirstlane((unsigned)(epw >> 32)) << 32) | (unsigned)__builtin_amdgcn_readfirstlane((unsigned)epw)) + GOLD;
    tg.hi = (unsigned)(t64 >> 32); tg.lo = (unsigned)t64;
    asm volatile("" : "+s"(tg.hi), "+s"(tg.lo) : "v"(pf) : "memory");
    if (w4 == 0) { cnt[13] = tg.hi; cnt[14] = tg.lo; COMPILER_FENCE(); lds_add(cnt + 15, 1u, lane); }
    STAMP(13);
  }
  float* red = reinterpret_cast<float*>(smem);
  if ((XK & 2) && cls == 2) { pin16(Wa); STAMP_FLUSH(0); TR_FLUSH(); return; }
  auto gemv = [&](float* dst) {
    float acc[2][8];
#pragma unroll
    for (int bb = 0; bb < 2; ++bb)
#pragma unroll
      for (int k = 0; k < 8; ++k) acc[bb][k] = 0.f;
#pragma unroll
    for (int ip = 0; ip < 8; ++ip) {
      const unsigned xp0 = xv[ip >> 2][ip & 3], xp1 = xv[2 + (ip >> 2)][ip & 3];
#pragma unroll
      for (int k = 0; k < 4; ++k) {
        const unsigned lo = plo(Wa[2 * ip][k], Wa[2 * ip + 1][k]), hi = phi(Wa[2 * ip][k], Wa[2 * ip + 1][k]);
        acc[0][2 * k] = dot2(lo, xp0, acc[0][2 * k]); acc[0][2 * k + 1] = dot2(hi, xp0, acc[0][2 * k + 1]);
        acc[1][2 * k] = dot2(lo, xp1, acc[1][2 * k]); acc[1][2 * k + 1] = dot2(hi, xp1, acc[1][2 * k + 1]);
      }
    }
    float v8[8], v4[4];
#pragma unroll
    for (int k = 0; k < 8; ++k) v8[k] = rs32(acc[0][k], acc[1][k]);
#pragma unroll
    for (int k = 0; k < 4; ++k) { const float t = rs16(v8[k], v8[k + 4]); v4[k] = t + dpp<0x128>(t); }
    *reinterpret_cast<f32x4*>(dst + (lane >> 5) * 64 + 8 * (lane & 7) + 4 * (r4 & 1)) = f32x4{v4[0], v4[1], v4[2], v4[3]};
    COMPILER_FENCE();
  };
  if (cls & 1) {
    gemv(red + w4 * 128);
    const unsigned got = lds_arrive_nw(L_CNT + 8);
    f32x4 t4[4];
#pragma unroll
    for (int w = 0; w < 4; ++w) t4[w] = *reinterpret_cast<const f32x4*>(red + w * 128 + 4 * (lane & 31));
    SCHED();
    if (lds_done(got) == 3u) {
      const f32x4 t = (t4[0] + t4[1]) + (t4[2] + t4[3]);
      st16c(rs, lane < 32 ? WS_QX + ((((cls >> 1) * 2 + r4) * 32 + h) * 64 + b * 32 + 2 * c16) * 8 : OOB, u32x4{pk_bf(t[0], t[1]), tg.hi, pk_bf(t[2], t[3]), tg.hi});
      SCHED();
      lds_add(cnt + 2, 1u, lane);
    } else lds_wait(cnt + 2, 5u);
    STAMP(8);
  } else if (cls == 2) {
    while (wall_clock64() - tstart < TQX) __builtin_amdgcn_s_sleep(4);
    u32x4 q0, q1;
    const unsigned qo = WS_QX + (h * 64 + 4 * c16) * 8;
#pragma unroll 1
    for (int spin = 0; spin < SPIN_MAX; ++spin) {
      COMPILER_FENCE();
      const u32x4 g0 = ld16c(rs, qo), g1 = ld16c(rs, qo + 16), g2 = ld16c(rs, qo + 16384), g3 = ld16c(rs, qo + 16400);
      q0 = u32x4{g0[0], g0[2], g1[0], g1[2]}; q1 = u32x4{g2[0], g2[2], g3[0], g3[2]};
      if (__all((g0[1] == tg.hi) & (g0[3] == tg.hi) & (g1[1] == tg.hi) & (g1[3] == tg.hi) & (g2[1] == tg.hi) & (g2[3] == tg.hi) & (g3[1] == tg.hi) & (g3[3] == tg.hi))) break;
      __builtin_amdgcn_s_sleep(1);
    }
    STAMP(9);
    float v[4];
    {
      float u[16], t8[8];
#pragma unroll
      for (int i = 0; i < 16; ++i) {
        float a = 0.f, c = 0.f;
#pragma unroll
        for (int k = 0; k < 4; ++k) { a = dot2(Wa[i][k], q0[k], a); c = dot2(Wa[i][k], q1[k], c); }
        u[i] = (c16 & 4 ? c : a) + dpp<0x141>(c16 & 4 ? a : c);
        asm volatile("" : "+v"(u[i]));
      }
#pragma unroll
      for (int i = 0; i < 8; ++i) { t8[i] = (c16 & 2 ? u[i + 8] : u[i]) + dpp<0x4E>(c16 & 2 ? u[i] : u[i + 8]); asm volatile("" : "+v"(t8[i])); }
#pragma unroll
      for (int i = 0; i < 4; ++i) {
        float t = (c16 & 1 ? t8[i + 4] : t8[i]) + dpp<0xB1>(c16 & 1 ? t8[i] : t8[i + 4]);
        asm volatile("" : "+v"(t));
        v[i] = t + dpp<0x128>(t);
      }
    }
    st16c(rs, c16 < 8 ? WS_QA + ((c16 >> 2) * 32 + h) * 2048 + (b * 256 + w4 * 64 + 16 * r4 + 4 * (c16 & 3)) * 4 : OOB, u32x4{pk_bf(v[0], v[1]), tg.hi, pk_bf(v[2], v[3]), tg.hi});
    STAMP(10);
  }
  if (cls >= 2) {
    const unsigned tw = __builtin_nontemporal_load(reinterpret_cast<const unsigned*>(out + (idx >> 5) * 1024 + (cls & 1) * 512 + 16 * (idx & 31)));
    asm volatile("" :: "v"(tw));
  }
  asm volatile("" :: "v"(xv[0]), "v"(xv[1]), "v"(xv[2]), "v"(xv[3]));
  SCHED();
  if (cls == 3) {
    const int fb = idx >> 5;
    const unsigned doff = WS_GB + (fb * 128 + 32 * w4 + (lane >> 2)) * 2048 + (idx & 31) * 64 + (lane & 3) * 16;
    lds_wait(cnt + 0, 4u);
    if (TFIN) { while (wall_clock64() - tstart < TFIN) __builtin_amdgcn_s_sleep(2); }
    STAMP(3);
    u32x4 d[2];
    bool ok = false;
#pragma unroll 1
    for (int spin = 0; spin < SPIN_MAX; ++spin) {
      COMPILER_FENCE();
      if (!ok) { d[0] = ld16c(rs, doff); d[1] = ld16c(rs, doff + 32768); }
      ok = (((d[0][1] ^ tg.hi) | (d[1][1] ^ tg.hi)) | (((d[0][3] ^ tg.lo) | (d[1][3] ^ tg.lo)) >> 8)) == 0u;
      if (__all(ok)) break;
      if (FSL) __builtin_amdgcn_s_sleep(FSL);
    }
    STAMP(14);
    float a[4] = {0.f, 0.f, 0.f, 0.f};
#pragma unroll
    for (int j = 0; j < 2; ++j) {
      unsigned zb = (d[j][3] & 0xffu) << (8 * (lane & 3));
      zb |= __float_as_uint(dpp<0xB1>(__uint_as_float(zb))); zb |= __float_as_uint(dpp<0x4E>(__uint_as_float(zb)));
      const float z = __uint_as_float(zb);
      float Z = fmaxf(z, dpp<0x124>(z)); Z = fmaxf(Z, dpp<0x128>(Z));
      const float wt = ex2(z - Z);
      float L = wt + dpp<0x124>(wt); L += dpp<0x128>(L);
      const float w = wt * __builtin_amdgcn_rcpf(L);
      a[0] = fmaf(w, bflo(d[j][0]), a[0]); a[1] = fmaf(w, bfhi(d[j][0]), a[1]); a[2] = fmaf(w, bflo(d[j][2]), a[2]); a[3] = fmaf(w, bfhi(d[j][2]), a[3]);
    }
    fin(a, out + fb * 1024 + 512 + 16 * (idx & 31));
    STAMP(6);
    STAMP_FLUSH(0); TR_FLUSH();
    return;
  }
  if (cls == 0) {
    {
      const u16* pb = (const u16*)p0 + (idx * 128 + 64 + w4) * 512 + 8 * lane;
#pragma unroll
      for (int i = 0; i < 16; ++i) {
        Wb[i] = ld16nt(pb + i * 2048);
        if (i >= PACE0 && (i & 1)) asm volatile("" : "+v"(Wb[i - PACE0]));
        if (i == 9) TR();
        SCHED();
      }
    }
    if (HG) lds_add(cnt + 8, 1u, lane);
    char* scr = smem + L_SCR;
    const int ab = idx >> 5, r = lane & 31, hh = lane >> 5;
#pragma unroll
    for (int i = 0; i < 16; ++i) *reinterpret_cast<u32x4*>(smem + ((i >> 3) * 4 + r4) * 8192 + toff(4 * (i & 7) + w4, c16)) = Wa[i];
#pragma unroll
    for (int i = 0; i < 16; ++i) *reinterpret_cast<u32x4*>(smem + ((2 + (i >> 3)) * 4 + r4) * 8192 + toff(4 * (i & 7) + w4, c16)) = Wb[i];
    STAMP(8);
    TR();
    if (XK & 2) { lds_arrive(cnt + 1, lane); if (idx == 0 && w4 == 0 && lane == 0) { __builtin_amdgcn_s_sleep(127); __builtin_amdgcn_s_sleep(127); __hip_atomic_store((gu64*)(ws + WS_EP), ((u64)tg.hi << 32) | tg.lo, __ATOMIC_RELAXED, __HIP_MEMORY_SCOPE_AGENT); } STAMP_FLUSH(0); TR_FLUSH(); return; }
    u32x4 qlast = {0u, 0u, 0u, 0u};
    {
      u32x4 g[18];
      const int fl = (idx & 1) * 8;
      const unsigned o8 = WS_QA + (ab * 32 + w4 * 8) * 2048 + lane * 16, ol = WS_QA + (ab * 32 + r) * 2048 + 1984 + 32 * hh;
      unsigned pend = (XK & 128) ? 0u : 0x3ffffu;
#pragma unroll 1
      for (int spin = 0; spin < SPIN_MAX && pend; ++spin) {
        COMPILER_FENCE();
#pragma unroll
        for (int i = 0; i < 18; ++i) g[i] = ld16c(rs, (pend >> i & 1) ? (i < 16 ? o8 + (i ^ fl) * 1024 : ol + 16 * (i - 16)) : OOB);
        if (HG == 1 && spin == 0) { SCHED(); lds_add(cnt + 8, ~0u, lane); SCHED(); }
#pragma unroll
        for (int i = 0; i < 18; ++i) {
          if (HG == 2 && spin == 0 && i == 10) lds_add(cnt + 8, ~0u, lane);
          if ((pend >> i & 1) && __all((g[i][1] == tg.hi) & (g[i][3] == tg.hi))) {
            pend &= ~(1u << i);
            if (i >= 16) { qlast[2 * (i - 16)] = g[i][0]; qlast[2 * (i - 16) + 1] = g[i][2]; }
            else if (!(i & 1) || lane < 60) *reinterpret_cast<u32x2*>(scr + (w4 * 8 + ((i ^ fl) >> 1)) * 1008 + (i & 1) * 512 + 8 * lane) = u32x2{g[i][0], g[i][2]};
          }
        }
        if (pend) __builtin_amdgcn_s_sleep(8);
      }
    }
    sbar4(cnt + 3, lane);
    STAMP(10);
    f32x16 sacc;
#pragma unroll
    for (int i = 0; i < 16; ++i) sacc[i] = 0.f;
#pragma unroll
    for (int jj = 0; jj < 32; ++jj) {
      const u32x4 af = *reinterpret_cast<const u32x4*>(smem + (w4 * 4 + (jj >> 3)) * 8192 + toff(r, 2 * (jj & 7) + hh));
      const u32x4 bf = jj < 31 ? *reinterpret_cast<const u32x4*>(scr + r * 1008 + 16 * (2 * (jj < 31 ? jj : 0) + hh)) : qlast;
      sacc = __builtin_amdgcn_mfma_f32_32x32x16_bf16(__builtin_bit_cast(bf16x8_t, af), __builtin_bit_cast(bf16x8_t, bf), sacc, 0, 0, 0);
    }
    float m = sacc[0];
#pragma unroll
    for (int i = 1; i < 16; ++i) m = fmaxf(m, sacc[i]);
    m = xhalf_max(m);
    if (hh == 0) *reinterpret_cast<float*>(scr + r * 1008 + 992 + 4 * w4) = m;
    sbar4(cnt + 4, lane);
    STAMP(11);
    {
      const f32x4 m4 = *reinterpret_cast<const f32x4*>(scr + r * 1008 + 992);
      m = fmaxf(fmaxf(m4[0], m4[1]), fmaxf(m4[2], m4[3]));
    }
    const float mc = m * C2;
    float p[16], ls = 0.f;
#pragma unroll
    for (int i = 0; i < 16; ++i) { p[i] = ex2(fmaf(sacc[i], C2, -mc)); ls += p[i]; }
    ls = xhalf_sum(ls);
    sbar4(cnt + 5, lane);
    u32x4* pf = reinterpret_cast<u32x4*>(scr);
    float* wl = reinterpret_cast<float*>(scr + 8192);
    if (hh == 0) wl[r * 4 + w4] = ls;
    pf[(2 * w4) * 64 + lane] = u32x4{pk_bf(p[0], p[1]), pk_bf(p[2], p[3]), pk_bf(p[4], p[5]), pk_bf(p[6], p[7])};
    pf[(2 * w4 + 1) * 64 + lane] = u32x4{pk_bf(p[8], p[9]), pk_bf(p[10], p[11]), pk_bf(p[12], p[13]), pk_bf(p[14], p[15])};
    sbar4(cnt + 6, lane);
    u32x4 pb8[8];
#pragma unroll
    for (int ks = 0; ks < 8; ++ks) pb8[ks] = pf[ks * 64 + lane];
    const f32x4 l4 = *reinterpret_cast<const f32x4*>(wl + r * 4);
    const int qq = (lane & 15) >> 2, pp = lane & 3, blk2 = (lane >> 4) & 1;
    char* ot = scr + 12288 + w4 * 4096;
#pragma unroll
    for (int c2 = 0; c2 < 2; ++c2) {
      f32x16 oacc[2];
#pragma unroll
      for (int ct = 0; ct < 2; ++ct)
#pragma unroll
        for (int i = 0; i < 16; ++i) oacc[ct][i] = 0.f;
#pragma unroll
      for (int ks = 0; ks < 8; ++ks)
#pragma unroll
        for (int ct = 0; ct < 2; ++ct) {
          typedef __attribute__((address_space(3))) s16x4* lp;
          const char* tile = smem + ((ks >> 1) * 4 + w4) * 8192;
          const int cc = 2 * c2 + ct;
          const s16x4 lo = __builtin_amdgcn_ds_read_tr16_b64_v4i16((lp)(tile + toff(16 * (ks & 1) + 4 * hh + qq, 4 * cc + 2 * blk2 + (pp >> 1)) + 8 * (pp & 1)));
          const s16x4 hi = __builtin_amdgcn_ds_read_tr16_b64_v4i16((lp)(tile + toff(16 * (ks & 1) + 8 + 4 * hh + qq, 4 * cc + 2 * blk2 + (pp >> 1)) + 8 * (pp & 1)));
          const s16x8 afr = __builtin_shufflevector(lo, hi, 0, 1, 2, 3, 4, 5, 6, 7);
          oacc[ct] = __builtin_amdgcn_mfma_f32_32x32x16_bf16(__builtin_bit_cast(bf16x8_t, afr), __builtin_bit_cast(bf16x8_t, pb8[ks]), oacc[ct], 0, 0, 0);
        }
#pragma unroll
      for (int ct = 0; ct < 2; ++ct)
#pragma unroll
        for (int g = 0; g < 4; ++g)
          *reinterpret_cast<u32x2*>(ot + r * 128 + 16 * ((4 * ct + g) ^ (r & 7)) + 8 * hh) = u32x2{pk_bf(oacc[ct][4 * g], oacc[ct][4 * g + 1]), pk_bf(oacc[ct][4 * g + 2], oacc[ct][4 * g + 3])};
      if (c2 == 0) lds_add(cnt + 8, 1u, lane);
      __builtin_amdgcn_wave_barrier();
#pragma unroll
      for (int i = 0; i < 4; ++i) {
        const int hd = 8 * i + (lane >> 3), pc = lane & 7;
        const u32x4 vv = *reinterpret_cast<const u32x4*>(ot + hd * 128 + 16 * (pc ^ (hd & 7)));
        st16c(rs, (XK & 16) ? OOB : WS_APO + ((hd * 64 + idx) * 512 + w4 * 128 + 64 * c2 + 8 * pc) * 2, vv);
      }
      __builtin_amdgcn_wave_barrier();
    }
    lds_arrive(cnt + 1, lane);
    STAMP(1);
    asm volatile("s_waitcnt vmcnt(0)" ::: "memory");
    if (lds_arrive(cnt + 9, lane) == 3u && hh == 0)
      st16c(rs, WS_AML + (idx * 32 + r) * 16, u32x4{__float_as_uint(m), tg.hi, __float_as_uint((l4[0] + l4[1]) + (l4[2] + l4[3])), tg.hi});
    lds_add(cnt + 8, ~0u, lane);
    STAMP(2);
    if (idx == 0 && w4 == 0) {
      lds_wait(cnt + 0, 4u);
#pragma unroll 1
      for (int spin = 0; spin < SPIN_MAX; ++spin) {
        COMPILER_FENCE();
        const u32x2 t0 = ld8c(rs, WS_AML + lane * 512), t1 = ld8c(rs, WS_QX + (64 * 64 + lane * 32) * 8);
        if (__all((t0[1] == tg.hi) & (t1[1] == tg.hi))) break;
        __builtin_amdgcn_s_sleep(8);
      }
      if (lane == 0) __hip_atomic_store((gu64*)(ws + WS_EP), ((u64)tg.hi << 32) | tg.lo, __ATOMIC_RELAXED, __HIP_MEMORY_SCOPE_AGENT);
    }
    STAMP_FLUSH(0); TR_FLUSH();
    return;
  }
  if (cls == 1) {
    const int idx = idx2, h = idx >> 1, b = idx & 1;
    {
      const u16* pa = (const u16*)p1 + (w4 * 128 + 16 * (lane >> 3)) * 4096 + h * 128 + b * 64 + 8 * (lane & 7);
      const u16* pb = (const u16*)p4 + (h * 128 + b * 64 + w4 * 2) * 512 + 8 * lane;
#pragma unroll
      for (int i = 0; i < 16; ++i) {
        Wa[i] = ld16nt(pa + i * 4096);
        SCHED();
        if ((i & 1) && !((idx & 7) == 3 || idx == 63)) asm volatile("s_waitcnt vmcnt(%0)" :: "n"(2 * PACE1) : "memory");
        if ((i & 7) == 7) TR();
        SCHED();
      }
#pragma unroll
      for (int i = 0; i < 16; ++i) {
        Wb[i] = ld16nt(pb + (i >> 1) * 4096 + (i & 1) * 512);
        SCHED();
        if (i & 1) asm volatile("s_waitcnt vmcnt(%0)" :: "n"(2 * PACE1) : "memory");
        if ((i & 7) == 7) TR();
        SCHED();
      }
    }
    if (XK & 2) { pin16(Wa); pin16(Wb); STAMP_FLUSH(0); TR_FLUSH(); return; }
    float* red2 = red + 2048;
    float* wsh = red + 3072;
    if (w4 == 0) {
      const unsigned fo = WS_AML + (lane * 32 + h) * 16;
      while (wall_clock64() - tstart < TAML) __builtin_amdgcn_s_sleep(8);
      if (H1) lds_add(cnt + 8, 4u, lane);
      u32x4 g;
      bool ok = false;
#pragma unroll 1
      for (int spin = 0; spin < SPIN_MAX; ++spin) {
        COMPILER_FENCE();
        if (!ok) g = ld16c(rs, fo);
        ok = (g[1] == tg.hi) & (g[3] == tg.hi);
        if (__all(ok)) break;
        __builtin_amdgcn_s_sleep(2);
      }
      lds_add(cnt + 7, 1u, lane);
      const float mu = __uint_as_float(g[0]), lu = __uint_as_float(g[2]);
      const float M = x16_max(row16_max(mu));
      const float wl = ex2((mu - M) * C2);
      wsh[lane] = wl / x16_sum(row16_sum(wl * lu));
      lds_arrive(cnt + 7, lane);
    } else lds_wait(cnt + 7, 1u);
    COMPILER_FENCE();
    STAMP(3);
    u32x4 pp[16];
#pragma unroll
    for (int i = 0; i < 16; ++i) {
      if (i >= PG) asm volatile("" : "+v"(pp[i >= PG ? i - PG : 0]));
      pp[i] = ld16c(rs, (XK & 32) ? OOB : WS_APO + (h * 64 + w4 * 16 + (i ^ (8 * b))) * 1024 + lane * 16);
      SCHED();
    }
    if (H1) lds_add(cnt + 8, ~0u, lane);
    SCHED();
    lds_wait(cnt + 7, 2u);
    float a8[8];
#pragma unroll
    for (int k = 0; k < 8; ++k) a8[k] = 0.f;
    const float wv = wsh[w4 * 16 + (c16 ^ (8 * b))];
#pragma unroll
    for (int i = 0; i < 16; ++i) {
      const float wt = rdlane(wv, i);
#pragma unroll
      for (int k = 0; k < 4; ++k) { a8[2 * k] = fmaf(wt, bflo(pp[i][k]), a8[2 * k]); a8[2 * k + 1] = fmaf(wt, bfhi(pp[i][k]), a8[2 * k + 1]); }
      asm volatile("" : "+v"(a8[0]), "+v"(a8[1]), "+v"(a8[2]), "+v"(a8[3]), "+v"(a8[4]), "+v"(a8[5]), "+v"(a8[6]), "+v"(a8[7]));
      SCHED();
    }
    *reinterpret_cast<f32x4*>(red + w4 * 512 + 8 * lane) = f32x4{a8[0], a8[1], a8[2], a8[3]};
    *reinterpret_cast<f32x4*>(red + w4 * 512 + 8 * lane + 4) = f32x4{a8[4], a8[5], a8[6], a8[7]};
    sbar4(cnt + 3, lane);
    STAMP(4);
#pragma unroll
    for (int i = 0; i < 4; ++i) {
      const float* src = red + (i >> 1) * 1024 + w4 * 128 + 16 * (lane >> 3) + 8 * (i & 1);
      const f32x4 t0 = *reinterpret_cast<const f32x4*>(src) + *reinterpret_cast<const f32x4*>(src + 512), t1 = *reinterpret_cast<const f32x4*>(src + 4) + *reinterpret_cast<const f32x4*>(src + 516);
      xv[i] = u32x4{pk_bf(t0[0], t0[1]), pk_bf(t0[2], t0[3]), pk_bf(t1[0], t1[1]), pk_bf(t1[2], t1[3])};
    }
    gemv(red2 + w4 * 128);
    sbar4(cnt + 4, lane);
    STAMP(12);
    unsigned op;
    {
      f32x2 t = {0.f, 0.f};
#pragma unroll
      for (int w = 0; w < 4; ++w) t += *reinterpret_cast<const f32x2*>(red2 + w * 128 + (lane >> 5) * 64 + 8 * (lane & 7) + 2 * w4);
      op = pk_bf(t[0], t[1]);
    }
    float* red3 = red + 4096;
    float a[2][8];
#pragma unroll
    for (int bb = 0; bb < 2; ++bb)
#pragma unroll
      for (int k = 0; k < 8; ++k) a[bb][k] = 0.f;
#pragma unroll
    for (int ip = 0; ip < 8; ++ip) {
      const unsigned v0 = __builtin_amdgcn_readlane(op, ip), v1 = __builtin_amdgcn_readlane(op, 32 + ip);
#pragma unroll
      for (int k = 0; k < 4; ++k) {
        const unsigned lo = plo(Wb[2 * ip][k], Wb[2 * ip + 1][k]), hi = phi(Wb[2 * ip][k], Wb[2 * ip + 1][k]);
        a[0][2 * k] = dot2(lo, v0, a[0][2 * k]); a[0][2 * k + 1] = dot2(hi, v0, a[0][2 * k + 1]);
        a[1][2 * k] = dot2(lo, v1, a[1][2 * k]); a[1][2 * k + 1] = dot2(hi, v1, a[1][2 * k + 1]);
      }
    }
#pragma unroll
    for (int bb = 0; bb < 2; ++bb) {
      *reinterpret_cast<f32x4*>(red3 + (w4 * 2 + bb) * 512 + 8 * lane) = f32x4{a[bb][0], a[bb][1], a[bb][2], a[bb][3]};
      *reinterpret_cast<f32x4*>(red3 + (w4 * 2 + bb) * 512 + 8 * lane + 4) = f32x4{a[bb][4], a[bb][5], a[bb][6], a[bb][7]};
    }
    sbar4(cnt + 5, lane);
    {
      f32x4 t = {0.f, 0.f, 0.f, 0.f};
#pragma unroll
      for (int w = 0; w < 4; ++w) t += *reinterpret_cast<const f32x4*>(red3 + w * 1024 + 4 * tid);
      st16c(rs, WS_GA + idx * 4096 + tid * 16, u32x4{pk_bf(t[0], t[1]), tg.hi, pk_bf(t[2], t[3]), tg.hi});
    }
    STAMP(5);
    STAMP_FLUSH(0); TR_FLUSH();
    return;
  }
  {
    const unsigned doff = WS_GA + (w4 * 16 + (lane >> 2)) * 4096 + idx * 64 + (lane & 3) * 16;
    lds_wait(cnt + 0, 4u);
    if (TFIN) { while (wall_clock64() - tstart < TFIN) __builtin_amdgcn_s_sleep(2); }
    u32x4 d;
    bool ok = false;
#pragma unroll 1
    for (int spin = 0; spin < SPIN_MAX; ++spin) {
      COMPILER_FENCE();
      if (!ok) d = ld16c(rs, doff);
      ok = (d[1] == tg.hi) & (d[3] == tg.hi);
      if (__all(ok)) break;
      __builtin_amdgcn_s_sleep(1);
    }
    const float a[4] = {bflo(d[0]), bfhi(d[0]), bflo(d[2]), bfhi(d[2])};
    fin(a, out + (idx >> 5) * 1024 + 16 * (idx & 31));
  }
  STAMP(6);
  STAMP_FLUSH(0); TR_FLUSH();
}

extern "C" void kernel_launch(void* const* d_in, const int* in_sizes, int n_in, void* d_out, int out_size, void* d_ws, size_t ws_size, hipStream_t stream) {
  const int sel[5][3] = {{1, 3, 6}, {2, 4, 0}, {3, 2, 6}, {9, 9, 6}, {7, 8, 10}};
  Args a;
  for (int r = 0; r < 5; ++r) {
    for (int c = 0; c < 3; ++c) a.t[r][c] = (gptr)d_in[sel[r][c]];
    a.t[r][3] = (gptr)d_ws;
  }
  a.w_o = (gptr)d_in[5];
  a.out = (gptr)d_out;
  kmain<<<256, 512, 0, stream>>>(a);
}
```

```cpp
#define PROBE 0
#include <hip/hip_runtime.h>
#include <hip/hip_bf16.h>
#include <stdint.h>

#define DI __device__ __forceinline__
typedef __attribute__((ext_vector_type(4))) unsigned u32x4;
typedef __attribute__((ext_vector_type(2))) unsigned u32x2;
typedef __attribute__((ext_vector_type(4))) float f32x4;
typedef __attribute__((ext_vector_type(2))) float f32x2;
typedef __attribute__((ext_vector_type(16))) float f32x16;
typedef __attribute__((ext_vector_type(8))) __bf16 bf16x8_t;
typedef __attribute__((ext_vector_type(2))) __bf16 bf16x2_t;
typedef __attribute__((ext_vector_type(4))) short s16x4;
typedef __attribute__((ext_vector_type(8))) short s16x8;
typedef unsigned short u16;
typedef unsigned long long u64;

#define QTAB 53,53,74,76, 53,52,73,78, 53,53,74,76, 53,55,73,75, 53,53,74,76, 53,52,76,75, 53,52,76,75, 53,53,74,76
#define QTAB7 QTAB
#define AHK 1
#define AHV 1
#define WROT 1
#define WS 0
DI constexpr int rounds(int q) { return q <= 48 ? 6 : (q <= 64 ? 8 : 10); }
constexpr int QX_[64] = {QTAB, QTAB7};
constexpr u64 qpair(int r) { u64 v = 0; for (int i = 0; i < 8; ++i) v |= (u64)QX_[8 * r + i] << (8 * i); return v; }
constexpr bool qok() { bool ok = true; for (int r = 0; r < 16; ++r) { int s = 0; for (int c = 0; c < 4; ++c) { s += QX_[4 * r + c]; ok &= QX_[4 * r + c] >= 10 && QX_[4 * r + c] <= 80; } ok &= s == 256; } return ok; }
static_assert(qok(), "every key once");
#define PACE0 4
#define PACE1 3
#define ROT2 0
#define TQX 500
#define TAML 1670
#define XK 0
#define TFIN 0
#define HG 1
#define PG 16
#define H1 0
#define TE 0
#define TD 95
#define TE1 400
#define PF 1
#define BURST 1
#define TPUB 0
#define FSL 0
#define QT 1
constexpr int SK = 4096;
constexpr float C2 = 0.08838834764831845f * 1.4426950408889634f;
constexpr u64 GOLD = 0x9E3779B97F4A7C15ull;

constexpr unsigned WS_EP = 0;
constexpr unsigned WS_QX = 4096;
constexpr unsigned WS_QA = WS_QX + 2 * 2 * 32 * 64 * 8;
constexpr unsigned WS_AML = WS_QA + 2 * 32 * 256 * 8;
constexpr unsigned WS_APO = WS_AML + 64 * 32 * 16;
constexpr unsigned WS_GB = WS_APO + 32 * 64 * 512 * 2;
constexpr unsigned WS_GA = WS_GB + 2 * 128 * 128 * 16;
constexpr unsigned WS_END = WS_GA + 64 * 2 * 128 * 16;

#define STAMP(i) do {} while (0)
#define STAMP_DECL u64 pb_ = 0
#define STAMP_FLUSH(grp) do { const u64 tend = tstart + 6000 + 20 * (pb_ ? pb_ - tstart : 0); for (int sp_ = 0; sp_ < SPIN_MAX && wall_clock64() < tend; ++sp_) __builtin_amdgcn_s_sleep(2); } while (0)
#define TR_DECL do {} while (0)
#define TR() do {} while (0)
#define TR_FLUSH() do {} while (0)
DI float bflo(unsigned u) { return __uint_as_float(u << 16); }
DI float bfhi(unsigned u) { return __uint_as_float(u & 0xffff0000u); }
DI unsigned pk_bf(float a, float b) { const f32x2 v = {a, b}; return __builtin_bit_cast(unsigned, __builtin_convertvector(v, bf16x2_t)); }
DI float dot2(unsigned k, unsigned q, float acc) {
  return __builtin_amdgcn_fdot2_f32_bf16(__builtin_bit_cast(bf16x2_t, k), __builtin_bit_cast(bf16x2_t, q), acc, false);
}
DI unsigned plo(unsigned w0, unsigned w1) { return __builtin_amdgcn_perm(w1, w0, 0x05040100u); }
DI unsigned phi(unsigned w0, unsigned w1) { return __builtin_amdgcn_perm(w1, w0, 0x07060302u); }
template <int CTRL> DI float dpp(float x) {
  return __builtin_bit_cast(float, __builtin_amdgcn_mov_dpp(__builtin_bit_cast(int, x), CTRL, 0xf, 0xf, true));
}
DI float row16_sum(float x) { x += dpp<0xB1>(x); x += dpp<0x4E>(x); x += dpp<0x141>(x); x += dpp<0x128>(x); return x; }
DI float row16_max(float x) { x = fmaxf(x, dpp<0xB1>(x)); x = fmaxf(x, dpp<0x4E>(x)); x = fmaxf(x, dpp<0x141>(x)); x = fmaxf(x, dpp<0x128>(x)); return x; }
DI float x16_sum(float x) { auto s = __builtin_amdgcn_permlane16_swap(__float_as_uint(x), __float_as_uint(x), false, false); return __uint_as_float(s[0]) + __uint_as_float(s[1]); }
DI float x16_max(float x) { auto s = __builtin_amdgcn_permlane16_swap(__float_as_uint(x), __float_as_uint(x), false, false); return fmaxf(__uint_as_float(s[0]), __uint_as_float(s[1])); }
DI float xhalf_max(float x) { auto t = __builtin_amdgcn_permlane32_swap(__float_as_uint(x), __float_as_uint(x), false, false); return fmaxf(__uint_as_float(t[0]), __uint_as_float(t[1])); }
DI float xhalf_sum(float x) { auto t = __builtin_amdgcn_permlane32_swap(__float_as_uint(x), __float_as_uint(x), false, false); return __uint_as_float(t[0]) + __uint_as_float(t[1]); }
DI float xrow_max(float x) { return xhalf_max(x16_max(x)); }
DI float xrow_sum(float x) { return xhalf_sum(x16_sum(x)); }
DI u32x4 ld16(const void* p) { return *reinterpret_cast<const u32x4*>(p); }
DI u32x4 ld16nt(const void* p) { return __builtin_nontemporal_load(reinterpret_cast<const u32x4*>(p)); }
DI float ex2(float x) { return __builtin_amdgcn_exp2f(x); }
#define SCHED() __builtin_amdgcn_sched_barrier(0)

typedef __attribute__((address_space(1))) u64 gu64;
typedef __amdgpu_buffer_rsrc_t rsrc_t;
DI u32x4 ld16c(rsrc_t rs, unsigned off) { return __builtin_amdgcn_raw_buffer_load_b128(rs, (int)off, 0, 16); }
DI u32x2 ld8c(rsrc_t rs, unsigned off) { return __builtin_amdgcn_raw_buffer_load_b64(rs, (int)off, 0, 16); }
DI void st16c(rsrc_t rs, unsigned off, u32x4 v) { __builtin_amdgcn_raw_buffer_store_b128(v, rs, (int)off, 0, 16); }
DI void st8c(rsrc_t rs, unsigned off, u32x2 v) { __builtin_amdgcn_raw_buffer_store_b64(v, rs, (int)off, 0, 16); }
constexpr int SPIN_MAX = 1 << 20;
constexpr unsigned OOB = 0x7ffffff0u;
DI void rawbar() { __builtin_amdgcn_sched_barrier(0); __builtin_amdgcn_s_barrier(); __builtin_amdgcn_sched_barrier(0); }
struct Tag { unsigned hi, lo; };
#define COMPILER_FENCE() asm volatile("" ::: "memory")

template <int N> struct IC { static constexpr int v = N; };
template <int I, int N, class F> DI void sfor(F&& f) { if constexpr (I < N) { f(IC<I>{}); sfor<I + 1, N>(f); } }
typedef __attribute__((address_space(3))) unsigned lu32;
DI void lds_wait(unsigned* cnt, unsigned target) {
#pragma unroll 1
  for (int spin = 0; spin < SPIN_MAX; ++spin) {
    if (__builtin_amdgcn_readfirstlane(__hip_atomic_load((lu32*)cnt, __ATOMIC_RELAXED, __HIP_MEMORY_SCOPE_WORKGROUP)) >= target) break;
    __builtin_amdgcn_s_sleep(1);
  }
  COMPILER_FENCE();
}
DI void lds_hold(unsigned* flag) {
#pragma unroll 1
  for (int spin = 0; spin < SPIN_MAX; ++spin) {
    if (__builtin_amdgcn_readfirstlane(__hip_atomic_load((lu32*)flag, __ATOMIC_RELAXED, __HIP_MEMORY_SCOPE_WORKGROUP)) == 0u) break;
    __builtin_amdgcn_s_sleep(2);
  }
}
DI unsigned lds_arrive_nw(unsigned addr) {
  unsigned old, one = 1u;
  asm volatile("s_mov_b64 exec, 1\n\tds_add_rtn_u32 %0, %1, %2\n\ts_mov_b64 exec, -1" : "=v"(old) : "v"(addr), "v"(one) : "memory");
  return old;
}
DI unsigned lds_done(unsigned old) { asm volatile("s_waitcnt lgkmcnt(0)" : "+v"(old) :: "memory"); return __builtin_amdgcn_readfirstlane(old); }
DI unsigned lds_arrive(unsigned* cnt, int) { return lds_done(lds_arrive_nw((unsigned)(size_t)(lu32*)cnt)); }
DI void lds_add(unsigned* cnt, unsigned v, int) {
  asm volatile("s_mov_b64 exec, 1\n\tds_add_u32 %0, %1\n\ts_mov_b64 exec, -1" :: "v"((unsigned)(size_t)(lu32*)cnt), "v"(v) : "memory");
}
DI void sbar4(unsigned* cnt, int lane) { if (lds_arrive(cnt, lane) < 3u) lds_wait(cnt, 4u); }
DI float rs32(float a, float b) { auto t = __builtin_amdgcn_permlane32_swap(__float_as_uint(a), __float_as_uint(b), false, false); return __uint_as_float(t[0]) + __uint_as_float(t[1]); }
DI float rs16(float a, float b) { auto t = __builtin_amdgcn_permlane16_swap(__float_as_uint(a), __float_as_uint(b), false, false); return __uint_as_float(t[0]) + __uint_as_float(t[1]); }
template <int N> DI unsigned rowbcast(unsigned x) { return (unsigned)__builtin_amdgcn_mov_dpp((int)x, 0x150 + N, 0xf, 0xf, true); }
DI void pin2(u32x4& a, u32x4& b) { asm volatile("" : "+v"(a), "+v"(b)); }
DI void pin16(u32x4* W) {
#pragma unroll
  for (int i = 0; i < 16; i += 2) asm volatile("" : "+v"(W[i]), "+v"(W[i + 1]));
}
DI float rdlane(float x, int l) { return __builtin_bit_cast(float, __builtin_amdgcn_readlane(__builtin_bit_cast(int, x), l)); }

DI unsigned toff(unsigned row, unsigned ch) { return 256u * row + 16u * (ch ^ (((row & 3) << 2) | ((row >> 2) & 3))); }
constexpr int SMEM = 160 * 1024;
constexpr int L_SCR = 131072;
constexpr int L_MRG = 65536;
constexpr int L_CNT = L_SCR + 32 * 1008;

struct KV { u32x4 r[8]; };

typedef const __attribute__((address_space(1))) char* gptr;
struct Args { gptr t[5][4]; gptr w_o; gptr out; };

__global__ __launch_bounds__(512, 2) __attribute__((aligned(4096))) void kmain(Args a) {
  __shared__ __attribute__((aligned(16))) char smem[SMEM];
  const int tid = threadIdx.x, blk = blockIdx.x, lane = tid & 63;
  const int wave = __builtin_amdgcn_readfirstlane(tid >> 6), w4 = wave & 3;
  const int cls = (blk >> 3) & 3;
  const int arow = wave < 4 ? cls : 4;
  gptr p0 = a.t[arow][0], p1 = a.t[arow][1], p2 = a.t[arow][2], p3 = a.t[arow][3], p4 = a.w_o, p5 = a.out;
  const u64 tstart = wall_clock64();
  SCHED();
  int idx = 8 * (blk & 7) + (blk >> 5);
  if (wave < 4) idx = (idx & ~7) | ((idx + (cls == 3 ? 2 : (cls == 2 ? 5 : 0))) & 7);
  const int h = idx >> 1, b = idx & 1;
  const int idx2 = (idx & ~7) | ((idx + ROT2) & 7);
  const int r4 = lane >> 4, c16 = lane & 15;
  unsigned* cnt = reinterpret_cast<unsigned*>(smem + L_CNT);
  STAMP_DECL;
  TR_DECL;
  STAMP(0);
  Tag tg;
  if (wave >= 4) {
    if (tid - 256 < 16) cnt[tid - 256] = 0u;
    const unsigned t0s = TE ? (unsigned)tstart - (cls == 1 ? TE1 : 0) : 0u;
    int nr;
    unsigned tab;
    {
      const int row = idx & 7;
      const u64 q2 = idx < 56 ? (row < 4 ? (row < 2 ? qpair(0) : qpair(1)) : (row < 6 ? qpair(2) : qpair(3))) : (row < 4 ? (row < 2 ? qpair(4) : qpair(5)) : (row < 6 ? qpair(6) : qpair(7)));
      const unsigned q4 = (unsigned)(q2 >> (32 * (row & 1)));
      nr = rounds((q4 >> (8 * cls)) & 255);
      int j = 2 * nr - 1 - lane;
      j = lane < nr ? lane : (j > 2 ? j : (j == 2 ? 0 : j + 1));
      int n = j + (h * nr >> 5);
      n = n >= nr ? n - nr : n;
      int off = 0, wd = 0;
#pragma unroll
      for (int d = 0; d < 4; ++d) {
        const int q = (q4 >> (8 * d)) & 255, nd = rounds(q), bs = q * (nd == 6 ? 342 : (nd == 8 ? 256 : 205)) >> 11, rm = q - nd * bs;
        const int k = ((2 * n + 1) * nd + (d < cls) - 1 + nr) * (nr == 6 ? 5462 : (nr == 8 ? 4096 : 3277)) >> 16;
        off += k * bs + min(k, rm);
        wd = d == cls ? bs + (n < rm) : wd;
      }
      tab = lane < 2 * nr ? (unsigned)(4 * off + w4 * wd) << 10 | (unsigned)wd : 0u;
    }
    SCHED();
    char* ws = (char*)p3;
    const rsrc_t rs = __builtin_amdgcn_make_buffer_rsrc(ws, 0, (int)WS_END, 0x00020000);
    const char* kb = (const char*)p0 + ((size_t)(b * 32 + h) << 20);
    const char* vb = (const char*)p1 + ((size_t)(b * 32 + h) << 20);
    const unsigned lo = lane * 16u, lo2 = lo + 4096u;
    int nx = 0;
    auto nexts = [&]() {
      const unsigned e = __builtin_amdgcn_readlane(tab, nx);
      const rsrc_t r = __builtin_amdgcn_make_buffer_rsrc(const_cast<char*>((nx < nr ? kb : vb) + (e & ~1023u)), 0, (int)((e & 15u) << 10), 0x00020000);
      ++nx;
      return r;
    };
    const rsrc_t s0 = nexts();
    const int rot = WROT ? cls : 0;
    const char* wb = (const char*)p2 + h * 131072;
    unsigned wl = r4 * 8192 + w4 * 256 + c16 * 16;
    asm volatile("s_waitcnt lgkmcnt(0)" : "+v"(wl), "+v"(tab) :: "memory");
    rawbar();
    KV A, B, X;
    u32x4 W[8][4];
    u32x4 T[4][8];
    auto ldp = [&](KV& S, auto P, rsrc_t q) {
      constexpr int p = P.v;
      S.r[2 * p] = __builtin_amdgcn_raw_buffer_load_b128(q, (int)((p < 2 ? lo : lo2) + (p & 1) * 2048), 0, 2);
      S.r[2 * p + 1] = __builtin_amdgcn_raw_buffer_load_b128(q, (int)((p < 2 ? lo : lo2) + (p & 1) * 2048 + 1024), 0, 2);
    };
    auto landed = [&](KV& S, auto P) { asm volatile("" : "+v"(S.r[2 * P.v]), "+v"(S.r[2 * P.v + 1])); };
    auto ldw = [&](auto G, auto J) { W[G.v][J.v] = (XK & 4) ? u32x4{wl, wl, wl, wl} : ld16(wb + ((((G.v >> 1) + rot) & 3) * 32768 + (G.v & 1) * 4096) + wl + J.v * 1024); };
    auto conv = [&](auto G) {
      constexpr int t = G.v >> 1, j0 = (G.v & 1) * 2;
#pragma unroll
      for (int jj = 0; jj < 2; ++jj)
#pragma unroll
        for (int e = 0; e < 4; ++e) {
          const unsigned a_ = W[G.v][2 * jj][e], b_ = W[G.v][2 * jj + 1][e];
          T[t][2 * e][j0 + jj] = plo(a_, b_); T[t][2 * e + 1][j0 + jj] = phi(a_, b_);
        }
#pragma unroll
      for (int e = 0; e < 8; e += 2) pin2(T[t][e], T[t][e + 1]);
    };
    u32x2 tw;
    {
      const rsrc_t s1 = nexts(), s2 = nexts();
      sfor<0, 12>([&](auto I) {
        constexpr int i = I.v;
        if constexpr (i >= 4) { if constexpr (i < 8) landed(X, IC<i & 3>{}); else landed(B, IC<i & 3>{}); }
        if constexpr (i == 7 || i == 11) TR();
        if constexpr (i == 8) tw = ld8c(rs, lane == 0 ? WS_GB + (b * 128 + h * 4 + cls) * 2048 + 512 * w4 : OOB);
        if constexpr (i < 4) { ldp(X, IC<i & 3>{}, s0); ldw(IC<0>{}, IC<i & 3>{}); }
        else if constexpr (i < 8) { ldp(B, IC<i & 3>{}, s1); ldw(IC<1>{}, IC<i & 3>{}); if constexpr (WS) ldw(IC<2>{}, IC<i & 3>{}); }
        else { ldp(A, IC<i & 3>{}, s2); ldw(IC<2 + WS>{}, IC<i & 3>{}); if constexpr (WS) ldw(IC<4>{}, IC<i & 3>{}); }
        SCHED();
        if constexpr (i == 8) { conv(IC<0>{}); SCHED(); }
      });
    }
    lds_wait(cnt + 15, 1u);
    tg.hi = __builtin_amdgcn_readfirstlane(cnt[13]); tg.lo = __builtin_amdgcn_readfirstlane(cnt[14]);
    asm volatile("" :: "v"(tw));
    u32x4 qpk;
    {
      const unsigned qo = WS_QX + (((2 + b) * 32 + h) * 64 + 4 * c16) * 8;
      u32x4 g0 = ld16c(rs, qo), g1 = ld16c(rs, qo + 16);
      SCHED();
      conv(IC<1>{}); if constexpr (WS) conv(IC<2>{});
      SCHED();
#pragma unroll 1
      for (int spin = 0; spin < SPIN_MAX; ++spin) {
        if ((XK & 1) || __all((g0[1] == tg.hi) & (g0[3] == tg.hi) & (g1[1] == tg.hi) & (g1[3] == tg.hi))) break;
        COMPILER_FENCE();
        g0 = ld16c(rs, qo); g1 = ld16c(rs, qo + 16);
      }
      qpk = (XK & 1) ? u32x4{lo, lo, lo, lo} : u32x4{g0[0], g0[2], g1[0], g1[2]};
    }
    STAMP(7);
    const bool holds = cls == 0 || (H1 && cls == 1);
    float sc[10], acc[8];
#pragma unroll
    for (int k = 0; k < 10; ++k) sc[k] = 0.f;
    auto point = [&](KV& S, KV& N, rsrc_t sn, auto P, auto REFILL, auto AH, auto WG) {
      constexpr int ah = AH.v;
      if constexpr (REFILL.v) {
        if constexpr (P.v == 0 && ah > 0) { if (holds) lds_hold(cnt + 8); sfor<0, ah>([&](auto Q) { ldp(N, Q, sn); }); SCHED(); }
        landed(S, P);
        if constexpr (P.v + ah < 4) { if constexpr ((P.v & 1) == 0) { if (holds) lds_hold(cnt + 8); } ldp(N, IC<(P.v + ah) & 3>{}, sn); }
        if constexpr (WG.v >= 0) ldw(WG, P);
        SCHED();
      }
    };
    auto step = [&](KV& S, KV& N, auto REFILL, auto WG, auto ISV) {
      rsrc_t sn = s0;
      if constexpr (TE && REFILL.v && ISV.v) {
        const unsigned due = t0s + TE - (2 * nr - nx) * TD;
#pragma unroll 1
        while ((int)((unsigned)wall_clock64() - due) < 0) __builtin_amdgcn_s_sleep(1);
      }
      if constexpr (REFILL.v) sn = nexts();
      sfor<0, 4>([&](auto P) {
        point(S, N, sn, P, REFILL, IC<(ISV.v ? AHV : (WG.v < 0 ? AHK : 0))>{}, WG);
        if constexpr (!ISV.v) {
#pragma unroll
          for (int t = 2 * P.v; t < 2 * P.v + 2; ++t) {
            acc[t] = 0.f;
#pragma unroll
            for (int k = 0; k < 4; ++k) acc[t] = dot2(S.r[t][k], qpk[k], acc[t]);
          }
          asm volatile("" : "+v"(acc[2 * P.v]), "+v"(acc[2 * P.v + 1]));
        } else {
          const unsigned pp = rowbcast<4 * P.v>(__float_as_uint(sc[0]));
#pragma unroll
          for (int k = 0; k < 4; ++k) { acc[2 * k] = dot2(plo(S.r[2 * P.v][k], S.r[2 * P.v + 1][k]), pp, acc[2 * k]); acc[2 * k + 1] = dot2(phi(S.r[2 * P.v][k], S.r[2 * P.v + 1][k]), pp, acc[2 * k + 1]); }
          asm volatile("" : "+v"(acc[0]), "+v"(acc[1]), "+v"(acc[2]), "+v"(acc[3]), "+v"(acc[4]), "+v"(acc[5]), "+v"(acc[6]), "+v"(acc[7]));
        }
        SCHED();
      });
      if constexpr (!ISV.v) {
        float u[4], v[2];
#pragma unroll
        for (int i = 0; i < 4; ++i) u[i] = (c16 & 8 ? acc[i + 4] : acc[i]) + dpp<0x128>(c16 & 8 ? acc[i] : acc[i + 4]);
#pragma unroll
        for (int i = 0; i < 2; ++i) v[i] = (c16 & 4 ? u[i + 2] : u[i]) + dpp<0x141>(c16 & 4 ? u[i] : u[i + 2]);
        float x = (c16 & 2 ? v[1] : v[0]) + dpp<0x4E>(c16 & 2 ? v[0] : v[1]);
        x += dpp<0xB1>(x);
#pragma unroll
        for (int k = 9; k > 0; --k) sc[k] = sc[k - 1];
        sc[0] = x;
      } else {
#pragma unroll
        for (int k = 0; k < 9; ++k) sc[k] = sc[k + 1];
      }
      SCHED();
      if constexpr (REFILL.v || ISV.v) TR();
    };
    step(B, B, IC<0>{}, IC<-1>{}, IC<0>{});
    step(A, B, IC<1>{}, IC<3 + 2 * WS>{}, IC<0>{});
    step(X, X, IC<0>{}, IC<-1>{}, IC<0>{});
    conv(IC<2 + WS>{}); if constexpr (WS) conv(IC<4>{});
    sfor<3, 6>([&](auto I) {
      if constexpr (I.v & 1) step(B, A, IC<1>{}, IC<I.v + 1>{}, IC<0>{}); else step(A, B, IC<1>{}, IC<I.v + 1>{}, IC<0>{});
      conv(I);
    });
#pragma unroll 1
    for (int i = 6; i < nr; i += 2) {
      step(A, B, IC<1>{}, IC<-1>{}, IC<0>{});
      step(B, A, IC<1>{}, IC<-1>{}, IC<0>{});
    }
    float m = -INFINITY, l = 0.f;
#pragma unroll
    for (int k = 0; k < 10; ++k) { sc[k] = (unsigned)(c16 >> 1) < (__builtin_amdgcn_readlane(tab, nr + k) & 15u) ? sc[k] : -INFINITY; m = fmaxf(m, sc[k]); }
    m = row16_max(m);
    {
      const float mc = m * C2;
#pragma unroll
      for (int k = 0; k < 10; ++k) {
        const float p = ex2(fmaf(sc[k], C2, -mc));
        const unsigned pk = pk_bf(dpp<0x00>(p), dpp<0xAA>(p));
        l = dot2(pk, 0x3f803f80u, l);
        sc[k] = __uint_as_float(pk);
      }
      l += dpp<0x124>(l); l += dpp<0x128>(l);
    }
#pragma unroll
    for (int k = 0; k < 8; ++k) acc[k] = 0.f;
    SCHED();
    STAMP(8);
    unsigned yoff = (r4 & 1) ? OOB : WS_GB + (b * 128 + h * 4 + cls) * 2048 + 512 * w4 + 32 * c16 + 16 * (lane >> 5), zsh = 16 * (c16 & 1) + 8 * (lane >> 5);
    unsigned rwo = L_MRG + (w4 * 4 + r4) * 272 + ((c16 - 4 * rot) & 15) * 16, rro = L_MRG + c16 * 272 + r4 * 16, mwo = L_MRG + 16 * 272 + ((c16 & 1) * 16 + w4 * 4 + r4) * 4, mro = L_MRG + 16 * 272 + c16 * 4, m4o = L_MRG + 16 * 272 + r4 * 16;
    asm volatile("" : "+v"(zsh), "+v"(yoff), "+v"(rwo), "+v"(rro), "+v"(mwo), "+v"(mro), "+v"(m4o));
    step(A, B, IC<1>{}, IC<7>{}, IC<1>{});
    conv(IC<6>{});
#pragma unroll 1
    for (int i = 2;;) {
      step(B, A, IC<1>{}, IC<-1>{}, IC<1>{});
      step(A, B, IC<1>{}, IC<-1>{}, IC<1>{});
      i += 2;
      if (i >= nr) break;
    }
    conv(IC<7>{});
    if (cls == 0) lds_wait(cnt + 1, 4u);
    step(B, B, IC<0>{}, IC<-1>{}, IC<1>{});
    STAMP(1);
    *reinterpret_cast<u32x4*>(smem + rwo) = u32x4{pk_bf(acc[0], acc[1]), pk_bf(acc[2], acc[3]), pk_bf(acc[4], acc[5]), pk_bf(acc[6], acc[7])};
    *reinterpret_cast<float*>(smem + mwo) = (c16 & 1) ? l : m;
    {
      u32x4 af[4];
      float mr, lr;
      f32x4 m4;
      unsigned got = lds_arrive_nw(L_CNT), need = 3u;
#pragma unroll 1
      for (int spin = 0; spin < SPIN_MAX; ++spin) {
#pragma unroll
        for (int t = 0; t < 4; ++t) af[t] = *reinterpret_cast<const u32x4*>(smem + rro + t * 64);
        mr = *reinterpret_cast<const float*>(smem + mro); lr = *reinterpret_cast<const float*>(smem + mro + 64);
        m4 = *reinterpret_cast<const f32x4*>(smem + m4o);
        SCHED();
        if (lds_done(got) >= need) break;
        need = 4u;
        __builtin_amdgcn_s_sleep(1);
        asm volatile("ds_read_b32 %0, %1" : "=v"(got) : "v"((unsigned)L_CNT) : "memory");
      }
      SCHED();
      u64 tq = TPUB ? wall_clock64() : 0;
      f32x4 d[8];
#pragma unroll
      for (int e = 0; e < 8; ++e) {
        d[e] = f32x4{0.f, 0.f, 0.f, 0.f};
#pragma unroll
        for (int t = 0; t < 4; ++t) d[e] = __builtin_amdgcn_mfma_f32_16x16x32_bf16(__builtin_bit_cast(bf16x8_t, af[t]), __builtin_bit_cast(bf16x8_t, T[t][e]), d[e], 0, 0, 0);
      }
      const float M = row16_max(mr);
      const float L = row16_sum(ex2((mr - M) * C2) * lr);
      const float z = fmaf(M, C2, __builtin_amdgcn_logf(L));
      float wt[4], a[8];
#pragma unroll
      for (int i = 0; i < 4; ++i) wt[i] = ex2(fmaf(m4[i], C2, -z));
#pragma unroll
      for (int e = 0; e < 8; ++e) a[e] = fmaf(wt[3], d[e][3], fmaf(wt[2], d[e][2], fmaf(wt[1], d[e][1], wt[0] * d[e][0])));
      float v4[4];
#pragma unroll
      for (int k = 0; k < 4; ++k) v4[k] = x16_sum(rs32(a[k], a[k + 4]));
      if (TPUB) { while (tq - tstart < TPUB) { __builtin_amdgcn_s_sleep(1); tq = wall_clock64(); } }
      st16c(rs, yoff, u32x4{pk_bf(v4[0], v4[1]), tg.hi, pk_bf(v4[2], v4[3]), (tg.lo & 0xffffff00u) | ((__float_as_uint(z) >> zsh) & 0xffu)});
    }
    STAMP(2);
    STAMP_FLUSH(1); TR_FLUSH();
    return;
  }
  u32x4 Wa[16], Wb[16], xv[4];
  unsigned pf;
  u64 epw;
  {
    unsigned oa[16];
    oa[0] = (cls & 1) ? (w4 * 128 + 16 * (lane >> 3)) * 8192 + idx * 128 + 16 * (lane & 7) : (cls == 2 ? (b * 256 + w4 * 64 + 16 * r4) * 8192 + h * 256 + 16 * c16 : (idx * 128 + w4) * 1024 + 16 * lane);
#pragma unroll
    for (int i = 1; i < 16; ++i) oa[i] = oa[i - 1] + (cls ? 8192 : 4096);
    unsigned op = (((blk >> 5) * 4 + w4) * 16 + c16) * 8192 + ((idx & 56) + 3 + (cls == 0 ? -1 : (idx >= 56 ? 4 : 0)) * (lane >> 5)) * 128 + (r4 & 1) * 64;
    unsigned ox = w4 * 256 + 32 * (lane >> 3);
#pragma unroll
    for (int i = 0; i < 16; i += 8) asm volatile("" : "+v"(oa[i]), "+v"(oa[i + 1]), "+v"(oa[i + 2]), "+v"(oa[i + 3]), "+v"(oa[i + 4]), "+v"(oa[i + 5]), "+v"(oa[i + 6]), "+v"(oa[i + 7]));
    asm volatile("" : "+v"(op), "+v"(ox));
    SCHED();
    if (PROBE == 1) { asm volatile("" :: "s"(p1)); SCHED(); pb_ = wall_clock64(); SCHED(); }
    pf = *reinterpret_cast<const unsigned*>((const char*)p1 + op);
    epw = __hip_atomic_load((gu64*)p3, __ATOMIC_RELAXED, __HIP_MEMORY_SCOPE_AGENT);
#pragma unroll
    for (int i = 0; i < 4; ++i) xv[i] = ld16((const char*)p2 + ox + (i >> 1) * 1024 + 16 * (i & 1));
    SCHED();
#pragma unroll
    for (int i = 0; i < 16; ++i) { Wa[i] = ld16nt((const char*)p0 + oa[i]); SCHED(); }
  }
  char* ws = (char*)p3;
  const rsrc_t rs = __builtin_amdgcn_make_buffer_rsrc(ws, 0, (int)WS_END, 0x00020000);
  u16* out = (u16*)p5;
  auto fin = [&](const float (&a)[4], u16* dst) {
    float y = rs16(rs32(a[0], a[2]), rs32(a[1], a[3]));
    y += dpp<0x124>(y); y += dpp<0x128>(y);
    float* fr = reinterpret_cast<float*>(smem + 32768);
    if ((lane & 12) == 0) fr[w4 * 16 + 4 * (lane & 3) + r4] = y;
    COMPILER_FENCE();
    const unsigned got = lds_arrive_nw(L_CNT + 24);
    f32x2 t[4];
#pragma unroll
    for (int w = 0; w < 4; ++w) t[w] = *reinterpret_cast<const f32x2*>(fr + w * 16 + 2 * (lane & 7));
    SCHED();
    if (lds_done(got) == 3u) {
      const f32x2 r = (t[0] + t[1]) + (t[2] + t[3]);
      if (lane < 8) *reinterpret_cast<unsigned*>(dst + 2 * lane) = pk_bf(r[0], r[1]);
    }
  };
  {
    rawbar();
    const u64 t64 = (((u64)(unsigned)__builtin_amdgcn_readfirstlane((unsigned)(epw >> 32)) << 32) | (unsigned)__builtin_amdgcn_readfirstlane((unsigned)epw)) + GOLD;
    tg.hi = (unsigned)(t64 >> 32); tg.lo = (unsigned)t64;
    asm volatile("" : "+s"(tg.hi), "+s"(tg.lo) : "v"(pf) : "memory");
    if (PROBE == 2 && blk < 8) { SCHED(); pb_ = wall_clock64(); SCHED(); }
    if (PROBE == 3) { asm volatile("" : "+v"(Wa[15])); SCHED(); pb_ = wall_clock64(); SCHED(); }
    if (w4 == 0) { cnt[13] = tg.hi; cnt[14] = tg.lo; COMPILER_FENCE(); lds_add(cnt + 15, 1u, lane); }
    STAMP(13);
  }
  float* red = reinterpret_cast<float*>(smem);
  if ((XK & 2) && cls == 2) { pin16(Wa); STAMP_FLUSH(0); TR_FLUSH(); return; }
  auto gemv = [&](float* dst) {
    float acc[2][8];
#pragma unroll
    for (int bb = 0; bb < 2; ++bb)
#pragma unroll
      for (int k = 0; k < 8; ++k) acc[bb][k] = 0.f;
#pragma unroll
    for (int ip = 0; ip < 8; ++ip) {
      const unsigned xp0 = xv[ip >> 2][ip & 3], xp1 = xv[2 + (ip >> 2)][ip & 3];
#pragma unroll
      for (int k = 0; k < 4; ++k) {
        const unsigned lo = plo(Wa[2 * ip][k], Wa[2 * ip + 1][k]), hi = phi(Wa[2 * ip][k], Wa[2 * ip + 1][k]);
        acc[0][2 * k] = dot2(lo, xp0, acc[0][2 * k]); acc[0][2 * k + 1] = dot2(hi, xp0, acc[0][2 * k + 1]);
        acc[1][2 * k] = dot2(lo, xp1, acc[1][2 * k]); acc[1][2 * k + 1] = dot2(hi, xp1, acc[1][2 * k + 1]);
      }
    }
    float v8[8], v4[4];
#pragma unroll
    for (int k = 0; k < 8; ++k) v8[k] = rs32(acc[0][k], acc[1][k]);
#pragma unroll
    for (int k = 0; k < 4; ++k) { const float t = rs16(v8[k], v8[k + 4]); v4[k] = t + dpp<0x128>(t); }
    *reinterpret_cast<f32x4*>(dst + (lane >> 5) * 64 + 8 * (lane & 7) + 4 * (r4 & 1)) = f32x4{v4[0], v4[1], v4[2], v4[3]};
    COMPILER_FENCE();
  };
  if (cls & 1) {
    gemv(red + w4 * 128);
    const unsigned got = lds_arrive_nw(L_CNT + 8);
    f32x4 t4[4];
#pragma unroll
    for (int w = 0; w < 4; ++w) t4[w] = *reinterpret_cast<const f32x4*>(red + w * 128 + 4 * (lane & 31));
    SCHED();
    if (lds_done(got) == 3u) {
      const f32x4 t = (t4[0] + t4[1]) + (t4[2] + t4[3]);
      st16c(rs, lane < 32 ? WS_QX + ((((cls >> 1) * 2 + r4) * 32 + h) * 64 + b * 32 + 2 * c16) * 8 : OOB, u32x4{pk_bf(t[0], t[1]), tg.hi, pk_bf(t[2], t[3]), tg.hi});
      SCHED();
      lds_add(cnt + 2, 1u, lane);
    } else lds_wait(cnt + 2, 5u);
    STAMP(8);
  } else if (cls == 2) {
    while (wall_clock64() - tstart < TQX) __builtin_amdgcn_s_sleep(4);
    u32x4 q0, q1;
    const unsigned qo = WS_QX + (h * 64 + 4 * c16) * 8;
#pragma unroll 1
    for (int spin = 0; spin < SPIN_MAX; ++spin) {
      COMPILER_FENCE();
      const u32x4 g0 = ld16c(rs, qo), g1 = ld16c(rs, qo + 16), g2 = ld16c(rs, qo + 16384), g3 = ld16c(rs, qo + 16400);
      q0 = u32x4{g0[0], g0[2], g1[0], g1[2]}; q1 = u32x4{g2[0], g2[2], g3[0], g3[2]};
      if (__all((g0[1] == tg.hi) & (g0[3] == tg.hi) & (g1[1] == tg.hi) & (g1[3] == tg.hi) & (g2[1] == tg.hi) & (g2[3] == tg.hi) & (g3[1] == tg.hi) & (g3[3] == tg.hi))) break;
      __builtin_amdgcn_s_sleep(1);
    }
    STAMP(9);
    float v[4];
    {
      float u[16], t8[8];
#pragma unroll
      for (int i = 0; i < 16; ++i) {
        float a = 0.f, c = 0.f;
#pragma unroll
        for (int k = 0; k < 4; ++k) { a = dot2(Wa[i][k], q0[k], a); c = dot2(Wa[i][k], q1[k], c); }
        u[i] = (c16 & 4 ? c : a) + dpp<0x141>(c16 & 4 ? a : c);
        asm volatile("" : "+v"(u[i]));
      }
#pragma unroll
      for (int i = 0; i < 8; ++i) { t8[i] = (c16 & 2 ? u[i + 8] : u[i]) + dpp<0x4E>(c16 & 2 ? u[i] : u[i + 8]); asm volatile("" : "+v"(t8[i])); }
#pragma unroll
      for (int i = 0; i < 4; ++i) {
        float t = (c16 & 1 ? t8[i + 4] : t8[i]) + dpp<0xB1>(c16 & 1 ? t8[i] : t8[i + 4]);
        asm volatile("" : "+v"(t));
        v[i] = t + dpp<0x128>(t);
      }
    }
    st16c(rs, c16 < 8 ? WS_QA + ((c16 >> 2) * 32 + h) * 2048 + (b * 256 + w4 * 64 + 16 * r4 + 4 * (c16 & 3)) * 4 : OOB, u32x4{pk_bf(v[0], v[1]), tg.hi, pk_bf(v[2], v[3]), tg.hi});
    STAMP(10);
  }
  if (cls >= 2) {
    const unsigned tw = __builtin_nontemporal_load(reinterpret_cast<const unsigned*>(out + (idx >> 5) * 1024 + (cls & 1) * 512 + 16 * (idx & 31)));
    asm volatile("" :: "v"(tw));
  }
  asm volatile("" :: "v"(xv[0]), "v"(xv[1]), "v"(xv[2]), "v"(xv[3]));
  SCHED();
  if (cls == 3) {
    const int fb = idx >> 5;
    const unsigned doff = WS_GB + (fb * 128 + 32 * w4 + (lane >> 2)) * 2048 + (idx & 31) * 64 + (lane & 3) * 16;
    lds_wait(cnt + 0, 4u);
    if (TFIN) { while (wall_clock64() - tstart < TFIN) __builtin_amdgcn_s_sleep(2); }
    STAMP(3);
    u32x4 d[2];
    bool ok = false;
#pragma unroll 1
    for (int spin = 0; spin < SPIN_MAX; ++spin) {
      COMPILER_FENCE();
      if (!ok) { d[0] = ld16c(rs, doff); d[1] = ld16c(rs, doff + 32768); }
      ok = (((d[0][1] ^ tg.hi) | (d[1][1] ^ tg.hi)) | (((d[0][3] ^ tg.lo) | (d[1][3] ^ tg.lo)) >> 8)) == 0u;
      if (__all(ok)) break;
      if (FSL) __builtin_amdgcn_s_sleep(FSL);
    }
    STAMP(14);
    float a[4] = {0.f, 0.f, 0.f, 0.f};
#pragma unroll
    for (int j = 0; j < 2; ++j) {
      unsigned zb = (d[j][3] & 0xffu) << (8 * (lane & 3));
      zb |= __float_as_uint(dpp<0xB1>(__uint_as_float(zb))); zb |= __float_as_uint(dpp<0x4E>(__uint_as_float(zb)));
      const float z = __uint_as_float(zb);
      float Z = fmaxf(z, dpp<0x124>(z)); Z = fmaxf(Z, dpp<0x128>(Z));
      const float wt = ex2(z - Z);
      float L = wt + dpp<0x124>(wt); L += dpp<0x128>(L);
      const float w = wt * __builtin_amdgcn_rcpf(L);
      a[0] = fmaf(w, bflo(d[j][0]), a[0]); a[1] = fmaf(w, bfhi(d[j][0]), a[1]); a[2] = fmaf(w, bflo(d[j][2]), a[2]); a[3] = fmaf(w, bfhi(d[j][2]), a[3]);
    }
    fin(a, out + fb * 1024 + 512 + 16 * (idx & 31));
    STAMP(6);
    STAMP_FLUSH(0); TR_FLUSH();
    return;
  }
  if (cls == 0) {
    {
      const u16* pb = (const u16*)p0 + (idx * 128 + 64 + w4) * 512 + 8 * lane;
#pragma unroll
      for (int i = 0; i < 16; ++i) {
        Wb[i] = ld16nt(pb + i * 2048);
        if (i >= PACE0 && (i & 1)) asm volatile("" : "+v"(Wb[i - PACE0]));
        if (i == 9) TR();
        SCHED();
      }
    }
    if (HG) lds_add(cnt + 8, 1u, lane);
    char* scr = smem + L_SCR;
    const int ab = idx >> 5, r = lane & 31, hh = lane >> 5;
#pragma unroll
    for (int i = 0; i < 16; ++i) *reinterpret_cast<u32x4*>(smem + ((i >> 3) * 4 + r4) * 8192 + toff(4 * (i & 7) + w4, c16)) = Wa[i];
#pragma unroll
    for (int i = 0; i < 16; ++i) *reinterpret_cast<u32x4*>(smem + ((2 + (i >> 3)) * 4 + r4) * 8192 + toff(4 * (i & 7) + w4, c16)) = Wb[i];
    STAMP(8);
    TR();
    if (XK & 2) { lds_arrive(cnt + 1, lane); if (idx == 0 && w4 == 0 && lane == 0) { __builtin_amdgcn_s_sleep(127); __builtin_amdgcn_s_sleep(127); __hip_atomic_store((gu64*)(ws + WS_EP), ((u64)tg.hi << 32) | tg.lo, __ATOMIC_RELAXED, __HIP_MEMORY_SCOPE_AGENT); } STAMP_FLUSH(0); TR_FLUSH(); return; }
    u32x4 qlast = {0u, 0u, 0u, 0u};
    {
      u32x4 g[18];
      const int fl = (idx & 1) * 8;
      const unsigned o8 = WS_QA + (ab * 32 + w4 * 8) * 2048 + lane * 16, ol = WS_QA + (ab * 32 + r) * 2048 + 1984 + 32 * hh;
      unsigned pend = (XK & 128) ? 0u : 0x3ffffu;
#pragma unroll 1
      for (int spin = 0; spin < SPIN_MAX && pend; ++spin) {
        COMPILER_FENCE();
#pragma unroll
        for (int i = 0; i < 18; ++i) g[i] = ld16c(rs, (pend >> i & 1) ? (i < 16 ? o8 + (i ^ fl) * 1024 : ol + 16 * (i - 16)) : OOB);
        if (HG == 1 && spin == 0) { SCHED(); lds_add(cnt + 8, ~0u, lane); SCHED(); }
#pragma unroll
        for (int i = 0; i < 18; ++i) {
          if (HG == 2 && spin == 0 && i == 10) lds_add(cnt + 8, ~0u, lane);
          if ((pend >> i & 1) && __all((g[i][1] == tg.hi) & (g[i][3] == tg.hi))) {
            pend &= ~(1u << i);
            if (i >= 16) { qlast[2 * (i - 16)] = g[i][0]; qlast[2 * (i - 16) + 1] = g[i][2]; }
            else if (!(i & 1) || lane < 60) *reinterpret_cast<u32x2*>(scr + (w4 * 8 + ((i ^ fl) >> 1)) * 1008 + (i & 1) * 512 + 8 * lane) = u32x2{g[i][0], g[i][2]};
          }
        }
        if (pend) __builtin_amdgcn_s_sleep(8);
      }
    }
    sbar4(cnt + 3, lane);
    STAMP(10);
    f32x16 sacc;
#pragma unroll
    for (int i = 0; i < 16; ++i) sacc[i] = 0.f;
#pragma unroll
    for (int jj = 0; jj < 32; ++jj) {
      const u32x4 af = *reinterpret_cast<const u32x4*>(smem + (w4 * 4 + (jj >> 3)) * 8192 + toff(r, 2 * (jj & 7) + hh));
      const u32x4 bf = jj < 31 ? *reinterpret_cast<const u32x4*>(scr + r * 1008 + 16 * (2 * (jj < 31 ? jj : 0) + hh)) : qlast;
      sacc = __builtin_amdgcn_mfma_f32_32x32x16_bf16(__builtin_bit_cast(bf16x8_t, af), __builtin_bit_cast(bf16x8_t, bf), sacc, 0, 0, 0);
    }
    float m = sacc[0];
#pragma unroll
    for (int i = 1; i < 16; ++i) m = fmaxf(m, sacc[i]);
    m = xhalf_max(m);
    if (hh == 0) *reinterpret_cast<float*>(scr + r * 1008 + 992 + 4 * w4) = m;
    sbar4(cnt + 4, lane);
    STAMP(11);
    {
      const f32x4 m4 = *reinterpret_cast<const f32x4*>(scr + r * 1008 + 992);
      m = fmaxf(fmaxf(m4[0], m4[1]), fmaxf(m4[2], m4[3]));
    }
    const float mc = m * C2;
    float p[16], ls = 0.f;
#pragma unroll
    for (int i = 0; i < 16; ++i) { p[i] = ex2(fmaf(sacc[i], C2, -mc)); ls += p[i]; }
    ls = xhalf_sum(ls);
    sbar4(cnt + 5, lane);
    u32x4* pf = reinterpret_cast<u32x4*>(scr);
    float* wl = reinterpret_cast<float*>(scr + 8192);
    if (hh == 0) wl[r * 4 + w4] = ls;
    pf[(2 * w4) * 64 + lane] = u32x4{pk_bf(p[0], p[1]), pk_bf(p[2], p[3]), pk_bf(p[4], p[5]), pk_bf(p[6], p[7])};
    pf[(2 * w4 + 1) * 64 + lane] = u32x4{pk_bf(p[8], p[9]), pk_bf(p[10], p[11]), pk_bf(p[12], p[13]), pk_bf(p[14], p[15])};
    sbar4(cnt + 6, lane);
    u32x4 pb8[8];
#pragma unroll
    for (int ks = 0; ks < 8; ++ks) pb8[ks] = pf[ks * 64 + lane];
    const f32x4 l4 = *reinterpret_cast<const f32x4*>(wl + r * 4);
    const int qq = (lane & 15) >> 2, pp = lane & 3, blk2 = (lane >> 4) & 1;
    char* ot = scr + 12288 + w4 * 4096;
#pragma unroll
    for (int c2 = 0; c2 < 2; ++c2) {
      f32x16 oacc[2];
#pragma unroll
      for (int ct = 0; ct < 2; ++ct)
#pragma unroll
        for (int i = 0; i < 16; ++i) oacc[ct][i] = 0.f;
#pragma unroll
      for (int ks = 0; ks < 8; ++ks)
#pragma unroll
        for (int ct = 0; ct < 2; ++ct) {
          typedef __attribute__((address_space(3))) s16x4* lp;
          const char* tile = smem + ((ks >> 1) * 4 + w4) * 8192;
          const int cc = 2 * c2 + ct;
          const s16x4 lo = __builtin_amdgcn_ds_read_tr16_b64_v4i16((lp)(tile + toff(16 * (ks & 1) + 4 * hh + qq, 4 * cc + 2 * blk2 + (pp >> 1)) + 8 * (pp & 1)));
          const s16x4 hi = __builtin_amdgcn_ds_read_tr16_b64_v4i16((lp)(tile + toff(16 * (ks & 1) + 8 + 4 * hh + qq, 4 * cc + 2 * blk2 + (pp >> 1)) + 8 * (pp & 1)));
          const s16x8 afr = __builtin_shufflevector(lo, hi, 0, 1, 2, 3, 4, 5, 6, 7);
          oacc[ct] = __builtin_amdgcn_mfma_f32_32x32x16_bf16(__builtin_bit_cast(bf16x8_t, afr), __builtin_bit_cast(bf16x8_t, pb8[ks]), oacc[ct], 0, 0, 0);
        }
#pragma unroll
      for (int ct = 0; ct < 2; ++ct)
#pragma unroll
        for (int g = 0; g < 4; ++g)
          *reinterpret_cast<u32x2*>(ot + r * 128 + 16 * ((4 * ct + g) ^ (r & 7)) + 8 * hh) = u32x2{pk_bf(oacc[ct][4 * g], oacc[ct][4 * g + 1]), pk_bf(oacc[ct][4 * g + 2], oacc[ct][4 * g + 3])};
      if (c2 == 0) lds_add(cnt + 8, 1u, lane);
      __builtin_amdgcn_wave_barrier();
#pragma unroll
      for (int i = 0; i < 4; ++i) {
        const int hd = 8 * i + (lane >> 3), pc = lane & 7;
        const u32x4 vv = *reinterpret_cast<const u32x4*>(ot + hd * 128 + 16 * (pc ^ (hd & 7)));
        st16c(rs, (XK & 16) ? OOB : WS_APO + ((hd * 64 + idx) * 512 + w4 * 128 + 64 * c2 + 8 * pc) * 2, vv);
      }
      __builtin_amdgcn_wave_barrier();
    }
    lds_arrive(cnt + 1, lane);
    STAMP(1);
    asm volatile("s_waitcnt vmcnt(0)" ::: "memory");
    if (lds_arrive(cnt + 9, lane) == 3u && hh == 0)
      st16c(rs, WS_AML + (idx * 32 + r) * 16, u32x4{__float_as_uint(m), tg.hi, __float_as_uint((l4[0] + l4[1]) + (l4[2] + l4[3])), tg.hi});
    lds_add(cnt + 8, ~0u, lane);
    STAMP(2);
    if (idx == 0 && w4 == 0) {
      lds_wait(cnt + 0, 4u);
#pragma unroll 1
      for (int spin = 0; spin < SPIN_MAX; ++spin) {
        COMPILER_FENCE();
        const u32x2 t0 = ld8c(rs, WS_AML + lane * 512), t1 = ld8c(rs, WS_QX + (64 * 64 + lane * 32) * 8);
        if (__all((t0[1] == tg.hi) & (t1[1] == tg.hi))) break;
        __builtin_amdgcn_s_sleep(8);
      }
      if (lane == 0) __hip_atomic_store((gu64*)(ws + WS_EP), ((u64)tg.hi << 32) | tg.lo, __ATOMIC_RELAXED, __HIP_MEMORY_SCOPE_AGENT);
    }
    STAMP_FLUSH(0); TR_FLUSH();
    return;
  }
  if (cls == 1) {
    const int idx = idx2, h = idx >> 1, b = idx & 1;
    {
      const u16* pa = (const u16*)p1 + (w4 * 128 + 16 * (lane >> 3)) * 4096 + h * 128 + b * 64 + 8 * (lane & 7);
      const u16* pb = (const u16*)p4 + (h * 128 + b * 64 + w4 * 2) * 512 + 8 * lane;
#pragma unroll
      for (int i = 0; i < 16; ++i) {
        Wa[i] = ld16nt(pa + i * 4096);
        SCHED();
        if ((i & 1) && !((idx & 7) == 3 || idx == 63)) asm volatile("s_waitcnt vmcnt(%0)" :: "n"(2 * PACE1) : "memory");
        if ((i & 7) == 7) TR();
        SCHED();
      }
#pragma unroll
      for (int i = 0; i < 16; ++i) {
        Wb[i] = ld16nt(pb + (i >> 1) * 4096 + (i & 1) * 512);
        SCHED();
        if (i & 1) asm volatile("s_waitcnt vmcnt(%0)" :: "n"(2 * PACE1) : "memory");
        if ((i & 7) == 7) TR();
        SCHED();
      }
    }
    if (XK & 2) { pin16(Wa); pin16(Wb); STAMP_FLUSH(0); TR_FLUSH(); return; }
    float* red2 = red + 2048;
    float* wsh = red + 3072;
    if (w4 == 0) {
      const unsigned fo = WS_AML + (lane * 32 + h) * 16;
      while (wall_clock64() - tstart < TAML) __builtin_amdgcn_s_sleep(8);
      if (H1) lds_add(cnt + 8, 4u, lane);
      u32x4 g;
      bool ok = false;
#pragma unroll 1
      for (int spin = 0; spin < SPIN_MAX; ++spin) {
        COMPILER_FENCE();
        if (!ok) g = ld16c(rs, fo);
        ok = (g[1] == tg.hi) & (g[3] == tg.hi);
        if (__all(ok)) break;
        __builtin_amdgcn_s_sleep(2);
      }
      lds_add(cnt + 7, 1u, lane);
      const float mu = __uint_as_float(g[0]), lu = __uint_as_float(g[2]);
      const float M = x16_max(row16_max(mu));
      const float wl = ex2((mu - M) * C2);
      wsh[lane] = wl / x16_sum(row16_sum(wl * lu));
      lds_arrive(cnt + 7, lane);
    } else lds_wait(cnt + 7, 1u);
    COMPILER_FENCE();
    STAMP(3);
    u32x4 pp[16];
#pragma unroll
    for (int i = 0; i < 16; ++i) {
      if (i >= PG) asm volatile("" : "+v"(pp[i >= PG ? i - PG : 0]));
      pp[i] = ld16c(rs, (XK & 32) ? OOB : WS_APO + (h * 64 + w4 * 16 + (i ^ (8 * b))) * 1024 + lane * 16);
      SCHED();
    }
    if (H1) lds_add(cnt + 8, ~0u, lane);
    SCHED();
    lds_wait(cnt + 7, 2u);
    float a8[8];
#pragma unroll
    for (int k = 0; k < 8; ++k) a8[k] = 0.f;
    const float wv = wsh[w4 * 16 + (c16 ^ (8 * b))];
#pragma unroll
    for (int i = 0; i < 16; ++i) {
      const float wt = rdlane(wv, i);
#pragma unroll
      for (int k = 0; k < 4; ++k) { a8[2 * k] = fmaf(wt, bflo(pp[i][k]), a8[2 * k]); a8[2 * k + 1] = fmaf(wt, bfhi(pp[i][k]), a8[2 * k + 1]); }
      asm volatile("" : "+v"(a8[0]), "+v"(a8[1]), "+v"(a8[2]), "+v"(a8[3]), "+v"(a8[4]), "+v"(a8[5]), "+v"(a8[6]), "+v"(a8[7]));
      SCHED();
    }
    *reinterpret_cast<f32x4*>(red + w4 * 512 + 8 * lane) = f32x4{a8[0], a8[1], a8[2], a8[3]};
    *reinterpret_cast<f32x4*>(red + w4 * 512 + 8 * lane + 4) = f32x4{a8[4], a8[5], a8[6], a8[7]};
    sbar4(cnt + 3, lane);
    STAMP(4);
#pragma unroll
    for (int i = 0; i < 4; ++i) {
      const float* src = red + (i >> 1) * 1024 + w4 * 128 + 16 * (lane >> 3) + 8 * (i & 1);
      const f32x4 t0 = *reinterpret_cast<const f32x4*>(src) + *reinterpret_cast<const f32x4*>(src + 512), t1 = *reinterpret_cast<const f32x4*>(src + 4) + *reinterpret_cast<const f32x4*>(src + 516);
      xv[i] = u32x4{pk_bf(t0[0], t0[1]), pk_bf(t0[2], t0[3]), pk_bf(t1[0], t1[1]), pk_bf(t1[2], t1[3])};
    }
    gemv(red2 + w4 * 128);
    sbar4(cnt + 4, lane);
    STAMP(12);
    unsigned op;
    {
      f32x2 t = {0.f, 0.f};
#pragma unroll
      for (int w = 0; w < 4; ++w) t += *reinterpret_cast<const f32x2*>(red2 + w * 128 + (lane >> 5) * 64 + 8 * (lane & 7) + 2 * w4);
      op = pk_bf(t[0], t[1]);
    }
    float* red3 = red + 4096;
    float a[2][8];
#pragma unroll
    for (int bb = 0; bb < 2; ++bb)
#pragma unroll
      for (int k = 0; k < 8; ++k) a[bb][k] = 0.f;
#pragma unroll
    for (int ip = 0; ip < 8; ++ip) {
      const unsigned v0 = __builtin_amdgcn_readlane(op, ip), v1 = __builtin_amdgcn_readlane(op, 32 + ip);
#pragma unroll
      for (int k = 0; k < 4; ++k) {
        const unsigned lo = plo(Wb[2 * ip][k], Wb[2 * ip + 1][k]), hi = phi(Wb[2 * ip][k], Wb[2 * ip + 1][k]);
        a[0][2 * k] = dot2(lo, v0, a[0][2 * k]); a[0][2 * k + 1] = dot2(hi, v0, a[0][2 * k + 1]);
        a[1][2 * k] = dot2(lo, v1, a[1][2 * k]); a[1][2 * k + 1] = dot2(hi, v1, a[1][2 * k + 1]);
      }
    }
#pragma unroll
    for (int bb = 0; bb < 2; ++bb) {
      *reinterpret_cast<f32x4*>(red3 + (w4 * 2 + bb) * 512 + 8 * lane) = f32x4{a[bb][0], a[bb][1], a[bb][2], a[bb][3]};
      *reinterpret_cast<f32x4*>(red3 + (w4 * 2 + bb) * 512 + 8 * lane + 4) = f32x4{a[bb][4], a[bb][5], a[bb][6], a[bb][7]};
    }
    sbar4(cnt + 5, lane);
    {
      f32x4 t = {0.f, 0.f, 0.f, 0.f};
#pragma unroll
      for (int w = 0; w < 4; ++w) t += *reinterpret_cast<const f32x4*>(red3 + w * 1024 + 4 * tid);
      st16c(rs, WS_GA + idx * 4096 + tid * 16, u32x4{pk_bf(t[0], t[1]), tg.hi, pk_bf(t[2], t[3]), tg.hi});
    }
    STAMP(5);
    STAMP_FLUSH(0); TR_FLUSH();
    return;
  }
  {
    const unsigned doff = WS_GA + (w4 * 16 + (lane >> 2)) * 4096 + idx * 64 + (lane & 3) * 16;
    lds_wait(cnt + 0, 4u);
    if (TFIN) { while (wall_clock64() - tstart < TFIN) __builtin_amdgcn_s_sleep(2); }
    u32x4 d;
    bool ok = false;
#pragma unroll 1
    for (int spin = 0; spin < SPIN_MAX; ++spin) {
      COMPILER_FENCE();
      if (!ok) d = ld16c(rs, doff);
      ok = (d[1] == tg.hi) & (d[3] == tg.hi);
      if (__all(ok)) break;
      __builtin_amdgcn_s_sleep(1);
    }
    const float a[4] = {bflo(d[0]), bfhi(d[0]), bflo(d[2]), bfhi(d[2])};
    fin(a, out + (idx >> 5) * 1024 + 16 * (idx & 31));
  }
  STAMP(6);
  STAMP_FLUSH(0); TR_FLUSH();
}

extern "C" void kernel_launch(void* const* d_in, const int* in_sizes, int n_in, void* d_out, int out_size, void* d_ws, size_t ws_size, hipStream_t stream) {
  const int sel[5][3] = {{1, 3, 6}, {2, 4, 0}, {3, 2, 6}, {9, 9, 6}, {7, 8, 10}};
  Args a;
  for (int r = 0; r < 5; ++r) {
    for (int c = 0; c < 3; ++c) a.t[r][c] = (gptr)d_in[sel[r][c]];
    a.t[r][3] = (gptr)d_ws;
  }
  a.w_o = (gptr)d_in[5];
  a.out = (gptr)d_out;
  kmain<<<256, 512, 0, stream>>>(a);
}
```

```cpp
#include <hip/hip_runtime.h>
#include <hip/hip_bf16.h>
#include <stdint.h>

#define DI __device__ __forceinline__
typedef __attribute__((ext_vector_type(4))) unsigned u32x4;
typedef __attribute__((ext_vector_type(2))) unsigned u32x2;
typedef __attribute__((ext_vector_type(4))) float f32x4;
typedef __attribute__((ext_vector_type(2))) float f32x2;
typedef __attribute__((ext_vector_type(16))) float f32x16;
typedef __attribute__((ext_vector_type(8))) __bf16 bf16x8_t;
typedef __attribute__((ext_vector_type(2))) __bf16 bf16x2_t;
typedef __attribute__((ext_vector_type(4))) short s16x4;
typedef __attribute__((ext_vector_type(8))) short s16x8;
typedef unsigned short u16;
typedef unsigned long long u64;

#define QTAB 53,53,74,76, 53,52,73,78, 53,53,74,76, 53,55,73,75, 53,53,74,76, 53,52,76,75, 53,52,76,75, 53,53,74,76
#define QTAB7 QTAB
#define AHK 1
#define AHV 1
#define WROT 1
#define WS 0
DI constexpr int rounds(int q) { return q <= 48 ? 6 : (q <= 64 ? 8 : 10); }
constexpr int QX_[64] = {QTAB, QTAB7};
constexpr u64 qpair(int r) { u64 v = 0; for (int i = 0; i < 8; ++i) v |= (u64)QX_[8 * r + i] << (8 * i); return v; }
constexpr bool qok() { bool ok = true; for (int r = 0; r < 16; ++r) { int s = 0; for (int c = 0; c < 4; ++c) { s += QX_[4 * r + c]; ok &= QX_[4 * r + c] >= 10 && QX_[4 * r + c] <= 80; } ok &= s == 256; } return ok; }
static_assert(qok(), "every key once");
#define PACE0 4
#define PACE1 3
#define ROT2 0
#define TQX 500
#define TAML 1670
#define XK 0
#define TFIN 0
#define HG 1
#define PG 16
#define H1 0
#define TE 0
#define TD 95
#define TE1 400
#define PF 1
#define BURST 1
#define TPUB 0
#define FSL 0
#define QT 1
constexpr int SK = 4096;
constexpr float C2 = 0.08838834764831845f * 1.4426950408889634f;
constexpr u64 GOLD = 0x9E3779B97F4A7C15ull;

constexpr unsigned WS_EP = 0;
constexpr unsigned WS_QX = 4096;
constexpr unsigned WS_QA = WS_QX + 2 * 2 * 32 * 64 * 8;
constexpr unsigned WS_AML = WS_QA + 2 * 32 * 256 * 8;
constexpr unsigned WS_APO = WS_AML + 64 * 32 * 16;
constexpr unsigned WS_GB = WS_APO + 32 * 64 * 512 * 2;
constexpr unsigned WS_GA = WS_GB + 2 * 128 * 128 * 16;
constexpr unsigned WS_END = WS_GA + 64 * 2 * 128 * 16;

#define STAMP(i) do { if ((i) == 13 && (wave < 4) == 1) { SCHED(); pb_ = wall_clock64(); SCHED(); } } while (0)
#define STAMP_DECL u64 pb_ = 0
#define STAMP_FLUSH(grp) do { const u64 tend = (pb_ ? pb_ : tstart) + 6000; for (int sp_ = 0; sp_ < SPIN_MAX && wall_clock64() < tend; ++sp_) __builtin_amdgcn_s_sleep(2); } while (0)
#define TR_DECL do {} while (0)
#define TR() do {} while (0)
#define TR_FLUSH() do {} while (0)
DI float bflo(unsigned u) { return __uint_as_float(u << 16); }
DI float bfhi(unsigned u) { return __uint_as_float(u & 0xffff0000u); }
DI unsigned pk_bf(float a, float b) { const f32x2 v = {a, b}; return __builtin_bit_cast(unsigned, __builtin_convertvector(v, bf16x2_t)); }
DI float dot2(unsigned k, unsigned q, float acc) {
  return __builtin_amdgcn_fdot2_f32_bf16(__builtin_bit_cast(bf16x2_t, k), __builtin_bit_cast(bf16x2_t, q), acc, false);
}
DI unsigned plo(unsigned w0, unsigned w1) { return __builtin_amdgcn_perm(w1, w0, 0x05040100u); }
DI unsigned phi(unsigned w0, unsigned w1) { return __builtin_amdgcn_perm(w1, w0, 0x07060302u); }
template <int CTRL> DI float dpp(float x) {
  return __builtin_bit_cast(float, __builtin_amdgcn_mov_dpp(__builtin_bit_cast(int, x), CTRL, 0xf, 0xf, true));
}
DI float row16_sum(float x) { x += dpp<0xB1>(x); x += dpp<0x4E>(x); x += dpp<0x141>(x); x += dpp<0x128>(x); return x; }
DI float row16_max(float x) { x = fmaxf(x, dpp<0xB1>(x)); x = fmaxf(x, dpp<0x4E>(x)); x = fmaxf(x, dpp<0x141>(x)); x = fmaxf(x, dpp<0x128>(x)); return x; }
DI float x16_sum(float x) { auto s = __builtin_amdgcn_permlane16_swap(__float_as_uint(x), __float_as_uint(x), false, false); return __uint_as_float(s[0]) + __uint_as_float(s[1]); }
DI float x16_max(float x) { auto s = __builtin_amdgcn_permlane16_swap(__float_as_uint(x), __float_as_uint(x), false, false); return fmaxf(__uint_as_float(s[0]), __uint_as_float(s[1])); }
DI float xhalf_max(float x) { auto t = __builtin_amdgcn_permlane32_swap(__float_as_uint(x), __float_as_uint(x), false, false); return fmaxf(__uint_as_float(t[0]), __uint_as_float(t[1])); }
DI float xhalf_sum(float x) { auto t = __builtin_amdgcn_permlane32_swap(__float_as_uint(x), __float_as_uint(x), false, false); return __uint_as_float(t[0]) + __uint_as_float(t[1]); }
DI float xrow_max(float x) { return xhalf_max(x16_max(x)); }
DI float xrow_sum(float x) { return xhalf_sum(x16_sum(x)); }
DI u32x4 ld16(const void* p) { return *reinterpret_cast<const u32x4*>(p); }
DI u32x4 ld16nt(const void* p) { return __builtin_nontemporal_load(reinterpret_cast<const u32x4*>(p)); }
DI float ex2(float x) { return __builtin_amdgcn_exp2f(x); }
#define SCHED() __builtin_amdgcn_sched_barrier(0)

typedef __attribute__((address_space(1))) u64 gu64;
typedef __amdgpu_buffer_rsrc_t rsrc_t;
DI u32x4 ld16c(rsrc_t rs, unsigned off) { return __builtin_amdgcn_raw_buffer_load_b128(rs, (int)off, 0, 16); }
DI u32x2 ld8c(rsrc_t rs, unsigned off) { return __builtin_amdgcn_raw_buffer_load_b64(rs, (int)off, 0, 16); }
DI void st16c(rsrc_t rs, unsigned off, u32x4 v) { __builtin_amdgcn_raw_buffer_store_b128(v, rs, (int)off, 0, 16); }
DI void st8c(rsrc_t rs, unsigned off, u32x2 v) { __builtin_amdgcn_raw_buffer_store_b64(v, rs, (int)off, 0, 16); }
constexpr int SPIN_MAX = 1 << 20;
constexpr unsigned OOB = 0x7ffffff0u;
DI void rawbar() { __builtin_amdgcn_sched_barrier(0); __builtin_amdgcn_s_barrier(); __builtin_amdgcn_sched_barrier(0); }
struct Tag { unsigned hi, lo; };
#define COMPILER_FENCE() asm volatile("" ::: "memory")

template <int N> struct IC { static constexpr int v = N; };
template <int I, int N, class F> DI void sfor(F&& f) { if constexpr (I < N) { f(IC<I>{}); sfor<I + 1, N>(f); } }
typedef __attribute__((address_space(3))) unsigned lu32;
DI void lds_wait(unsigned* cnt, unsigned target) {
#pragma unroll 1
  for (int spin = 0; spin < SPIN_MAX; ++spin) {
    if (__builtin_amdgcn_readfirstlane(__hip_atomic_load((lu32*)cnt, __ATOMIC_RELAXED, __HIP_MEMORY_SCOPE_WORKGROUP)) >= target) break;
    __builtin_amdgcn_s_sleep(1);
  }
  COMPILER_FENCE();
}
DI void lds_hold(unsigned* flag) {
#pragma unroll 1
  for (int spin = 0; spin < SPIN_MAX; ++spin) {
    if (__builtin_amdgcn_readfirstlane(__hip_atomic_load((lu32*)flag, __ATOMIC_RELAXED, __HIP_MEMORY_SCOPE_WORKGROUP)) == 0u) break;
    __builtin_amdgcn_s_sleep(2);
  }
}
DI unsigned lds_arrive_nw(unsigned addr) {
  unsigned old, one = 1u;
  asm volatile("s_mov_b64 exec, 1\n\tds_add_rtn_u32 %0, %1, %2\n\ts_mov_b64 exec, -1" : "=v"(old) : "v"(addr), "v"(one) : "memory");
  return old;
}
DI unsigned lds_done(unsigned old) { asm volatile("s_waitcnt lgkmcnt(0)" : "+v"(old) :: "memory"); return __builtin_amdgcn_readfirstlane(old); }
DI unsigned lds_arrive(unsigned* cnt, int) { return lds_done(lds_arrive_nw((unsigned)(size_t)(lu32*)cnt)); }
DI void lds_add(unsigned* cnt, unsigned v, int) {
  asm volatile("s_mov_b64 exec, 1\n\tds_add_u32 %0, %1\n\ts_mov_b64 exec, -1" :: "v"((unsigned)(size_t)(lu32*)cnt), "v"(v) : "memory");
}
DI void sbar4(unsigned* cnt, int lane) { if (lds_arrive(cnt, lane) < 3u) lds_wait(cnt, 4u); }
DI float rs32(float a, float b) { auto t = __builtin_amdgcn_permlane32_swap(__float_as_uint(a), __float_as_uint(b), false, false); return __uint_as_float(t[0]) + __uint_as_float(t[1]); }
DI float rs16(float a, float b) { auto t = __builtin_amdgcn_permlane16_swap(__float_as_uint(a), __float_as_uint(b), false, false); return __uint_as_float(t[0]) + __uint_as_float(t[1]); }
template <int N> DI unsigned rowbcast(unsigned x) { return (unsigned)__builtin_amdgcn_mov_dpp((int)x, 0x150 + N, 0xf, 0xf, true); }
DI void pin2(u32x4& a, u32x4& b) { asm volatile("" : "+v"(a), "+v"(b)); }
DI void pin16(u32x4* W) {
#pragma unroll
  for (int i = 0; i < 16; i += 2) asm volatile("" : "+v"(W[i]), "+v"(W[i + 1]));
}
DI float rdlane(float x, int l) { return __builtin_bit_cast(float, __builtin_amdgcn_readlane(__builtin_bit_cast(int, x), l)); }

DI unsigned toff(unsigned row, unsigned ch) { return 256u * row + 16u * (ch ^ (((row & 3) << 2) | ((row >> 2) & 3))); }
constexpr int SMEM = 160 * 1024;
constexpr int L_SCR = 131072;
constexpr int L_MRG = 65536;
constexpr int L_CNT = L_SCR + 32 * 1008;

struct KV { u32x4 r[8]; };

typedef const __attribute__((address_space(1))) char* gptr;
struct Args { gptr t[5][4]; gptr w_o; gptr out; };

__global__ __launch_bounds__(512, 2) __attribute__((aligned(4096))) void kmain(Args a) {
  __shared__ __attribute__((aligned(16))) char smem[SMEM];
  const int tid = threadIdx.x, blk = blockIdx.x, lane = tid & 63;
  const int wave = __builtin_amdgcn_readfirstlane(tid >> 6), w4 = wave & 3;
  const int cls = (blk >> 3) & 3;
  const int arow = wave < 4 ? cls : 4;
  gptr p0 = a.t[arow][0], p1 = a.t[arow][1], p2 = a.t[arow][2], p3 = a.t[arow][3], p4 = a.w_o, p5 = a.out;
  const u64 tstart = wall_clock64();
  SCHED();
  int idx = 8 * (blk & 7) + (blk >> 5);
  if (wave < 4) idx = (idx & ~7) | ((idx + (cls == 3 ? 2 : (cls == 2 ? 5 : 0))) & 7);
  const int h = idx >> 1, b = idx & 1;
  const int idx2 = (idx & ~7) | ((idx + ROT2) & 7);
  const int r4 = lane >> 4, c16 = lane & 15;
  unsigned* cnt = reinterpret_cast<unsigned*>(smem + L_CNT);
  STAMP_DECL;
  TR_DECL;
  STAMP(0);
  Tag tg;
  if (wave >= 4) {
    if (tid - 256 < 16) cnt[tid - 256] = 0u;
    const unsigned t0s = TE ? (unsigned)tstart - (cls == 1 ? TE1 : 0) : 0u;
    int nr;
    unsigned tab;
    {
      const int row = idx & 7;
      const u64 q2 = idx < 56 ? (row < 4 ? (row < 2 ? qpair(0) : qpair(1)) : (row < 6 ? qpair(2) : qpair(3))) : (row < 4 ? (row < 2 ? qpair(4) : qpair(5)) : (row < 6 ? qpair(6) : qpair(7)));
      const unsigned q4 = (unsigned)(q2 >> (32 * (row & 1)));
      nr = rounds((q4 >> (8 * cls)) & 255);
      int j = 2 * nr - 1 - lane;
      j = lane < nr ? lane : (j > 2 ? j : (j == 2 ? 0 : j + 1));
      int n = j + (h * nr >> 5);
      n = n >= nr ? n - nr : n;
      int off = 0, wd = 0;
#pragma unroll
      for (int d = 0; d < 4; ++d) {
        const int q = (q4 >> (8 * d)) & 255, nd = rounds(q), bs = q * (nd == 6 ? 342 : (nd == 8 ? 256 : 205)) >> 11, rm = q - nd * bs;
        const int k = ((2 * n + 1) * nd + (d < cls) - 1 + nr) * (nr == 6 ? 5462 : (nr == 8 ? 4096 : 3277)) >> 16;
        off += k * bs + min(k, rm);
        wd = d == cls ? bs + (n < rm) : wd;
      }
      tab = lane < 2 * nr ? (unsigned)(4 * off + w4 * wd) << 10 | (unsigned)wd : 0u;
    }
    SCHED();
    char* ws = (char*)p3;
    const rsrc_t rs = __builtin_amdgcn_make_buffer_rsrc(ws, 0, (int)WS_END, 0x00020000);
    const char* kb = (const char*)p0 + ((size_t)(b * 32 + h) << 20);
    const char* vb = (const char*)p1 + ((size_t)(b * 32 + h) << 20);
    const unsigned lo = lane * 16u, lo2 = lo + 4096u;
    int nx = 0;
    auto nexts = [&]() {
      const unsigned e = __builtin_amdgcn_readlane(tab, nx);
      const rsrc_t r = __builtin_amdgcn_make_buffer_rsrc(const_cast<char*>((nx < nr ? kb : vb) + (e & ~1023u)), 0, (int)((e & 15u) << 10), 0x00020000);
      ++nx;
      return r;
    };
    const rsrc_t s0 = nexts();
    const int rot = WROT ? cls : 0;
    const char* wb = (const char*)p2 + h * 131072;
    unsigned wl = r4 * 8192 + w4 * 256 + c16 * 16;
    asm volatile("s_waitcnt lgkmcnt(0)" : "+v"(wl), "+v"(tab) :: "memory");
    rawbar();
    KV A, B, X;
    u32x4 W[8][4];
    u32x4 T[4][8];
    auto ldp = [&](KV& S, auto P, rsrc_t q) {
      constexpr int p = P.v;
      S.r[2 * p] = __builtin_amdgcn_raw_buffer_load_b128(q, (int)((p < 2 ? lo : lo2) + (p & 1) * 2048), 0, 2);
      S.r[2 * p + 1] = __builtin_amdgcn_raw_buffer_load_b128(q, (int)((p < 2 ? lo : lo2) + (p & 1) * 2048 + 1024), 0, 2);
    };
    auto landed = [&](KV& S, auto P) { asm volatile("" : "+v"(S.r[2 * P.v]), "+v"(S.r[2 * P.v + 1])); };
    auto ldw = [&](auto G, auto J) { W[G.v][J.v] = (XK & 4) ? u32x4{wl, wl, wl, wl} : ld16(wb + ((((G.v >> 1) + rot) & 3) * 32768 + (G.v & 1) * 4096) + wl + J.v * 1024); };
    auto conv = [&](auto G) {
      constexpr int t = G.v >> 1, j0 = (G.v & 1) * 2;
#pragma unroll
      for (int jj = 0; jj < 2; ++jj)
#pragma unroll
        for (int e = 0; e < 4; ++e) {
          const unsigned a_ = W[G.v][2 * jj][e], b_ = W[G.v][2 * jj + 1][e];
          T[t][2 * e][j0 + jj] = plo(a_, b_); T[t][2 * e + 1][j0 + jj] = phi(a_, b_);
        }
#pragma unroll
      for (int e = 0; e < 8; e += 2) pin2(T[t][e], T[t][e + 1]);
    };
    u32x2 tw;
    {
      const rsrc_t s1 = nexts(), s2 = nexts();
      sfor<0, 12>([&](auto I) {
        constexpr int i = I.v;
        if constexpr (i >= 4) { if constexpr (i < 8) landed(X, IC<i & 3>{}); else landed(B, IC<i & 3>{}); }
        if constexpr (i == 7 || i == 11) TR();
        if constexpr (i == 8) tw = ld8c(rs, lane == 0 ? WS_GB + (b * 128 + h * 4 + cls) * 2048 + 512 * w4 : OOB);
        if constexpr (i < 4) { ldp(X, IC<i & 3>{}, s0); ldw(IC<0>{}, IC<i & 3>{}); }
        else if constexpr (i < 8) { ldp(B, IC<i & 3>{}, s1); ldw(IC<1>{}, IC<i & 3>{}); if constexpr (WS) ldw(IC<2>{}, IC<i & 3>{}); }
        else { ldp(A, IC<i & 3>{}, s2); ldw(IC<2 + WS>{}, IC<i & 3>{}); if constexpr (WS) ldw(IC<4>{}, IC<i & 3>{}); }
        SCHED();
        if constexpr (i == 8) { conv(IC<0>{}); SCHED(); }
      });
    }
    lds_wait(cnt + 15, 1u);
    tg.hi = __builtin_amdgcn_readfirstlane(cnt[13]); tg.lo = __builtin_amdgcn_readfirstlane(cnt[14]);
    asm volatile("" :: "v"(tw));
    u32x4 qpk;
    {
      const unsigned qo = WS_QX + (((2 + b) * 32 + h) * 64 + 4 * c16) * 8;
      u32x4 g0 = ld16c(rs, qo), g1 = ld16c(rs, qo + 16);
      SCHED();
      conv(IC<1>{}); if constexpr (WS) conv(IC<2>{});
      SCHED();
#pragma unroll 1
      for (int spin = 0; spin < SPIN_MAX; ++spin) {
        if ((XK & 1) || __all((g0[1] == tg.hi) & (g0[3] == tg.hi) & (g1[1] == tg.hi) & (g1[3] == tg.hi))) break;
        COMPILER_FENCE();
        g0 = ld16c(rs, qo); g1 = ld16c(rs, qo + 16);
      }
      qpk = (XK & 1) ? u32x4{lo, lo, lo, lo} : u32x4{g0[0], g0[2], g1[0], g1[2]};
    }
    STAMP(7);
    const bool holds = cls == 0 || (H1 && cls == 1);
    float sc[10], acc[8];
#pragma unroll
    for (int k = 0; k < 10; ++k) sc[k] = 0.f;
    auto point = [&](KV& S, KV& N, rsrc_t sn, auto P, auto REFILL, auto AH, auto WG) {
      constexpr int ah = AH.v;
      if constexpr (REFILL.v) {
        if constexpr (P.v == 0 && ah > 0) { if (holds) lds_hold(cnt + 8); sfor<0, ah>([&](auto Q) { ldp(N, Q, sn); }); SCHED(); }
        landed(S, P);
        if constexpr (P.v + ah < 4) { if constexpr ((P.v & 1) == 0) { if (holds) lds_hold(cnt + 8); } ldp(N, IC<(P.v + ah) & 3>{}, sn); }
        if constexpr (WG.v >= 0) ldw(WG, P);
        SCHED();
      }
    };
    auto step = [&](KV& S, KV& N, auto REFILL, auto WG, auto ISV) {
      rsrc_t sn = s0;
      if constexpr (TE && REFILL.v && ISV.v) {
        const unsigned due = t0s + TE - (2 * nr - nx) * TD;
#pragma unroll 1
        while ((int)((unsigned)wall_clock64() - due) < 0) __builtin_amdgcn_s_sleep(1);
      }
      if constexpr (REFILL.v) sn = nexts();
      sfor<0, 4>([&](auto P) {
        point(S, N, sn, P, REFILL, IC<(ISV.v ? AHV : (WG.v < 0 ? AHK : 0))>{}, WG);
        if constexpr (!ISV.v) {
#pragma unroll
          for (int t = 2 * P.v; t < 2 * P.v + 2; ++t) {
            acc[t] = 0.f;
#pragma unroll
            for (int k = 0; k < 4; ++k) acc[t] = dot2(S.r[t][k], qpk[k], acc[t]);
          }
          asm volatile("" : "+v"(acc[2 * P.v]), "+v"(acc[2 * P.v + 1]));
        } else {
          const unsigned pp = rowbcast<4 * P.v>(__float_as_uint(sc[0]));
#pragma unroll
          for (int k = 0; k < 4; ++k) { acc[2 * k] = dot2(plo(S.r[2 * P.v][k], S.r[2 * P.v + 1][k]), pp, acc[2 * k]); acc[2 * k + 1] = dot2(phi(S.r[2 * P.v][k], S.r[2 * P.v + 1][k]), pp, acc[2 * k + 1]); }
          asm volatile("" : "+v"(acc[0]), "+v"(acc[1]), "+v"(acc[2]), "+v"(acc[3]), "+v"(acc[4]), "+v"(acc[5]), "+v"(acc[6]), "+v"(acc[7]));
        }
        SCHED();
      });
      if constexpr (!ISV.v) {
        float u[4], v[2];
#pragma unroll
        for (int i = 0; i < 4; ++i) u[i] = (c16 & 8 ? acc[i + 4] : acc[i]) + dpp<0x128>(c16 & 8 ? acc[i] : acc[i + 4]);
#pragma unroll
        for (int i = 0; i < 2; ++i) v[i] = (c16 & 4 ? u[i + 2] : u[i]) + dpp<0x141>(c16 & 4 ? u[i] : u[i + 2]);
        float x = (c16 & 2 ? v[1] : v[0]) + dpp<0x4E>(c16 & 2 ? v[0] : v[1]);
        x += dpp<0xB1>(x);
#pragma unroll
        for (int k = 9; k > 0; --k) sc[k] = sc[k - 1];
        sc[0] = x;
      } else {
#pragma unroll
        for (int k = 0; k < 9; ++k) sc[k] = sc[k + 1];
      }
      SCHED();
      if constexpr (REFILL.v || ISV.v) TR();
    };
    step(B, B, IC<0>{}, IC<-1>{}, IC<0>{});
    step(A, B, IC<1>{}, IC<3 + 2 * WS>{}, IC<0>{});
    step(X, X, IC<0>{}, IC<-1>{}, IC<0>{});
    conv(IC<2 + WS>{}); if constexpr (WS) conv(IC<4>{});
    sfor<3, 6>([&](auto I) {
      if constexpr (I.v & 1) step(B, A, IC<1>{}, IC<I.v + 1>{}, IC<0>{}); else step(A, B, IC<1>{}, IC<I.v + 1>{}, IC<0>{});
      conv(I);
    });
#pragma unroll 1
    for (int i = 6; i < nr; i += 2) {
      step(A, B, IC<1>{}, IC<-1>{}, IC<0>{});
      step(B, A, IC<1>{}, IC<-1>{}, IC<0>{});
    }
    float m = -INFINITY, l = 0.f;
#pragma unroll
    for (int k = 0; k < 10; ++k) { sc[k] = (unsigned)(c16 >> 1) < (__builtin_amdgcn_readlane(tab, nr + k) & 15u) ? sc[k] : -INFINITY; m = fmaxf(m, sc[k]); }
    m = row16_max(m);
    {
      const float mc = m * C2;
#pragma unroll
      for (int k = 0; k < 10; ++k) {
        const float p = ex2(fmaf(sc[k], C2, -mc));
        const unsigned pk = pk_bf(dpp<0x00>(p), dpp<0xAA>(p));
        l = dot2(pk, 0x3f803f80u, l);
        sc[k] = __uint_as_float(pk);
      }
      l += dpp<0x124>(l); l += dpp<0x128>(l);
    }
#pragma unroll
    for (int k = 0; k < 8; ++k) acc[k] = 0.f;
    SCHED();
    STAMP(8);
    unsigned yoff = (r4 & 1) ? OOB : WS_GB + (b * 128 + h * 4 + cls) * 2048 + 512 * w4 + 32 * c16 + 16 * (lane >> 5), zsh = 16 * (c16 & 1) + 8 * (lane >> 5);
    unsigned rwo = L_MRG + (w4 * 4 + r4) * 272 + ((c16 - 4 * rot) & 15) * 16, rro = L_MRG + c16 * 272 + r4 * 16, mwo = L_MRG + 16 * 272 + ((c16 & 1) * 16 + w4 * 4 + r4) * 4, mro = L_MRG + 16 * 272 + c16 * 4, m4o = L_MRG + 16 * 272 + r4 * 16;
    asm volatile("" : "+v"(zsh), "+v"(yoff), "+v"(rwo), "+v"(rro), "+v"(mwo), "+v"(mro), "+v"(m4o));
    step(A, B, IC<1>{}, IC<7>{}, IC<1>{});
    conv(IC<6>{});
#pragma unroll 1
    for (int i = 2;;) {
      step(B, A, IC<1>{}, IC<-1>{}, IC<1>{});
      step(A, B, IC<1>{}, IC<-1>{}, IC<1>{});
      i += 2;
      if (i >= nr) break;
    }
    conv(IC<7>{});
    if (cls == 0) lds_wait(cnt + 1, 4u);
    step(B, B, IC<0>{}, IC<-1>{}, IC<1>{});
    STAMP(1);
    *reinterpret_cast<u32x4*>(smem + rwo) = u32x4{pk_bf(acc[0], acc[1]), pk_bf(acc[2], acc[3]), pk_bf(acc[4], acc[5]), pk_bf(acc[6], acc[7])};
    *reinterpret_cast<float*>(smem + mwo) = (c16 & 1) ? l : m;
    {
      u32x4 af[4];
      float mr, lr;
      f32x4 m4;
      unsigned got = lds_arrive_nw(L_CNT), need = 3u;
#pragma unroll 1
      for (int spin = 0; spin < SPIN_MAX; ++spin) {
#pragma unroll
        for (int t = 0; t < 4; ++t) af[t] = *reinterpret_cast<const u32x4*>(smem + rro + t * 64);
        mr = *reinterpret_cast<const float*>(smem + mro); lr = *reinterpret_cast<const float*>(smem + mro + 64);
        m4 = *reinterpret_cast<const f32x4*>(smem + m4o);
        SCHED();
        if (lds_done(got) >= need) break;
        need = 4u;
        __builtin_amdgcn_s_sleep(1);
        asm volatile("ds_read_b32 %0, %1" : "=v"(got) : "v"((unsigned)L_CNT) : "memory");
      }
      SCHED();
      u64 tq = TPUB ? wall_clock64() : 0;
      f32x4 d[8];
#pragma unroll
      for (int e = 0; e < 8; ++e) {
        d[e] = f32x4{0.f, 0.f, 0.f, 0.f};
#pragma unroll
        for (int t = 0; t < 4; ++t) d[e] = __builtin_amdgcn_mfma_f32_16x16x32_bf16(__builtin_bit_cast(bf16x8_t, af[t]), __builtin_bit_cast(bf16x8_t, T[t][e]), d[e], 0, 0, 0);
      }
      const float M = row16_max(mr);
      const float L = row16_sum(ex2((mr - M) * C2) * lr);
      const float z = fmaf(M, C2, __builtin_amdgcn_logf(L));
      float wt[4], a[8];
#pragma unroll
      for (int i = 0; i < 4; ++i) wt[i] = ex2(fmaf(m4[i], C2, -z));
#pragma unroll
      for (int e = 0; e < 8; ++e) a[e] = fmaf(wt[3], d[e][3], fmaf(wt[2], d[e][2], fmaf(wt[1], d[e][1], wt[0] * d[e][0])));
      float v4[4];
#pragma unroll
      for (int k = 0; k < 4; ++k) v4[k] = x16_sum(rs32(a[k], a[k + 4]));
      if (TPUB) { while (tq - tstart < TPUB) { __builtin_amdgcn_s_sleep(1); tq = wall_clock64(); } }
      st16c(rs, yoff, u32x4{pk_bf(v4[0], v4[1]), tg.hi, pk_bf(v4[2], v4[3]), (tg.lo & 0xffffff00u) | ((__float_as_uint(z) >> zsh) & 0xffu)});
    }
    STAMP(2);
    STAMP_FLUSH(1); TR_FLUSH();
    return;
  }
  u32x4 Wa[16], Wb[16], xv[4];
  unsigned pf;
  u64 epw;
  {
    unsigned oa[16];
    oa[0] = (cls & 1) ? (w4 * 128 + 16 * (lane >> 3)) * 8192 + idx * 128 + 16 * (lane & 7) : (cls == 2 ? (b * 256 + w4 * 64 + 16 * r4) * 8192 + h * 256 + 16 * c16 : (idx * 128 + w4) * 1024 + 16 * lane);
#pragma unroll
    for (int i = 1; i < 16; ++i) oa[i] = oa[i - 1] + (cls ? 8192 : 4096);
    unsigned op = (((blk >> 5) * 4 + w4) * 16 + c16) * 8192 + ((idx & 56) + 3 + (cls == 0 ? -1 : (idx >= 56 ? 4 : 0)) * (lane >> 5)) * 128 + (r4 & 1) * 64;
    unsigned ox = w4 * 256 + 32 * (lane >> 3);
#pragma unroll
    for (int i = 0; i < 16; i += 8) asm volatile("" : "+v"(oa[i]), "+v"(oa[i + 1]), "+v"(oa[i + 2]), "+v"(oa[i + 3]), "+v"(oa[i + 4]), "+v"(oa[i + 5]), "+v"(oa[i + 6]), "+v"(oa[i + 7]));
    asm volatile("" : "+v"(op), "+v"(ox));
    SCHED();
    pf = *reinterpret_cast<const unsigned*>((const char*)p1 + op);
    epw = __hip_atomic_load((gu64*)p3, __ATOMIC_RELAXED, __HIP_MEMORY_SCOPE_AGENT);
#pragma unroll
    for (int i = 0; i < 4; ++i) xv[i] = ld16((const char*)p2 + ox + (i >> 1) * 1024 + 16 * (i & 1));
    SCHED();
#pragma unroll
    for (int i = 0; i < 16; ++i) { Wa[i] = ld16nt((const char*)p0 + oa[i]); SCHED(); }
  }
  char* ws = (char*)p3;
  const rsrc_t rs = __builtin_amdgcn_make_buffer_rsrc(ws, 0, (int)WS_END, 0x00020000);
  u16* out = (u16*)p5;
  auto fin = [&](const float (&a)[4], u16* dst) {
    float y = rs16(rs32(a[0], a[2]), rs32(a[1], a[3]));
    y += dpp<0x124>(y); y += dpp<0x128>(y);
    float* fr = reinterpret_cast<float*>(smem + 32768);
    if ((lane & 12) == 0) fr[w4 * 16 + 4 * (lane & 3) + r4] = y;
    COMPILER_FENCE();
    const unsigned got = lds_arrive_nw(L_CNT + 24);
    f32x2 t[4];
#pragma unroll
    for (int w = 0; w < 4; ++w) t[w] = *reinterpret_cast<const f32x2*>(fr + w * 16 + 2 * (lane & 7));
    SCHED();
    if (lds_done(got) == 3u) {
      const f32x2 r = (t[0] + t[1]) + (t[2] + t[3]);
      if (lane < 8) *reinterpret_cast<unsigned*>(dst + 2 * lane) = pk_bf(r[0], r[1]);
    }
  };
  {
    rawbar();
    const u64 t64 = (((u64)(unsigned)__builtin_amdgcn_readfirstlane((unsigned)(epw >> 32)) << 32) | (unsigned)__builtin_amdgcn_readfirstlane((unsigned)epw)) + GOLD;
    tg.hi = (unsigned)(t64 >> 32); tg.lo = (unsigned)t64;
    asm volatile("" : "+s"(tg.hi), "+s"(tg.lo) : "v"(pf) : "memory");
    if (w4 == 0) { cnt[13] = tg.hi; cnt[14] = tg.lo; COMPILER_FENCE(); lds_add(cnt + 15, 1u, lane); }
    asm volatile("" : "+v"(Wa[15])); STAMP(13);
  }
  float* red = reinterpret_cast<float*>(smem);
  if ((XK & 2) && cls == 2) { pin16(Wa); STAMP_FLUSH(0); TR_FLUSH(); return; }
  auto gemv = [&](float* dst) {
    float acc[2][8];
#pragma unroll
    for (int bb = 0; bb < 2; ++bb)
#pragma unroll
      for (int k = 0; k < 8; ++k) acc[bb][k] = 0.f;
#pragma unroll
    for (int ip = 0; ip < 8; ++ip) {
      const unsigned xp0 = xv[ip >> 2][ip & 3], xp1 = xv[2 + (ip >> 2)][ip & 3];
#pragma unroll
      for (int k = 0; k < 4; ++k) {
        const unsigned lo = plo(Wa[2 * ip][k], Wa[2 * ip + 1][k]), hi = phi(Wa[2 * ip][k], Wa[2 * ip + 1][k]);
        acc[0][2 * k] = dot2(lo, xp0, acc[0][2 * k]); acc[0][2 * k + 1] = dot2(hi, xp0, acc[0][2 * k + 1]);
        acc[1][2 * k] = dot2(lo, xp1, acc[1][2 * k]); acc[1][2 * k + 1] = dot2(hi, xp1, acc[1][2 * k + 1]);
      }
    }
    float v8[8], v4[4];
#pragma unroll
    for (int k = 0; k < 8; ++k) v8[k] = rs32(acc[0][k], acc[1][k]);
#pragma unroll
    for (int k = 0; k < 4; ++k) { const float t = rs16(v8[k], v8[k + 4]); v4[k] = t + dpp<0x128>(t); }
    *reinterpret_cast<f32x4*>(dst + (lane >> 5) * 64 + 8 * (lane & 7) + 4 * (r4 & 1)) = f32x4{v4[0], v4[1], v4[2], v4[3]};
    COMPILER_FENCE();
  };
  if (cls & 1) {
    gemv(red + w4 * 128);
    const unsigned got = lds_arrive_nw(L_CNT + 8);
    f32x4 t4[4];
#pragma unroll
    for (int w = 0; w < 4; ++w) t4[w] = *reinterpret_cast<const f32x4*>(red + w * 128 + 4 * (lane & 31));
    SCHED();
    if (lds_done(got) == 3u) {
      const f32x4 t = (t4[0] + t4[1]) + (t4[2] + t4[3]);
      st16c(rs, lane < 32 ? WS_QX + ((((cls >> 1) * 2 + r4) * 32 + h) * 64 + b * 32 + 2 * c16) * 8 : OOB, u32x4{pk_bf(t[0], t[1]), tg.hi, pk_bf(t[2], t[3]), tg.hi});
      SCHED();
      lds_add(cnt + 2, 1u, lane);
    } else lds_wait(cnt + 2, 5u);
    STAMP(8);
  } else if (cls == 2) {
    while (wall_clock64() - tstart < TQX) __builtin_amdgcn_s_sleep(4);
    u32x4 q0, q1;
    const unsigned qo = WS_QX + (h * 64 + 4 * c16) * 8;
#pragma unroll 1
    for (int spin = 0; spin < SPIN_MAX; ++spin) {
      COMPILER_FENCE();
      const u32x4 g0 = ld16c(rs, qo), g1 = ld16c(rs, qo + 16), g2 = ld16c(rs, qo + 16384), g3 = ld16c(rs, qo + 16400);
      q0 = u32x4{g0[0], g0[2], g1[0], g1[2]}; q1 = u32x4{g2[0], g2[2], g3[0], g3[2]};
      if (__all((g0[1] == tg.hi) & (g0[3] == tg.hi) & (g1[1] == tg.hi) & (g1[3] == tg.hi) & (g2[1] == tg.hi) & (g2[3] == tg.hi) & (g3[1] == tg.hi) & (g3[3] == tg.hi))) break;
      __builtin_amdgcn_s_sleep(1);
    }
    STAMP(9);
    float v[4];
    {
      float u[16], t8[8];
#pragma unroll
      for (int i = 0; i < 16; ++i) {
        float a = 0.f, c = 0.f;
#pragma unroll
        for (int k = 0; k < 4; ++k) { a = dot2(Wa[i][k], q0[k], a); c = dot2(Wa[i][k], q1[k], c); }
        u[i] = (c16 & 4 ? c : a) + dpp<0x141>(c16 & 4 ? a : c);
        asm volatile("" : "+v"(u[i]));
      }
#pragma unroll
      for (int i = 0; i < 8; ++i) { t8[i] = (c16 & 2 ? u[i + 8] : u[i]) + dpp<0x4E>(c16 & 2 ? u[i] : u[i + 8]); asm volatile("" : "+v"(t8[i])); }
#pragma unroll
      for (int i = 0; i < 4; ++i) {
        float t = (c16 & 1 ? t8[i + 4] : t8[i]) + dpp<0xB1>(c16 & 1 ? t8[i] : t8[i + 4]);
        asm volatile("" : "+v"(t));
        v[i] = t + dpp<0x128>(t);
      }
    }
    st16c(rs, c16 < 8 ? WS_QA + ((c16 >> 2) * 32 + h) * 2048 + (b * 256 + w4 * 64 + 16 * r4 + 4 * (c16 & 3)) * 4 : OOB, u32x4{pk_bf(v[0], v[1]), tg.hi, pk_bf(v[2], v[3]), tg.hi});
    STAMP(10);
  }
  if (cls >= 2) {
    const unsigned tw = __builtin_nontemporal_load(reinterpret_cast<const unsigned*>(out + (idx >> 5) * 1024 + (cls & 1) * 512 + 16 * (idx & 31)));
    asm volatile("" :: "v"(tw));
  }
  asm volatile("" :: "v"(xv[0]), "v"(xv[1]), "v"(xv[2]), "v"(xv[3]));
  SCHED();
  if (cls == 3) {
    const int fb = idx >> 5;
    const unsigned doff = WS_GB + (fb * 128 + 32 * w4 + (lane >> 2)) * 2048 + (idx & 31) * 64 + (lane & 3) * 16;
    lds_wait(cnt + 0, 4u);
    if (TFIN) { while (wall_clock64() - tstart < TFIN) __builtin_amdgcn_s_sleep(2); }
    STAMP(3);
    u32x4 d[2];
    bool ok = false;
#pragma unroll 1
    for (int spin = 0; spin < SPIN_MAX; ++spin) {
      COMPILER_FENCE();
      if (!ok) { d[0] = ld16c(rs, doff); d[1] = ld16c(rs, doff + 32768); }
      ok = (((d[0][1] ^ tg.hi) | (d[1][1] ^ tg.hi)) | (((d[0][3] ^ tg.lo) | (d[1][3] ^ tg.lo)) >> 8)) == 0u;
      if (__all(ok)) break;
      if (FSL) __builtin_amdgcn_s_sleep(FSL);
    }
    STAMP(14);
    float a[4] = {0.f, 0.f, 0.f, 0.f};
#pragma unroll
    for (int j = 0; j < 2; ++j) {
      unsigned zb = (d[j][3] & 0xffu) << (8 * (lane & 3));
      zb |= __float_as_uint(dpp<0xB1>(__uint_as_float(zb))); zb |= __float_as_uint(dpp<0x4E>(__uint_as_float(zb)));
      const float z = __uint_as_float(zb);
      float Z = fmaxf(z, dpp<0x124>(z)); Z = fmaxf(Z, dpp<0x128>(Z));
      const float wt = ex2(z - Z);
      float L = wt + dpp<0x124>(wt); L += dpp<0x128>(L);
      const float w = wt * __builtin_amdgcn_rcpf(L);
      a[0] = fmaf(w, bflo(d[j][0]), a[0]); a[1] = fmaf(w, bfhi(d[j][0]), a[1]); a[2] = fmaf(w, bflo(d[j][2]), a[2]); a[3] = fmaf(w, bfhi(d[j][2]), a[3]);
    }
    fin(a, out + fb * 1024 + 512 + 16 * (idx & 31));
    STAMP(6);
    STAMP_FLUSH(0); TR_FLUSH();
    return;
  }
  if (cls == 0) {
    {
      const u16* pb = (const u16*)p0 + (idx * 128 + 64 + w4) * 512 + 8 * lane;
#pragma unroll
      for (int i = 0; i < 16; ++i) {
        Wb[i] = ld16nt(pb + i * 2048);
        if (i >= PACE0 && (i & 1)) asm volatile("" : "+v"(Wb[i - PACE0]));
        if (i == 9) TR();
        SCHED();
      }
    }
    if (HG) lds_add(cnt + 8, 1u, lane);
    char* scr = smem + L_SCR;
    const int ab = idx >> 5, r = lane & 31, hh = lane >> 5;
#pragma unroll
    for (int i = 0; i < 16; ++i) *reinterpret_cast<u32x4*>(smem + ((i >> 3) * 4 + r4) * 8192 + toff(4 * (i & 7) + w4, c16)) = Wa[i];
#pragma unroll
    for (int i = 0; i < 16; ++i) *reinterpret_cast<u32x4*>(smem + ((2 + (i >> 3)) * 4 + r4) * 8192 + toff(4 * (i & 7) + w4, c16)) = Wb[i];
    STAMP(8);
    TR();
    if (XK & 2) { lds_arrive(cnt + 1, lane); if (idx == 0 && w4 == 0 && lane == 0) { __builtin_amdgcn_s_sleep(127); __builtin_amdgcn_s_sleep(127); __hip_atomic_store((gu64*)(ws + WS_EP), ((u64)tg.hi << 32) | tg.lo, __ATOMIC_RELAXED, __HIP_MEMORY_SCOPE_AGENT); } STAMP_FLUSH(0); TR_FLUSH(); return; }
    u32x4 qlast = {0u, 0u, 0u, 0u};
    {
      u32x4 g[18];
      const int fl = (idx & 1) * 8;
      const unsigned o8 = WS_QA + (ab * 32 + w4 * 8) * 2048 + lane * 16, ol = WS_QA + (ab * 32 + r) * 2048 + 1984 + 32 * hh;
      unsigned pend = (XK & 128) ? 0u : 0x3ffffu;
#pragma unroll 1
      for (int spin = 0; spin < SPIN_MAX && pend; ++spin) {
        COMPILER_FENCE();
#pragma unroll
        for (int i = 0; i < 18; ++i) g[i] = ld16c(rs, (pend >> i & 1) ? (i < 16 ? o8 + (i ^ fl) * 1024 : ol + 16 * (i - 16)) : OOB);
        if (HG == 1 && spin == 0) { SCHED(); lds_add(cnt + 8, ~0u, lane); SCHED(); }
#pragma unroll
        for (int i = 0; i < 18; ++i) {
          if (HG == 2 && spin == 0 && i == 10) lds_add(cnt + 8, ~0u, lane);
          if ((pend >> i & 1) && __all((g[i][1] == tg.hi) & (g[i][3] == tg.hi))) {
            pend &= ~(1u << i);
            if (i >= 16) { qlast[2 * (i - 16)] = g[i][0]; qlast[2 * (i - 16) + 1] = g[i][2]; }
            else if (!(i & 1) || lane < 60) *reinterpret_cast<u32x2*>(scr + (w4 * 8 + ((i ^ fl) >> 1)) * 1008 + (i & 1) * 512 + 8 * lane) = u32x2{g[i][0], g[i][2]};
          }
        }
        if (pend) __builtin_amdgcn_s_sleep(8);
      }
    }
    sbar4(cnt + 3, lane);
    STAMP(10);
    f32x16 sacc;
#pragma unroll
    for (int i = 0; i < 16; ++i) sacc[i] = 0.f;
#pragma unroll
    for (int jj = 0; jj < 32; ++jj) {
      const u32x4 af = *reinterpret_cast<const u32x4*>(smem + (w4 * 4 + (jj >> 3)) * 8192 + toff(r, 2 * (jj & 7) + hh));
      const u32x4 bf = jj < 31 ? *reinterpret_cast<const u32x4*>(scr + r * 1008 + 16 * (2 * (jj < 31 ? jj : 0) + hh)) : qlast;
      sacc = __builtin_amdgcn_mfma_f32_32x32x16_bf16(__builtin_bit_cast(bf16x8_t, af), __builtin_bit_cast(bf16x8_t, bf), sacc, 0, 0, 0);
    }
    float m = sacc[0];
#pragma unroll
    for (int i = 1; i < 16; ++i) m = fmaxf(m, sacc[i]);
    m = xhalf_max(m);
    if (hh == 0) *reinterpret_cast<float*>(scr + r * 1008 + 992 + 4 * w4) = m;
    sbar4(cnt + 4, lane);
    STAMP(11);
    {
      const f32x4 m4 = *reinterpret_cast<const f32x4*>(scr + r * 1008 + 992);
      m = fmaxf(fmaxf(m4[0], m4[1]), fmaxf(m4[2], m4[3]));
    }
    const float mc = m * C2;
    float p[16], ls = 0.f;
#pragma unroll
    for (int i = 0; i < 16; ++i) { p[i] = ex2(fmaf(sacc[i], C2, -mc)); ls += p[i]; }
    ls = xhalf_sum(ls);
    sbar4(cnt + 5, lane);
    u32x4* pf = reinterpret_cast<u32x4*>(scr);
    float* wl = reinterpret_cast<float*>(scr + 8192);
    if (hh == 0) wl[r * 4 + w4] = ls;
    pf[(2 * w4) * 64 + lane] = u32x4{pk_bf(p[0], p[1]), pk_bf(p[2], p[3]), pk_bf(p[4], p[5]), pk_bf(p[6], p[7])};
    pf[(2 * w4 + 1) * 64 + lane] = u32x4{pk_bf(p[8], p[9]), pk_bf(p[10], p[11]), pk_bf(p[12], p[13]), pk_bf(p[14], p[15])};
    sbar4(cnt + 6, lane);
    u32x4 pb8[8];
#pragma unroll
    for (int ks = 0; ks < 8; ++ks) pb8[ks] = pf[ks * 64 + lane];
    const f32x4 l4 = *reinterpret_cast<const f32x4*>(wl + r * 4);
    const int qq = (lane & 15) >> 2, pp = lane & 3, blk2 = (lane >> 4) & 1;
    char* ot = scr + 12288 + w4 * 4096;
#pragma unroll
    for (int c2 = 0; c2 < 2; ++c2) {
      f32x16 oacc[2];
#pragma unroll
      for (int ct = 0; ct < 2; ++ct)
#pragma unroll
        for (int i = 0; i < 16; ++i) oacc[ct][i] = 0.f;
#pragma unroll
      for (int ks = 0; ks < 8; ++ks)
#pragma unroll
        for (int ct = 0; ct < 2; ++ct) {
          typedef __attribute__((address_space(3))) s16x4* lp;
          const char* tile = smem + ((ks >> 1) * 4 + w4) * 8192;
          const int cc = 2 * c2 + ct;
          const s16x4 lo = __builtin_amdgcn_ds_read_tr16_b64_v4i16((lp)(tile + toff(16 * (ks & 1) + 4 * hh + qq, 4 * cc + 2 * blk2 + (pp >> 1)) + 8 * (pp & 1)));
          const s16x4 hi = __builtin_amdgcn_ds_read_tr16_b64_v4i16((lp)(tile + toff(16 * (ks & 1) + 8 + 4 * hh + qq, 4 * cc + 2 * blk2 + (pp >> 1)) + 8 * (pp & 1)));
          const s16x8 afr = __builtin_shufflevector(lo, hi, 0, 1, 2, 3, 4, 5, 6, 7);
          oacc[ct] = __builtin_amdgcn_mfma_f32_32x32x16_bf16(__builtin_bit_cast(bf16x8_t, afr), __builtin_bit_cast(bf16x8_t, pb8[ks]), oacc[ct], 0, 0, 0);
        }
#pragma unroll
      for (int ct = 0; ct < 2; ++ct)
#pragma unroll
        for (int g = 0; g < 4; ++g)
          *reinterpret_cast<u32x2*>(ot + r * 128 + 16 * ((4 * ct + g) ^ (r & 7)) + 8 * hh) = u32x2{pk_bf(oacc[ct][4 * g], oacc[ct][4 * g + 1]), pk_bf(oacc[ct][4 * g + 2], oacc[ct][4 * g + 3])};
      if (c2 == 0) lds_add(cnt + 8, 1u, lane);
      __builtin_amdgcn_wave_barrier();
#pragma unroll
      for (int i = 0; i < 4; ++i) {
        const int hd = 8 * i + (lane >> 3), pc = lane & 7;
        const u32x4 vv = *reinterpret_cast<const u32x4*>(ot + hd * 128 + 16 * (pc ^ (hd & 7)));
        st16c(rs, (XK & 16) ? OOB : WS_APO + ((hd * 64 + idx) * 512 + w4 * 128 + 64 * c2 + 8 * pc) * 2, vv);
      }
      __builtin_amdgcn_wave_barrier();
    }
    lds_arrive(cnt + 1, lane);
    STAMP(1);
    asm volatile("s_waitcnt vmcnt(0)" ::: "memory");
    if (lds_arrive(cnt + 9, lane) == 3u && hh == 0)
      st16c(rs, WS_AML + (idx * 32 + r) * 16, u32x4{__float_as_uint(m), tg.hi, __float_as_uint((l4[0] + l4[1]) + (l4[2] + l4[3])), tg.hi});
    lds_add(cnt + 8, ~0u, lane);
    STAMP(2);
    if (idx == 0 && w4 == 0) {
      lds_wait(cnt + 0, 4u);
#pragma unroll 1
      for (int spin = 0; spin < SPIN_MAX; ++spin) {
        COMPILER_FENCE();
        const u32x2 t0 = ld8c(rs, WS_AML + lane * 512), t1 = ld8c(rs, WS_QX + (64 * 64 + lane * 32) * 8);
        if (__all((t0[1] == tg.hi) & (t1[1] == tg.hi))) break;
        __builtin_amdgcn_s_sleep(8);
      }
      if (lane == 0) __hip_atomic_store((gu64*)(ws + WS_EP), ((u64)tg.hi << 32) | tg.lo, __ATOMIC_RELAXED, __HIP_MEMORY_SCOPE_AGENT);
    }
    STAMP_FLUSH(0); TR_FLUSH();
    return;
  }
  if (cls == 1) {
    const int idx = idx2, h = idx >> 1, b = idx & 1;
    {
      const u16* pa = (const u16*)p1 + (w4 * 128 + 16 * (lane >> 3)) * 4096 + h * 128 + b * 64 + 8 * (lane & 7);
      const u16* pb = (const u16*)p4 + (h * 128 + b * 64 + w4 * 2) * 512 + 8 * lane;
#pragma unroll
      for (int i = 0; i < 16; ++i) {
        Wa[i] = ld16nt(pa + i * 4096);
        SCHED();
        if ((i & 1) && !((idx & 7) == 3 || idx == 63)) asm volatile("s_waitcnt vmcnt(%0)" :: "n"(2 * PACE1) : "memory");
        if ((i & 7) == 7) TR();
        SCHED();
      }
#pragma unroll
      for (int i = 0; i < 16; ++i) {
        Wb[i] = ld16nt(pb + (i >> 1) * 4096 + (i & 1) * 512);
        SCHED();
        if (i & 1) asm volatile("s_waitcnt vmcnt(%0)" :: "n"(2 * PACE1) : "memory");
        if ((i & 7) == 7) TR();
        SCHED();
      }
    }
    if (XK & 2) { pin16(Wa); pin16(Wb); STAMP_FLUSH(0); TR_FLUSH(); return; }
    float* red2 = red + 2048;
    float* wsh = red + 3072;
    if (w4 == 0) {
      const unsigned fo = WS_AML + (lane * 32 + h) * 16;
      while (wall_clock64() - tstart < TAML) __builtin_amdgcn_s_sleep(8);
      if (H1) lds_add(cnt + 8, 4u, lane);
      u32x4 g;
      bool ok = false;
#pragma unroll 1
      for (int spin = 0; spin < SPIN_MAX; ++spin) {
        COMPILER_FENCE();
        if (!ok) g = ld16c(rs, fo);
        ok = (g[1] == tg.hi) & (g[3] == tg.hi);
        if (__all(ok)) break;
        __builtin_amdgcn_s_sleep(2);
      }
      lds_add(cnt + 7, 1u, lane);
      const float mu = __uint_as_float(g[0]), lu = __uint_as_float(g[2]);
      const float M = x16_max(row16_max(mu));
      const float wl = ex2((mu - M) * C2);
      wsh[lane] = wl / x16_sum(row16_sum(wl * lu));
      lds_arrive(cnt + 7, lane);
    } else lds_wait(cnt + 7, 1u);
    COMPILER_FENCE();
    STAMP(3);
    u32x4 pp[16];
#pragma unroll
    for (int i = 0; i < 16; ++i) {
      if (i >= PG) asm volatile("" : "+v"(pp[i >= PG ? i - PG : 0]));
      pp[i] = ld16c(rs, (XK & 32) ? OOB : WS_APO + (h * 64 + w4 * 16 + (i ^ (8 * b))) * 1024 + lane * 16);
      SCHED();
    }
    if (H1) lds_add(cnt + 8, ~0u, lane);
    SCHED();
    lds_wait(cnt + 7, 2u);
    float a8[8];
#pragma unroll
    for (int k = 0; k < 8; ++k) a8[k] = 0.f;
    const float wv = wsh[w4 * 16 + (c16 ^ (8 * b))];
#pragma unroll
    for (int i = 0; i < 16; ++i) {
      const float wt = rdlane(wv, i);
#pragma unroll
      for (int k = 0; k < 4; ++k) { a8[2 * k] = fmaf(wt, bflo(pp[i][k]), a8[2 * k]); a8[2 * k + 1] = fmaf(wt, bfhi(pp[i][k]), a8[2 * k + 1]); }
      asm volatile("" : "+v"(a8[0]), "+v"(a8[1]), "+v"(a8[2]), "+v"(a8[3]), "+v"(a8[4]), "+v"(a8[5]), "+v"(a8[6]), "+v"(a8[7]));
      SCHED();
    }
    *reinterpret_cast<f32x4*>(red + w4 * 512 + 8 * lane) = f32x4{a8[0], a8[1], a8[2], a8[3]};
    *reinterpret_cast<f32x4*>(red + w4 * 512 + 8 * lane + 4) = f32x4{a8[4], a8[5], a8[6], a8[7]};
    sbar4(cnt + 3, lane);
    STAMP(4);
#pragma unroll
    for (int i = 0; i < 4; ++i) {
      const float* src = red + (i >> 1) * 1024 + w4 * 128 + 16 * (lane >> 3) + 8 * (i & 1);
      const f32x4 t0 = *reinterpret_cast<const f32x4*>(src) + *reinterpret_cast<const f32x4*>(src + 512), t1 = *reinterpret_cast<const f32x4*>(src + 4) + *reinterpret_cast<const f32x4*>(src + 516);
      xv[i] = u32x4{pk_bf(t0[0], t0[1]), pk_bf(t0[2], t0[3]), pk_bf(t1[0], t1[1]), pk_bf(t1[2], t1[3])};
    }
    gemv(red2 + w4 * 128);
    sbar4(cnt + 4, lane);
    STAMP(12);
    unsigned op;
    {
      f32x2 t = {0.f, 0.f};
#pragma unroll
      for (int w = 0; w < 4; ++w) t += *reinterpret_cast<const f32x2*>(red2 + w * 128 + (lane >> 5) * 64 + 8 * (lane & 7) + 2 * w4);
      op = pk_bf(t[0], t[1]);
    }
    float* red3 = red + 4096;
    float a[2][8];
#pragma unroll
    for (int bb = 0; bb < 2; ++bb)
#pragma unroll
      for (int k = 0; k < 8; ++k) a[bb][k] = 0.f;
#pragma unroll
    for (int ip = 0; ip < 8; ++ip) {
      const unsigned v0 = __builtin_amdgcn_readlane(op, ip), v1 = __builtin_amdgcn_readlane(op, 32 + ip);
#pragma unroll
      for (int k = 0; k < 4; ++k) {
        const unsigned lo = plo(Wb[2 * ip][k], Wb[2 * ip + 1][k]), hi = phi(Wb[2 * ip][k], Wb[2 * ip + 1][k]);
        a[0][2 * k] = dot2(lo, v0, a[0][2 * k]); a[0][2 * k + 1] = dot2(hi, v0, a[0][2 * k + 1]);
        a[1][2 * k] = dot2(lo, v1, a[1][2 * k]); a[1][2 * k + 1] = dot2(hi, v1, a[1][2 * k + 1]);
      }
    }
#pragma unroll
    for (int bb = 0; bb < 2; ++bb) {
      *reinterpret_cast<f32x4*>(red3 + (w4 * 2 + bb) * 512 + 8 * lane) = f32x4{a[bb][0], a[bb][1], a[bb][2], a[bb][3]};
      *reinterpret_cast<f32x4*>(red3 + (w4 * 2 + bb) * 512 + 8 * lane + 4) = f32x4{a[bb][4], a[bb][5], a[bb][6], a[bb][7]};
    }
    sbar4(cnt + 5, lane);
    {
      f32x4 t = {0.f, 0.f, 0.f, 0.f};
#pragma unroll
      for (int w = 0; w < 4; ++w) t += *reinterpret_cast<const f32x4*>(red3 + w * 1024 + 4 * tid);
      st16c(rs, WS_GA + idx * 4096 + tid * 16, u32x4{pk_bf(t[0], t[1]), tg.hi, pk_bf(t[2], t[3]), tg.hi});
    }
    STAMP(5);
    STAMP_FLUSH(0); TR_FLUSH();
    return;
  }
  {
    const unsigned doff = WS_GA + (w4 * 16 + (lane >> 2)) * 4096 + idx * 64 + (lane & 3) * 16;
    lds_wait(cnt + 0, 4u);
    if (TFIN) { while (wall_clock64() - tstart < TFIN) __builtin_amdgcn_s_sleep(2); }
    u32x4 d;
    bool ok = false;
#pragma unroll 1
    for (int spin = 0; spin < SPIN_MAX; ++spin) {
      COMPILER_FENCE();
      if (!ok) d = ld16c(rs, doff);
      ok = (d[1] == tg.hi) & (d[3] == tg.hi);
      if (__all(ok)) break;
      __builtin_amdgcn_s_sleep(1);
    }
    const float a[4] = {bflo(d[0]), bfhi(d[0]), bflo(d[2]), bfhi(d[2])};
    fin(a, out + (idx >> 5) * 1024 + 16 * (idx & 31));
  }
  STAMP(6);
  STAMP_FLUSH(0); TR_FLUSH();
}

extern "C" void kernel_launch(void* const* d_in, const int* in_sizes, int n_in, void* d_out, int out_size, void* d_ws, size_t ws_size, hipStream_t stream) {
  const int sel[5][3] = {{1, 3, 6}, {2, 4, 0}, {3, 2, 6}, {9, 9, 6}, {7, 8, 10}};
  Args a;
  for (int r = 0; r < 5; ++r) {
    for (int c = 0; c < 3; ++c) a.t[r][c] = (gptr)d_in[sel[r][c]];
    a.t[r][3] = (gptr)d_ws;
  }
  a.w_o = (gptr)d_in[5];
  a.out = (gptr)d_out;
  kmain<<<256, 512, 0, stream>>>(a);
}
```

```cpp
#include <hip/hip_runtime.h>
#include <hip/hip_bf16.h>
#include <stdint.h>

#define DI __device__ __forceinline__
typedef __attribute__((ext_vector_type(4))) unsigned u32x4;
typedef __attribute__((ext_vector_type(2))) unsigned u32x2;
typedef __attribute__((ext_vector_type(4))) float f32x4;
typedef __attribute__((ext_vector_type(2))) float f32x2;
typedef __attribute__((ext_vector_type(16))) float f32x16;
typedef __attribute__((ext_vector_type(8))) __bf16 bf16x8_t;
typedef __attribute__((ext_vector_type(2))) __bf16 bf16x2_t;
typedef __attribute__((ext_vector_type(4))) short s16x4;
typedef __attribute__((ext_vector_type(8))) short s16x8;
typedef unsigned short u16;
typedef unsigned long long u64;

#define SHARES 51, 51, 76, 78
constexpr int SH[4] = {SHARES};
constexpr int NR[4] = {8, 8, 10, 10};
static_assert(SH[0] + SH[1] + SH[2] + SH[3] == 256 && SH[0] <= 64 && SH[1] <= 64 && SH[2] <= 80 && SH[3] <= 80 && SH[0] >= 8 && SH[1] >= 8, "every key once");
#define TQX 430
#define TAML 1590
#define TGA 0
#define APL 0
#define AHW 0
#define ROT 1
#define ADV 10
#define SDL 0
#define TQA 0
#define PADN 100
constexpr float C2 = 0.08838834764831845f * 1.4426950408889634f;
constexpr u64 GOLD = 0x9E3779B97F4A7C15ull;

constexpr unsigned WS_EP = 0;
constexpr unsigned WS_QX = 4096;
constexpr unsigned WS_QA = WS_QX + 2 * 2 * 32 * 64 * 8;
constexpr unsigned WS_AML = WS_QA + 2 * 32 * 256 * 8;
constexpr unsigned WS_APO = WS_AML + 64 * 32 * 16;
constexpr unsigned WS_GB = WS_APO + 32 * 64 * 512 * 2;
constexpr unsigned WS_GA = WS_GB + 2 * 128 * 128 * 16;
constexpr unsigned WS_END = WS_GA + 2 * 64 * 128 * 16;
#define STAMP(i) do {} while (0)
#define STAMP_DECL do {} while (0)
#define STAMP_FLUSH(grp) do {} while (0)

#define TR_DECL do {} while (0)
#define TR() do {} while (0)
#define TR_FLUSH() do {} while (0)
DI float bflo(unsigned u) { return __uint_as_float(u << 16); }
DI float bfhi(unsigned u) { return __uint_as_float(u & 0xffff0000u); }
DI unsigned pk_bf(float a, float b) { const f32x2 v = {a, b}; return __builtin_bit_cast(unsigned, __builtin_convertvector(v, bf16x2_t)); }
DI float dot2(unsigned k, unsigned q, float acc) {
  return __builtin_amdgcn_fdot2_f32_bf16(__builtin_bit_cast(bf16x2_t, k), __builtin_bit_cast(bf16x2_t, q), acc, false);
}
DI unsigned plo(unsigned w0, unsigned w1) { return __builtin_amdgcn_perm(w1, w0, 0x05040100u); }
DI unsigned phi(unsigned w0, unsigned w1) { return __builtin_amdgcn_perm(w1, w0, 0x07060302u); }
template <int CTRL> DI float dpp(float x) {
  return __builtin_bit_cast(float, __builtin_amdgcn_mov_dpp(__builtin_bit_cast(int, x), CTRL, 0xf, 0xf, true));
}
DI float row16_sum(float x) { x += dpp<0xB1>(x); x += dpp<0x4E>(x); x += dpp<0x141>(x); x += dpp<0x128>(x); return x; }
DI float row16_max(float x) { x = fmaxf(x, dpp<0xB1>(x)); x = fmaxf(x, dpp<0x4E>(x)); x = fmaxf(x, dpp<0x141>(x)); x = fmaxf(x, dpp<0x128>(x)); return x; }
DI float x16_sum(float x) { auto s = __builtin_amdgcn_permlane16_swap(__float_as_uint(x), __float_as_uint(x), false, false); return __uint_as_float(s[0]) + __uint_as_float(s[1]); }
DI float x16_max(float x) { auto s = __builtin_amdgcn_permlane16_swap(__float_as_uint(x), __float_as_uint(x), false, false); return fmaxf(__uint_as_float(s[0]), __uint_as_float(s[1])); }
DI float xhalf_max(float x) { auto t = __builtin_amdgcn_permlane32_swap(__float_as_uint(x), __float_as_uint(x), false, false); return fmaxf(__uint_as_float(t[0]), __uint_as_float(t[1])); }
DI float xhalf_sum(float x) { auto t = __builtin_amdgcn_permlane32_swap(__float_as_uint(x), __float_as_uint(x), false, false); return __uint_as_float(t[0]) + __uint_as_float(t[1]); }
DI u32x4 ld16(const void* p) { return *reinterpret_cast<const u32x4*>(p); }
DI u32x4 ld16nt(const void* p) { return __builtin_nontemporal_load(reinterpret_cast<const u32x4*>(p)); }
DI float ex2(float x) { return __builtin_amdgcn_exp2f(x); }
#define SCHED() __builtin_amdgcn_sched_barrier(0)

typedef __attribute__((address_space(1))) u64 gu64;
typedef __amdgpu_buffer_rsrc_t rsrc_t;
DI u32x4 ld16c(rsrc_t rs, unsigned off) { return __builtin_amdgcn_raw_buffer_load_b128(rs, (int)off, 0, 16); }
DI u32x2 ld8c(rsrc_t rs, unsigned off) { return __builtin_amdgcn_raw_buffer_load_b64(rs, (int)off, 0, 16); }
DI void st16c(rsrc_t rs, unsigned off, u32x4 v) { __builtin_amdgcn_raw_buffer_store_b128(v, rs, (int)off, 0, 16); }
constexpr int SPIN_MAX = 1 << 20;
constexpr unsigned OOB = 0x7ffffff0u;
DI void rawbar() { __builtin_amdgcn_sched_barrier(0); __builtin_amdgcn_s_barrier(); __builtin_amdgcn_sched_barrier(0); }
struct Tag { unsigned hi, lo; };
#define COMPILER_FENCE() asm volatile("" ::: "memory")

template <int N> struct IC { static constexpr int v = N; };
template <int I, int N, class F> DI void sfor(F&& f) { if constexpr (I < N) { f(IC<I>{}); sfor<I + 1, N>(f); } }
typedef __attribute__((address_space(3))) unsigned lu32;
DI void lds_wait(unsigned* cnt, unsigned target) {
#pragma unroll 1
  for (int spin = 0; spin < SPIN_MAX; ++spin) {
    if (__builtin_amdgcn_readfirstlane(__hip_atomic_load((lu32*)cnt, __ATOMIC_RELAXED, __HIP_MEMORY_SCOPE_WORKGROUP)) >= target) break;
    __builtin_amdgcn_s_sleep(1);
  }
  COMPILER_FENCE();
}
DI void lds_hold(unsigned* flag) {
#pragma unroll 1
  for (int spin = 0; spin < SPIN_MAX; ++spin) {
    if (__builtin_amdgcn_readfirstlane(__hip_atomic_load((lu32*)flag, __ATOMIC_RELAXED, __HIP_MEMORY_SCOPE_WORKGROUP)) == 0u) break;
    __builtin_amdgcn_s_sleep(2);
  }
}
DI unsigned lds_arrive_nw(unsigned addr) {
  unsigned old, one = 1u;
  asm volatile("s_mov_b64 exec, 1\n\tds_add_rtn_u32 %0, %1, %2\n\ts_mov_b64 exec, -1" : "=v"(old) : "v"(addr), "v"(one) : "memory");
  return old;
}
DI unsigned lds_done(unsigned old) { asm volatile("s_waitcnt lgkmcnt(0)" : "+v"(old) :: "memory"); return __builtin_amdgcn_readfirstlane(old); }
DI unsigned lds_arrive(unsigned* cnt, int) { return lds_done(lds_arrive_nw((unsigned)(size_t)(lu32*)cnt)); }
DI void lds_add(unsigned* cnt, unsigned v, int) {
  asm volatile("s_mov_b64 exec, 1\n\tds_add_u32 %0, %1\n\ts_mov_b64 exec, -1" :: "v"((unsigned)(size_t)(lu32*)cnt), "v"(v) : "memory");
}
DI void sbar4(unsigned* cnt, int lane) { if (lds_arrive(cnt, lane) < 3u) lds_wait(cnt, 4u); }
DI float rs32(float a, float b) { auto t = __builtin_amdgcn_permlane32_swap(__float_as_uint(a), __float_as_uint(b), false, false); return __uint_as_float(t[0]) + __uint_as_float(t[1]); }
DI float rs16(float a, float b) { auto t = __builtin_amdgcn_permlane16_swap(__float_as_uint(a), __float_as_uint(b), false, false); return __uint_as_float(t[0]) + __uint_as_float(t[1]); }
template <int N> DI unsigned rowbcast(unsigned x) { return (unsigned)__builtin_amdgcn_mov_dpp((int)x, 0x150 + N, 0xf, 0xf, true); }
DI void pin2(u32x4& a, u32x4& b) { asm volatile("" : "+v"(a), "+v"(b)); }
DI float rdlane(float x, int l) { return __builtin_bit_cast(float, __builtin_amdgcn_readlane(__builtin_bit_cast(int, x), l)); }

DI unsigned toff(unsigned row, unsigned ch) { return 256u * row + 16u * (ch ^ (((row & 3) << 2) | ((row >> 2) & 3))); }
constexpr int SMEM = 160 * 1024;
constexpr int L_SCR = 131072;
constexpr int L_MRG = 65536;
constexpr int L_CNT = L_SCR + 32 * 1008;
constexpr int L_ML = L_CNT + 64;

struct KV { u32x4 r[8]; };

typedef const __attribute__((address_space(1))) char* gptr;
struct Args { gptr t[5][4]; gptr w_o; gptr out; };

__global__ __launch_bounds__(512, 2) __attribute__((aligned(4096))) void kmain(Args a) {
  __shared__ __attribute__((aligned(16))) char smem[SMEM];
  const int tid = threadIdx.x, blk = blockIdx.x, lane = tid & 63;
  const int wave = __builtin_amdgcn_readfirstlane(tid >> 6), w4 = wave & 3;
  const int cls = (blk >> 3) & 3;
  const int arow = wave < 4 ? cls : 4;
  gptr p0 = a.t[arow][0], p1 = a.t[arow][1], p2 = a.t[arow][2], p3 = a.t[arow][3], p4 = a.w_o, p5 = a.out;
  SCHED();
  int idx = 8 * (blk & 7) + (blk >> 5);
  if (ROT && wave < 4) idx = (idx & ~7) | ((idx + (cls == 3 ? 2 : (cls == 2 ? 5 : 0))) & 7);
  const int h = idx >> 1, b = idx & 1;
  const int r4 = lane >> 4, c16 = lane & 15;
  unsigned* cnt = reinterpret_cast<unsigned*>(smem + L_CNT);
  STAMP_DECL;
  TR_DECL;
  STAMP(0);
  Tag tg;
  if (wave >= 4) {
    if (tid - 256 < 16) cnt[tid - 256] = 0u;
    const int nr = cls < 2 ? 8 : 10;
    unsigned tab;
    {
      int j = 2 * nr - 1 - lane;
      j = lane < nr ? lane : (j > 2 ? j : (j == 2 ? 0 : j + 1));
      int n = j + (h * nr >> 5);
      n = n >= nr ? n - nr : n;
      int off = 0, wd = 0;
#pragma unroll
      for (int d = 0; d < 4; ++d) {
        const int k = ((2 * n + 1) * NR[d] + (d < cls) - 1 + nr) * (nr == 8 ? 4096 : 3277) >> 16;
        off += k * (SH[d] / NR[d]) + min(k, SH[d] % NR[d]);
        wd = d == cls ? SH[d] / NR[d] + (n < SH[d] % NR[d]) : wd;
      }
      tab = lane < 2 * nr ? (unsigned)(4 * off + w4 * wd) << 10 | (unsigned)wd : 0u;
    }
    asm volatile("" : "+v"(tab), "+s"(p0), "+s"(p1), "+s"(p2), "+s"(p3));
    SCHED();
    char* ws = (char*)p3;
    const rsrc_t rs = __builtin_amdgcn_make_buffer_rsrc(ws, 0, (int)WS_END, 0x00020000);
    const char* kb = (const char*)p0 + ((size_t)(b * 32 + h) << 20);
    const char* vb = (const char*)p1 + ((size_t)(b * 32 + h) << 20);
    const unsigned lo = lane * 16u, lo2 = lo + 4096u;
    int nx = 0;
    auto nexts = [&]() {
      const unsigned e = __builtin_amdgcn_readlane(tab, nx);
      const rsrc_t r = __builtin_amdgcn_make_buffer_rsrc(const_cast<char*>((nx < nr ? kb : vb) + (e & ~1023u)), 0, (int)((e & 15u) << 10), 0x00020000);
      ++nx;
      return r;
    };
    const rsrc_t s0 = nexts();
    const int rot = cls;
    const char* wb = (const char*)p2 + h * 131072;
    unsigned wl = r4 * 8192 + w4 * 256 + c16 * 16;
    asm volatile("s_waitcnt lgkmcnt(0)" : "+v"(wl), "+v"(tab) :: "memory");
    if (SDL) __builtin_amdgcn_s_sleep(SDL);
    rawbar();
    KV A, B, X;
    u32x4 W[8][4];
    u32x4 T[4][8];
    auto ldp = [&](KV& S, auto P, rsrc_t q) {
      constexpr int p = P.v;
      S.r[2 * p] = __builtin_amdgcn_raw_buffer_load_b128(q, (int)((p < 2 ? lo : lo2) + (p & 1) * 2048), 0, 2);
      S.r[2 * p + 1] = __builtin_amdgcn_raw_buffer_load_b128(q, (int)((p < 2 ? lo : lo2) + (p & 1) * 2048 + 1024), 0, 2);
    };
    auto landed = [&](KV& S, auto P) { asm volatile("" : "+v"(S.r[2 * P.v]), "+v"(S.r[2 * P.v + 1])); };
    auto ldw = [&](auto G, auto J) { W[G.v][J.v] = ld16(wb + ((((G.v >> 1) + rot) & 3) * 32768 + (G.v & 1) * 4096) + wl + J.v * 1024); };
    auto conv = [&](auto G) {
      constexpr int t = G.v >> 1, j0 = (G.v & 1) * 2;
#pragma unroll
      for (int jj = 0; jj < 2; ++jj)
#pragma unroll
        for (int e = 0; e < 4; ++e) {
          const unsigned a_ = W[G.v][2 * jj][e], b_ = W[G.v][2 * jj + 1][e];
          T[t][2 * e][j0 + jj] = plo(a_, b_); T[t][2 * e + 1][j0 + jj] = phi(a_, b_);
        }
#pragma unroll
      for (int e = 0; e < 8; e += 2) pin2(T[t][e], T[t][e + 1]);
    };
    const unsigned qo = WS_QX + (((2 + b) * 32 + h) * 64 + 4 * c16) * 8;
    u32x4 g0, g1;
    u32x2 tw;
    {
      const rsrc_t s1 = nexts(), s2 = nexts();
      sfor<0, 12>([&](auto I) {
        constexpr int i = I.v;
        if constexpr (i >= 4) { if constexpr (i < 8) landed(X, IC<i & 3>{}); else landed(B, IC<i & 3>{}); }
        if constexpr (i == 7 || i == 11) TR();
        if constexpr (i == 8) tw = ld8c(rs, lane == 0 ? WS_GB + (b * 128 + h * 4 + cls) * 2048 + 512 * w4 : OOB);
        if constexpr (i == 9) { g0 = ld16c(rs, qo); g1 = ld16c(rs, qo + 16); }
        if constexpr (i < 4) { ldp(X, IC<i & 3>{}, s0); ldw(IC<0>{}, IC<i & 3>{}); }
        else if constexpr (i < 8) { ldp(B, IC<i & 3>{}, s1); ldw(IC<1>{}, IC<i & 3>{}); }
        else { ldp(A, IC<i & 3>{}, s2); ldw(IC<2>{}, IC<i & 3>{}); }
        SCHED();
        if constexpr (i == 8) { conv(IC<0>{}); SCHED(); }
      });
    }
    lds_wait(cnt + 15, 1u);
    tg.hi = __builtin_amdgcn_readfirstlane(cnt[13]); tg.lo = __builtin_amdgcn_readfirstlane(cnt[14]);
    asm volatile("" :: "v"(tw));
    u32x4 qpk;
    {
      SCHED();
      conv(IC<1>{});
      SCHED();
      if (!__all((g0[1] == tg.hi) & (g0[3] == tg.hi) & (g1[1] == tg.hi) & (g1[3] == tg.hi))) {
#pragma unroll 1
        for (int spin = 0; spin < SPIN_MAX; ++spin) {
          COMPILER_FENCE();
          g0 = ld16c(rs, qo); g1 = ld16c(rs, qo + 16);
          if (__all((g0[1] == tg.hi) & (g0[3] == tg.hi) & (g1[1] == tg.hi) & (g1[3] == tg.hi))) break;
        }
      }
      qpk = u32x4{g0[0], g0[2], g1[0], g1[2]};
    }
    STAMP(7);
    const bool holds = cls == 0;
    float sc[10], acc[8];
#pragma unroll
    for (int k = 0; k < 10; ++k) sc[k] = 0.f;
    auto point = [&](KV& S, KV& N, rsrc_t sn, auto P, auto REFILL, auto AH, auto WG) {
      constexpr int ah = AH.v;
      if constexpr (REFILL.v) {
        if constexpr (P.v == 0 && ah > 0) { if (holds) lds_hold(cnt + 8); sfor<0, ah>([&](auto Q) { ldp(N, Q, sn); }); SCHED(); }
        landed(S, P);
        if constexpr (P.v + ah < 4) { if constexpr ((P.v & 1) == 0) { if (holds) lds_hold(cnt + 8); } ldp(N, IC<(P.v + ah) & 3>{}, sn); }
        if constexpr (WG.v >= 0) ldw(WG, P);
        SCHED();
      }
    };
    auto step = [&](KV& S, KV& N, auto REFILL, auto WG, auto ISV) {
      rsrc_t sn = s0;
      if constexpr (REFILL.v) sn = nexts();
      sfor<0, 4>([&](auto P) {
        point(S, N, sn, P, REFILL, IC<(ISV.v || WG.v < 0 ? 1 : AHW)>{}, WG);
        if constexpr (!ISV.v) {
#pragma unroll
          for (int t = 2 * P.v; t < 2 * P.v + 2; ++t) {
            acc[t] = 0.f;
#pragma unroll
            for (int k = 0; k < 4; ++k) acc[t] = dot2(S.r[t][k], qpk[k], acc[t]);
          }
          asm volatile("" : "+v"(acc[2 * P.v]), "+v"(acc[2 * P.v + 1]));
        } else {
          const unsigned pp = rowbcast<4 * P.v>(__float_as_uint(sc[0]));
#pragma unroll
          for (int k = 0; k < 4; ++k) { acc[2 * k] = dot2(plo(S.r[2 * P.v][k], S.r[2 * P.v + 1][k]), pp, acc[2 * k]); acc[2 * k + 1] = dot2(phi(S.r[2 * P.v][k], S.r[2 * P.v + 1][k]), pp, acc[2 * k + 1]); }
          asm volatile("" : "+v"(acc[0]), "+v"(acc[1]), "+v"(acc[2]), "+v"(acc[3]), "+v"(acc[4]), "+v"(acc[5]), "+v"(acc[6]), "+v"(acc[7]));
        }
        SCHED();
      });
      if constexpr (!ISV.v) {
        float u[4], v[2];
#pragma unroll
        for (int i = 0; i < 4; ++i) u[i] = (c16 & 8 ? acc[i + 4] : acc[i]) + dpp<0x128>(c16 & 8 ? acc[i] : acc[i + 4]);
#pragma unroll
        for (int i = 0; i < 2; ++i) v[i] = (c16 & 4 ? u[i + 2] : u[i]) + dpp<0x141>(c16 & 4 ? u[i] : u[i + 2]);
        float x = (c16 & 2 ? v[1] : v[0]) + dpp<0x4E>(c16 & 2 ? v[0] : v[1]);
        x += dpp<0xB1>(x);
#pragma unroll
        for (int k = 9; k > 0; --k) sc[k] = sc[k - 1];
        sc[0] = x;
      } else {
#pragma unroll
        for (int k = 0; k < 9; ++k) sc[k] = sc[k + 1];
      }
      SCHED();
      if constexpr (REFILL.v || ISV.v) TR();
    };
    step(B, B, IC<0>{}, IC<-1>{}, IC<0>{});
    step(A, B, IC<1>{}, IC<3>{}, IC<0>{});
    step(X, X, IC<0>{}, IC<-1>{}, IC<0>{});
    conv(IC<2>{});
    sfor<3, 6>([&](auto I) {
      if constexpr (I.v & 1) step(B, A, IC<1>{}, IC<I.v + 1>{}, IC<0>{}); else step(A, B, IC<1>{}, IC<I.v + 1>{}, IC<0>{});
      conv(I);
    });
#pragma unroll 1
    for (int i = 6; i < nr; i += 2) {
      step(A, B, IC<1>{}, IC<-1>{}, IC<0>{});
      step(B, A, IC<1>{}, IC<-1>{}, IC<0>{});
    }
    float m = -INFINITY, l = 0.f;
#pragma unroll
    for (int k = 0; k < 10; ++k) { sc[k] = (unsigned)(c16 >> 1) < (__builtin_amdgcn_readlane(tab, nr + k) & 15u) ? sc[k] : -INFINITY; m = fmaxf(m, sc[k]); }
    m = row16_max(m);
    {
      const float mc = m * C2;
#pragma unroll
      for (int k = 0; k < 10; ++k) {
        const float p = ex2(fmaf(sc[k], C2, -mc));
        const unsigned pk = pk_bf(dpp<0x00>(p), dpp<0xAA>(p));
        l = dot2(pk, 0x3f803f80u, l);
        sc[k] = __uint_as_float(pk);
      }
      l += dpp<0x124>(l); l += dpp<0x128>(l);
      *reinterpret_cast<float*>(smem + L_ML + ((c16 & 1) * 16 + w4 * 4 + r4) * 4) = (c16 & 1) ? l : m;
      COMPILER_FENCE();
      lds_add(cnt + 10, 1u, lane);
    }
#pragma unroll
    for (int k = 0; k < 8; ++k) acc[k] = 0.f;
    SCHED();
    STAMP(8);
    unsigned yoff = (r4 & 1) ? OOB : WS_GB + (b * 128 + h * 4 + cls) * 2048 + 512 * w4 + 32 * c16 + 16 * (lane >> 5), zw;
    unsigned rwo = L_MRG + (w4 * 4 + r4) * 272 + ((c16 - 4 * rot) & 15) * 16, rro = L_MRG + c16 * 272 + r4 * 16;
    asm volatile("" : "+v"(yoff), "+v"(rwo), "+v"(rro));
    step(A, B, IC<1>{}, IC<7>{}, IC<1>{});
    conv(IC<6>{});
#pragma unroll 1
    for (int i = 2;;) {
      step(B, A, IC<1>{}, IC<-1>{}, IC<1>{});
      i += 2;
      step(A, B, IC<1>{}, IC<-1>{}, IC<1>{});
      if (i >= nr) break;
    }
    conv(IC<7>{});
    lds_wait(cnt + 10, 4u);
    f32x4 wt;
    {
      const f32x4 m4 = *reinterpret_cast<const f32x4*>(smem + L_ML + r4 * 16);
      const float mr = *reinterpret_cast<const float*>(smem + L_ML + c16 * 4), lr = *reinterpret_cast<const float*>(smem + L_ML + 64 + c16 * 4);
      const float M = row16_max(mr);
      const float Z = fmaf(M, C2, __builtin_amdgcn_logf(row16_sum(ex2((mr - M) * C2) * lr)));
      zw = (tg.lo & 0xffffff00u) | ((__float_as_uint(Z) >> (16 * (c16 & 1) + 8 * (lane >> 5))) & 0xffu);
#pragma unroll
      for (int i = 0; i < 4; ++i) wt[i] = ex2(fmaf(m4[i], C2, -Z));
      asm volatile("" : "+v"(wt), "+v"(zw));
    }
    if (cls == 0) lds_wait(cnt + 1, 4u);
    SCHED();
    step(B, B, IC<0>{}, IC<-1>{}, IC<1>{});
    STAMP(1);
    *reinterpret_cast<u32x4*>(smem + rwo) = u32x4{pk_bf(acc[0], acc[1]), pk_bf(acc[2], acc[3]), pk_bf(acc[4], acc[5]), pk_bf(acc[6], acc[7])};
    {
      u32x4 af[4];
      unsigned got = lds_arrive_nw(L_CNT), need = 3u;
#pragma unroll 1
      for (int spin = 0; spin < SPIN_MAX; ++spin) {
#pragma unroll
        for (int t = 0; t < 4; ++t) af[t] = *reinterpret_cast<const u32x4*>(smem + rro + t * 64);
        SCHED();
        if (lds_done(got) >= need) break;
        need = 4u;
        __builtin_amdgcn_s_sleep(1);
        asm volatile("ds_read_b32 %0, %1" : "=v"(got) : "v"((unsigned)L_CNT) : "memory");
      }
      SCHED();
      f32x4 d[8];
      float v4[4];
      sfor<0, 5>([&](auto K) {
        constexpr int k = K.v;
        if constexpr (k < 4) {
          d[k] = d[k + 4] = f32x4{0.f, 0.f, 0.f, 0.f};
#pragma unroll
          for (int t = 0; t < 4; ++t) {
            d[k] = __builtin_amdgcn_mfma_f32_16x16x32_bf16(__builtin_bit_cast(bf16x8_t, af[t]), __builtin_bit_cast(bf16x8_t, T[t][k]), d[k], 0, 0, 0);
            d[k + 4] = __builtin_amdgcn_mfma_f32_16x16x32_bf16(__builtin_bit_cast(bf16x8_t, af[t]), __builtin_bit_cast(bf16x8_t, T[t][k + 4]), d[k + 4], 0, 0, 0);
          }
        }
        if constexpr (k > 0) {
          const f32x2 x = __builtin_elementwise_fma(d[k - 1].zw, wt.zw, d[k - 1].xy * wt.xy), y = __builtin_elementwise_fma(d[k + 3].zw, wt.zw, d[k + 3].xy * wt.xy);
          v4[k - 1] = x16_sum(rs32(x[0] + x[1], y[0] + y[1]));
        }
        SCHED();
      });
      st16c(rs, yoff, u32x4{pk_bf(v4[0], v4[1]), tg.hi, pk_bf(v4[2], v4[3]), zw});
    }
    STAMP(2);
    STAMP_FLUSH(1); TR_FLUSH();
    return;
  }
  u32x4 Wa[16], Wb[16], xv[4];
  unsigned pf;
  u64 epw, tstart;
  {
    unsigned oa[16];
    oa[0] = (cls & 1) ? (w4 * 128 + 16 * (lane >> 3)) * 8192 + idx * 128 + 16 * (lane & 7) : (cls == 2 ? (b * 256 + w4 * 64 + 16 * r4) * 8192 + h * 256 + 16 * c16 : (idx * 128 + w4) * 1024 + 16 * lane);
#pragma unroll
    for (int i = 1; i < 16; ++i) oa[i] = oa[i - 1] + (cls ? 8192 : 4096);
    unsigned op = (((blk >> 5) * 4 + w4) * 16 + c16) * 8192 + ((idx & 56) + 3 + (cls == 0 ? -1 : (idx >= 56 ? 4 : 0)) * (lane >> 5)) * 128 + (r4 & 1) * 64;
    unsigned ox = w4 * 256 + 32 * (lane >> 3);
#pragma unroll
    for (int i = 0; i < 16; i += 8) asm volatile("" : "+v"(oa[i]), "+v"(oa[i + 1]), "+v"(oa[i + 2]), "+v"(oa[i + 3]), "+v"(oa[i + 4]), "+v"(oa[i + 5]), "+v"(oa[i + 6]), "+v"(oa[i + 7]));
    asm volatile("" : "+v"(op), "+v"(ox), "+s"(p0), "+s"(p1), "+s"(p2), "+s"(p3));
    SCHED();
    tstart = wall_clock64();
    SCHED();
    pf = *reinterpret_cast<const unsigned*>((const char*)p1 + op);
    epw = __hip_atomic_load((gu64*)p3, __ATOMIC_RELAXED, __HIP_MEMORY_SCOPE_AGENT);
#pragma unroll
    for (int i = 0; i < 4; ++i) xv[i] = ld16((const char*)p2 + ox + (i >> 1) * 1024 + 16 * (i & 1));
    SCHED();
#pragma unroll
    for (int i = 0; i < 16; ++i) { Wa[i] = ld16nt((const char*)p0 + oa[i]); SCHED(); }
  }
  char* ws = (char*)p3;
  const rsrc_t rs = __builtin_amdgcn_make_buffer_rsrc(ws, 0, (int)WS_END, 0x00020000);
  u16* out = (u16*)p5;
  {
    rawbar();
    const u64 t64 = (((u64)(unsigned)__builtin_amdgcn_readfirstlane((unsigned)(epw >> 32)) << 32) | (unsigned)__builtin_amdgcn_readfirstlane((unsigned)epw)) + GOLD;
    tg.hi = (unsigned)(t64 >> 32); tg.lo = (unsigned)t64;
    asm volatile("" : "+s"(tg.hi), "+s"(tg.lo) : "v"(pf) : "memory");
    if (w4 == 0) { cnt[13] = tg.hi; cnt[14] = tg.lo; COMPILER_FENCE(); lds_add(cnt + 15, 1u, lane); }
    STAMP(13);
  }
  float* red = reinterpret_cast<float*>(smem);
  auto gemv = [&](float* dst) {
    float acc[2][8];
#pragma unroll
    for (int bb = 0; bb < 2; ++bb)
#pragma unroll
      for (int k = 0; k < 8; ++k) acc[bb][k] = 0.f;
#pragma unroll
    for (int ip = 0; ip < 8; ++ip) {
      const unsigned xp0 = xv[ip >> 2][ip & 3], xp1 = xv[2 + (ip >> 2)][ip & 3];
#pragma unroll
      for (int k = 0; k < 4; ++k) {
        const unsigned lo = plo(Wa[2 * ip][k], Wa[2 * ip + 1][k]), hi = phi(Wa[2 * ip][k], Wa[2 * ip + 1][k]);
        acc[0][2 * k] = dot2(lo, xp0, acc[0][2 * k]); acc[0][2 * k + 1] = dot2(hi, xp0, acc[0][2 * k + 1]);
        acc[1][2 * k] = dot2(lo, xp1, acc[1][2 * k]); acc[1][2 * k + 1] = dot2(hi, xp1, acc[1][2 * k + 1]);
      }
    }
    float v8[8], v4[4];
#pragma unroll
    for (int k = 0; k < 8; ++k) v8[k] = rs32(acc[0][k], acc[1][k]);
#pragma unroll
    for (int k = 0; k < 4; ++k) { const float t = rs16(v8[k], v8[k + 4]); v4[k] = t + dpp<0x128>(t); }
    *reinterpret_cast<f32x4*>(dst + (lane >> 5) * 64 + 8 * (lane & 7) + 4 * (r4 & 1)) = f32x4{v4[0], v4[1], v4[2], v4[3]};
    COMPILER_FENCE();
  };
  if (cls & 1) {
    gemv(red + w4 * 128);
    const unsigned got = lds_arrive_nw(L_CNT + 8);
    f32x4 t4[4];
#pragma unroll
    for (int w = 0; w < 4; ++w) t4[w] = *reinterpret_cast<const f32x4*>(red + w * 128 + 4 * (lane & 31));
    SCHED();
    if (lds_done(got) == 3u) {
      const f32x4 t = (t4[0] + t4[1]) + (t4[2] + t4[3]);
      st16c(rs, lane < 32 ? WS_QX + ((((cls >> 1) * 2 + r4) * 32 + h) * 64 + b * 32 + 2 * c16) * 8 : OOB, u32x4{pk_bf(t[0], t[1]), tg.hi, pk_bf(t[2], t[3]), tg.hi});
      SCHED();
      lds_add(cnt + 2, 1u, lane);
    } else lds_wait(cnt + 2, 5u);
    STAMP(8);
  } else if (cls == 2) {
    while (wall_clock64() - tstart < TQX) __builtin_amdgcn_s_sleep(4);
    u32x4 q0, q1;
    const unsigned qo = WS_QX + (h * 64 + 4 * c16) * 8;
#pragma unroll 1
    for (int spin = 0; spin < SPIN_MAX; ++spin) {
      COMPILER_FENCE();
      const u32x4 g0 = ld16c(rs, qo), g1 = ld16c(rs, qo + 16), g2 = ld16c(rs, qo + 16384), g3 = ld16c(rs, qo + 16400);
      q0 = u32x4{g0[0], g0[2], g1[0], g1[2]}; q1 = u32x4{g2[0], g2[2], g3[0], g3[2]};
      if (__all((g0[1] == tg.hi) & (g0[3] == tg.hi) & (g1[1] == tg.hi) & (g1[3] == tg.hi) & (g2[1] == tg.hi) & (g2[3] == tg.hi) & (g3[1] == tg.hi) & (g3[3] == tg.hi))) break;
      __builtin_amdgcn_s_sleep(1);
    }
    STAMP(9);
    float v[4];
    {
      float u[16], t8[8];
#pragma unroll
      for (int i = 0; i < 16; ++i) {
        float a = 0.f, c = 0.f;
#pragma unroll
        for (int k = 0; k < 4; ++k) { a = dot2(Wa[i][k], q0[k], a); c = dot2(Wa[i][k], q1[k], c); }
        u[i] = (c16 & 4 ? c : a) + dpp<0x141>(c16 & 4 ? a : c);
        asm volatile("" : "+v"(u[i]));
      }
#pragma unroll
      for (int i = 0; i < 8; ++i) { t8[i] = (c16 & 2 ? u[i + 8] : u[i]) + dpp<0x4E>(c16 & 2 ? u[i] : u[i + 8]); asm volatile("" : "+v"(t8[i])); }
#pragma unroll
      for (int i = 0; i < 4; ++i) {
        float t = (c16 & 1 ? t8[i + 4] : t8[i]) + dpp<0xB1>(c16 & 1 ? t8[i] : t8[i + 4]);
        asm volatile("" : "+v"(t));
        v[i] = t + dpp<0x128>(t);
      }
    }
    st16c(rs, c16 < 8 ? WS_QA + ((c16 >> 2) * 32 + h) * 2048 + (b * 256 + w4 * 64 + 16 * r4 + 4 * (c16 & 3)) * 4 : OOB, u32x4{pk_bf(v[0], v[1]), tg.hi, pk_bf(v[2], v[3]), tg.hi});
    STAMP(10);
  }
  if (cls >= 2) {
    const unsigned tw = __builtin_nontemporal_load(reinterpret_cast<const unsigned*>(out + (idx >> 5) * 1024 + (cls & 1) * 512 + 16 * (idx & 31)));
    asm volatile("" :: "v"(tw));
  }
  asm volatile("" :: "v"(xv[0]), "v"(xv[1]), "v"(xv[2]), "v"(xv[3]));
  SCHED();
  if (cls >= 2) {
    unsigned d0 = (cls == 3 ? WS_GB + (idx >> 5) * 128 * 2048 : WS_GA + (idx >> 5) * 64 * 2048) + (16 * w4 + (lane >> 2)) * 2048 + (idx & 31) * 64 + (lane & 3) * 16;
    unsigned d1 = cls == 3 ? d0 + 64 * 2048 : d0;
    unsigned fw = (lane & 12) ? 32768 + 256 + lane * 4 : 32768 + (w4 * 16 + 4 * (lane & 3) + r4) * 4, fr = 32768 + 8 * (lane & 7);
    unsigned oo = lane < 8 ? ((idx >> 5) * 1024 + (cls & 1) * 512 + 16 * (idx & 31) + 2 * lane) * 2 : OOB;
    float f1 = cls == 3 ? 1.f : 2.f;
    unsigned zs = 8 * (lane & 3);
    const rsrc_t ro = __builtin_amdgcn_make_buffer_rsrc(out, 0, 4096, 0x00020000);
    asm volatile("" : "+v"(d0), "+v"(d1), "+v"(fw), "+v"(fr), "+v"(oo), "+v"(f1), "+v"(zs));
    lds_wait(cnt + 0, 4u);
    if (TGA && cls == 2) { while (wall_clock64() - tstart < TGA) __builtin_amdgcn_s_sleep(2); }
    STAMP(3);
    u32x4 x, y;
    bool ok = false;
#pragma unroll 1
    for (int spin = 0; spin < SPIN_MAX; ++spin) {
      COMPILER_FENCE();
      if (!ok) { x = ld16c(rs, d0); y = ld16c(rs, d1); }
      ok = (((x[1] ^ tg.hi) | (y[1] ^ tg.hi)) | (((x[3] ^ tg.lo) | (y[3] ^ tg.lo)) >> 8)) == 0u;
      if (__all(ok)) break;
    }
    STAMP(14);
    float w2[2];
#pragma unroll
    for (int j = 0; j < 2; ++j) {
      unsigned zb = ((j ? y[3] : x[3]) & 0xffu) << zs;
      zb |= __float_as_uint(dpp<0xB1>(__uint_as_float(zb))); zb |= __float_as_uint(dpp<0x4E>(__uint_as_float(zb)));
      const float z = __uint_as_float(zb);
      float Z = fmaxf(z, dpp<0x124>(z)); Z = fmaxf(Z, dpp<0x128>(Z));
      const float e = ex2(z - Z);
      float L = e + dpp<0x124>(e); L += dpp<0x128>(L);
      w2[j] = e * f1 * __builtin_amdgcn_rcpf(L);
    }
    float s = rs16(rs32(fmaf(w2[1], bflo(y[0]), w2[0] * bflo(x[0])), fmaf(w2[1], bflo(y[2]), w2[0] * bflo(x[2]))), rs32(fmaf(w2[1], bfhi(y[0]), w2[0] * bfhi(x[0])), fmaf(w2[1], bfhi(y[2]), w2[0] * bfhi(x[2]))));
    s += dpp<0x124>(s); s += dpp<0x128>(s);
    *reinterpret_cast<float*>(smem + fw) = s;
    COMPILER_FENCE();
    const unsigned got = lds_arrive_nw(L_CNT + 24);
    f32x2 t[4];
#pragma unroll
    for (int w = 0; w < 4; ++w) t[w] = *reinterpret_cast<const f32x2*>(smem + fr + w * 64);
    SCHED();
    if (lds_done(got) == 3u) {
      const f32x2 r = (t[0] + t[1]) + (t[2] + t[3]);
      __builtin_amdgcn_raw_buffer_store_b32(pk_bf(r[0], r[1]), ro, (int)oo, 0, 0);
    }
    STAMP(6);
    STAMP_FLUSH(0); TR_FLUSH();
    return;
  }
  if (cls == 0) {
    {
      const u16* pb = (const u16*)p0 + (idx * 128 + 64 + w4) * 512 + 8 * lane;
#pragma unroll
      for (int i = 0; i < 16; ++i) {
        Wb[i] = ld16nt(pb + i * 2048);
        if (i >= 4 && (i & 1)) asm volatile("" : "+v"(Wb[i - 4]));
        if (i == 9) TR();
        SCHED();
      }
    }
    if (TQA) { while (wall_clock64() - tstart < TQA) __builtin_amdgcn_s_sleep(2); }
    lds_add(cnt + 8, 1u, lane);
    char* scr = smem + L_SCR;
    const int ab = idx >> 5, r = lane & 31, hh = lane >> 5;
#pragma unroll
    for (int i = 0; i < 16; ++i) *reinterpret_cast<u32x4*>(smem + ((i >> 3) * 4 + r4) * 8192 + toff(4 * (i & 7) + w4, c16)) = Wa[i];
#pragma unroll
    for (int i = 0; i < 16; ++i) *reinterpret_cast<u32x4*>(smem + ((2 + (i >> 3)) * 4 + r4) * 8192 + toff(4 * (i & 7) + w4, c16)) = Wb[i];
    STAMP(8);
    TR();
    u32x4 qlast = {0u, 0u, 0u, 0u};
    {
      u32x4 g[18];
      const int fl = (idx & 1) * 8;
      const unsigned o8 = WS_QA + (ab * 32 + w4 * 8) * 2048 + lane * 16, ol = WS_QA + (ab * 32 + r) * 2048 + 1984 + 32 * hh;
      unsigned pend = 0x3ffffu;
#pragma unroll 1
      for (int spin = 0; spin < SPIN_MAX && pend; ++spin) {
        COMPILER_FENCE();
#pragma unroll
        for (int i = 0; i < 18; ++i) g[i] = ld16c(rs, (pend >> i & 1) ? (i < 16 ? o8 + (i ^ fl) * 1024 : ol + 16 * (i - 16)) : OOB);
        if (spin == 0) { SCHED(); lds_add(cnt + 8, ~0u, lane); SCHED(); }
#pragma unroll
        for (int i = 0; i < 18; ++i) {
          if ((pend >> i & 1) && __all((g[i][1] == tg.hi) & (g[i][3] == tg.hi))) {
            pend &= ~(1u << i);
            if (i >= 16) { qlast[2 * (i - 16)] = g[i][0]; qlast[2 * (i - 16) + 1] = g[i][2]; }
            else if (!(i & 1) || lane < 60) *reinterpret_cast<u32x2*>(scr + (w4 * 8 + ((i ^ fl) >> 1)) * 1008 + (i & 1) * 512 + 8 * lane) = u32x2{g[i][0], g[i][2]};
          }
        }
        if (pend) __builtin_amdgcn_s_sleep(8);
      }
    }
    sbar4(cnt + 3, lane);
    STAMP(10);
    f32x16 sacc;
#pragma unroll
    for (int i = 0; i < 16; ++i) sacc[i] = 0.f;
#pragma unroll
    for (int jj = 0; jj < 32; ++jj) {
      const u32x4 af = *reinterpret_cast<const u32x4*>(smem + (w4 * 4 + (jj >> 3)) * 8192 + toff(r, 2 * (jj & 7) + hh));
      const u32x4 bf = jj < 31 ? *reinterpret_cast<const u32x4*>(scr + r * 1008 + 16 * (2 * (jj < 31 ? jj : 0) + hh)) : qlast;
      sacc = __builtin_amdgcn_mfma_f32_32x32x16_bf16(__builtin_bit_cast(bf16x8_t, af), __builtin_bit_cast(bf16x8_t, bf), sacc, 0, 0, 0);
    }
    float m = sacc[0];
#pragma unroll
    for (int i = 1; i < 16; ++i) m = fmaxf(m, sacc[i]);
    m = xhalf_max(m);
    if (hh == 0) *reinterpret_cast<float*>(scr + r * 1008 + 992 + 4 * w4) = m;
    sbar4(cnt + 4, lane);
    STAMP(11);
    {
      const f32x4 m4 = *reinterpret_cast<const f32x4*>(scr + r * 1008 + 992);
      m = fmaxf(fmaxf(m4[0], m4[1]), fmaxf(m4[2], m4[3]));
    }
    const float mc = m * C2;
    float p[16], ls = 0.f;
#pragma unroll
    for (int i = 0; i < 16; ++i) { p[i] = ex2(fmaf(sacc[i], C2, -mc)); ls += p[i]; }
    ls = xhalf_sum(ls);
    sbar4(cnt + 5, lane);
    u32x4* pf = reinterpret_cast<u32x4*>(scr);
    float* wl = reinterpret_cast<float*>(scr + 8192);
    if (hh == 0) wl[r * 4 + w4] = ls;
    pf[(2 * w4) * 64 + lane] = u32x4{pk_bf(p[0], p[1]), pk_bf(p[2], p[3]), pk_bf(p[4], p[5]), pk_bf(p[6], p[7])};
    pf[(2 * w4 + 1) * 64 + lane] = u32x4{pk_bf(p[8], p[9]), pk_bf(p[10], p[11]), pk_bf(p[12], p[13]), pk_bf(p[14], p[15])};
    sbar4(cnt + 6, lane);
    u32x4 pb8[8];
#pragma unroll
    for (int ks = 0; ks < 8; ++ks) pb8[ks] = pf[ks * 64 + lane];
    const f32x4 l4 = *reinterpret_cast<const f32x4*>(wl + r * 4);
    const int qq = (lane & 15) >> 2, pp = lane & 3, blk2 = (lane >> 4) & 1;
    char* ot = scr + 12288 + w4 * 4096;
#pragma unroll
    for (int c2 = 0; c2 < 2; ++c2) {
      f32x16 oacc[2];
#pragma unroll
      for (int ct = 0; ct < 2; ++ct)
#pragma unroll
        for (int i = 0; i < 16; ++i) oacc[ct][i] = 0.f;
#pragma unroll
      for (int ks = 0; ks < 8; ++ks)
#pragma unroll
        for (int ct = 0; ct < 2; ++ct) {
          typedef __attribute__((address_space(3))) s16x4* lp;
          const char* tile = smem + ((ks >> 1) * 4 + w4) * 8192;
          const int cc = 2 * c2 + ct;
          const s16x4 lo = __builtin_amdgcn_ds_read_tr16_b64_v4i16((lp)(tile + toff(16 * (ks & 1) + 4 * hh + qq, 4 * cc + 2 * blk2 + (pp >> 1)) + 8 * (pp & 1)));
          const s16x4 hi = __builtin_amdgcn_ds_read_tr16_b64_v4i16((lp)(tile + toff(16 * (ks & 1) + 8 + 4 * hh + qq, 4 * cc + 2 * blk2 + (pp >> 1)) + 8 * (pp & 1)));
          const s16x8 afr = __builtin_shufflevector(lo, hi, 0, 1, 2, 3, 4, 5, 6, 7);
          oacc[ct] = __builtin_amdgcn_mfma_f32_32x32x16_bf16(__builtin_bit_cast(bf16x8_t, afr), __builtin_bit_cast(bf16x8_t, pb8[ks]), oacc[ct], 0, 0, 0);
        }
#pragma unroll
      for (int ct = 0; ct < 2; ++ct)
#pragma unroll
        for (int g = 0; g < 4; ++g)
          *reinterpret_cast<u32x2*>(ot + r * 128 + 16 * ((4 * ct + g) ^ (r & 7)) + 8 * hh) = u32x2{pk_bf(oacc[ct][4 * g], oacc[ct][4 * g + 1]), pk_bf(oacc[ct][4 * g + 2], oacc[ct][4 * g + 3])};
      if (c2 == 0) lds_add(cnt + 8, 1u, lane);
      __builtin_amdgcn_wave_barrier();
#pragma unroll
      for (int i = 0; i < 4; ++i) {
        const int hd = 8 * i + (lane >> 3), pc = lane & 7;
        const u32x4 vv = *reinterpret_cast<const u32x4*>(ot + hd * 128 + 16 * (pc ^ (hd & 7)));
        st16c(rs, WS_APO + ((APL ? idx * 32 + hd : hd * 64 + idx) * 512 + w4 * 128 + 64 * c2 + 8 * pc) * 2, vv);
      }
      __builtin_amdgcn_wave_barrier();
    }
    lds_arrive(cnt + 1, lane);
    STAMP(1);
    asm volatile("s_waitcnt vmcnt(0)" ::: "memory");
    if (lds_arrive(cnt + 9, lane) == 3u && hh == 0)
      st16c(rs, WS_AML + (idx * 32 + r) * 16, u32x4{__float_as_uint(m), tg.hi, __float_as_uint((l4[0] + l4[1]) + (l4[2] + l4[3])), tg.hi});
    lds_add(cnt + 8, ~0u, lane);
    STAMP(2);
    if (idx == 0 && w4 == 0) {
      lds_wait(cnt + ADV, 4u);
#pragma unroll 1
      for (int spin = 0; spin < SPIN_MAX; ++spin) {
        COMPILER_FENCE();
        const u32x2 t0 = ld8c(rs, WS_AML + lane * 512), t1 = ld8c(rs, WS_QX + (64 * 64 + lane * 32) * 8);
        if (__all((t0[1] == tg.hi) & (t1[1] == tg.hi))) break;
        __builtin_amdgcn_s_sleep(8);
      }
      if (lane == 0) __hip_atomic_store((gu64*)(ws + WS_EP), ((u64)tg.hi << 32) | tg.lo, __ATOMIC_RELAXED, __HIP_MEMORY_SCOPE_AGENT);
    }
    STAMP_FLUSH(0); TR_FLUSH();
    return;
  }
  if (cls == 1) {
    {
      const u16* pa = (const u16*)p1 + (w4 * 128 + 16 * (lane >> 3)) * 4096 + h * 128 + b * 64 + 8 * (lane & 7);
      const u16* pb = (const u16*)p4 + (h * 128 + b * 64 + w4 * 2) * 512 + 8 * lane;
#pragma unroll
      for (int i = 0; i < 16; ++i) {
        Wa[i] = ld16nt(pa + i * 4096);
        SCHED();
        if ((i & 1) && !((idx & 7) == 3 || idx == 63)) asm volatile("s_waitcnt vmcnt(6)" ::: "memory");
        if ((i & 7) == 7) TR();
        SCHED();
      }
#pragma unroll
      for (int i = 0; i < 16; ++i) {
        Wb[i] = ld16nt(pb + (i >> 1) * 4096 + (i & 1) * 512);
        SCHED();
        if (i & 1) asm volatile("s_waitcnt vmcnt(6)" ::: "memory");
        if ((i & 7) == 7) TR();
        SCHED();
      }
    }
    float* red2 = red + 2048;
    float* wsh = red + 3072;
    if (w4 == 0) {
      const unsigned fo = WS_AML + (lane * 32 + h) * 16;
      while (wall_clock64() - tstart < TAML) __builtin_amdgcn_s_sleep(8);
      u32x4 g;
      bool ok = false;
#pragma unroll 1
      for (int spin = 0; spin < SPIN_MAX; ++spin) {
        COMPILER_FENCE();
        if (!ok) g = ld16c(rs, fo);
        ok = (g[1] == tg.hi) & (g[3] == tg.hi);
        if (__all(ok)) break;
        __builtin_amdgcn_s_sleep(2);
      }
      lds_add(cnt + 7, 1u, lane);
      const float mu = __uint_as_float(g[0]), lu = __uint_as_float(g[2]);
      const float M = x16_max(row16_max(mu));
      const float wl = ex2((mu - M) * C2);
      wsh[lane] = wl / x16_sum(row16_sum(wl * lu));
      lds_arrive(cnt + 7, lane);
    } else lds_wait(cnt + 7, 1u);
    COMPILER_FENCE();
    STAMP(3);
    u32x4 pp[16];
#pragma unroll
    for (int i = 0; i < 16; ++i) { pp[i] = ld16c(rs, WS_APO + (APL ? (w4 * 16 + (i ^ (8 * b))) * 32 + h : h * 64 + w4 * 16 + (i ^ (8 * b))) * 1024 + lane * 16); SCHED(); }
    lds_wait(cnt + 7, 2u);
    float a8[8];
#pragma unroll
    for (int k = 0; k < 8; ++k) a8[k] = 0.f;
    const float wv = wsh[w4 * 16 + (c16 ^ (8 * b))];
#pragma unroll
    for (int i = 0; i < 16; ++i) {
      const float wt = rdlane(wv, i);
#pragma unroll
      for (int k = 0; k < 4; ++k) { a8[2 * k] = fmaf(wt, bflo(pp[i][k]), a8[2 * k]); a8[2 * k + 1] = fmaf(wt, bfhi(pp[i][k]), a8[2 * k + 1]); }
      asm volatile("" : "+v"(a8[0]), "+v"(a8[1]), "+v"(a8[2]), "+v"(a8[3]), "+v"(a8[4]), "+v"(a8[5]), "+v"(a8[6]), "+v"(a8[7]));
      SCHED();
    }
    *reinterpret_cast<f32x4*>(red + w4 * 512 + 8 * lane) = f32x4{a8[0], a8[1], a8[2], a8[3]};
    *reinterpret_cast<f32x4*>(red + w4 * 512 + 8 * lane + 4) = f32x4{a8[4], a8[5], a8[6], a8[7]};
    sbar4(cnt + 3, lane);
    STAMP(4);
#pragma unroll
    for (int i = 0; i < 4; ++i) {
      const float* src = red + (i >> 1) * 1024 + w4 * 128 + 16 * (lane >> 3) + 8 * (i & 1);
      const f32x4 t0 = *reinterpret_cast<const f32x4*>(src) + *reinterpret_cast<const f32x4*>(src + 512), t1 = *reinterpret_cast<const f32x4*>(src + 4) + *reinterpret_cast<const f32x4*>(src + 516);
      xv[i] = u32x4{pk_bf(t0[0], t0[1]), pk_bf(t0[2], t0[3]), pk_bf(t1[0], t1[1]), pk_bf(t1[2], t1[3])};
    }
    gemv(red2 + w4 * 128);
    sbar4(cnt + 4, lane);
    STAMP(12);
    unsigned op;
    {
      f32x2 t = {0.f, 0.f};
#pragma unroll
      for (int w = 0; w < 4; ++w) t += *reinterpret_cast<const f32x2*>(red2 + w * 128 + (lane >> 5) * 64 + 8 * (lane & 7) + 2 * w4);
      op = pk_bf(t[0], t[1]);
    }
    float* red3 = red + 4096;
    float a[2][8];
#pragma unroll
    for (int bb = 0; bb < 2; ++bb)
#pragma unroll
      for (int k = 0; k < 8; ++k) a[bb][k] = 0.f;
#pragma unroll
    for (int ip = 0; ip < 8; ++ip) {
      const unsigned v0 = __builtin_amdgcn_readlane(op, ip), v1 = __builtin_amdgcn_readlane(op, 32 + ip);
#pragma unroll
      for (int k = 0; k < 4; ++k) {
        const unsigned lo = plo(Wb[2 * ip][k], Wb[2 * ip + 1][k]), hi = phi(Wb[2 * ip][k], Wb[2 * ip + 1][k]);
        a[0][2 * k] = dot2(lo, v0, a[0][2 * k]); a[0][2 * k + 1] = dot2(hi, v0, a[0][2 * k + 1]);
        a[1][2 * k] = dot2(lo, v1, a[1][2 * k]); a[1][2 * k + 1] = dot2(hi, v1, a[1][2 * k + 1]);
      }
    }
#pragma unroll
    for (int bb = 0; bb < 2; ++bb) {
      *reinterpret_cast<f32x4*>(red3 + (w4 * 2 + bb) * 512 + 8 * lane) = f32x4{a[bb][0], a[bb][1], a[bb][2], a[bb][3]};
      *reinterpret_cast<f32x4*>(red3 + (w4 * 2 + bb) * 512 + 8 * lane + 4) = f32x4{a[bb][4], a[bb][5], a[bb][6], a[bb][7]};
    }
    sbar4(cnt + 5, lane);
    {
      f32x4 t = {0.f, 0.f, 0.f, 0.f};
#pragma unroll
      for (int w = 0; w < 4; ++w) t += *reinterpret_cast<const f32x4*>(red3 + w * 1024 + 4 * tid);
      st16c(rs, WS_GA + ((tid >> 7) * 64 + idx) * 2048 + (tid & 127) * 16, u32x4{pk_bf(t[0], t[1]), tg.hi, pk_bf(t[2], t[3]), tg.lo & 0xffffff00u});
    }
    STAMP(5);
    STAMP_FLUSH(0); TR_FLUSH();
    if (PADN) asm volatile("s_endpgm\n\t.fill %0,4,0xBF800000" :: "n"(PADN));
    return;
  }
}

extern "C" void kernel_launch(void* const* d_in, const int* in_sizes, int n_in, void* d_out, int out_size, void* d_ws, size_t ws_size, hipStream_t stream) {
  const int sel[5][3] = {{1, 3, 6}, {2, 4, 0}, {3, 2, 6}, {9, 9, 6}, {7, 8, 10}};
  Args a;
  for (int r = 0; r < 5; ++r) {
    for (int c = 0; c < 3; ++c) a.t[r][c] = (gptr)d_in[sel[r][c]];
    a.t[r][3] = (gptr)d_ws;
  }
  a.w_o = (gptr)d_in[5];
  a.out = (gptr)d_out;
  kmain<<<256, 512, 0, stream>>>(a);
}
```

```cpp
#include <hip/hip_runtime.h>
#include <hip/hip_bf16.h>
#include <stdint.h>

#define DI __device__ __forceinline__
typedef __attribute__((ext_vector_type(4))) unsigned u32x4;
typedef __attribute__((ext_vector_type(2))) unsigned u32x2;
typedef __attribute__((ext_vector_type(8))) unsigned u32x8;
typedef __attribute__((ext_vector_type(16))) __bf16 bf16x16_t;
typedef __attribute__((ext_vector_type(4))) float f32x4;
typedef __attribute__((ext_vector_type(2))) float f32x2;
typedef __attribute__((ext_vector_type(16))) float f32x16;
typedef __attribute__((ext_vector_type(8))) __bf16 bf16x8_t;
typedef __attribute__((ext_vector_type(2))) __bf16 bf16x2_t;
typedef __attribute__((ext_vector_type(4))) short s16x4;
typedef __attribute__((ext_vector_type(8))) short s16x8;
typedef unsigned short u16;
typedef unsigned long long u64;

#define SHARES 51, 51, 76, 78
constexpr int SH[4] = {SHARES};
constexpr int NR[4] = {8, 8, 10, 10};
static_assert(SH[0] + SH[1] + SH[2] + SH[3] == 256 && SH[0] <= 64 && SH[1] <= 64 && SH[2] <= 80 && SH[3] <= 80 && SH[0] >= 8 && SH[1] >= 8, "every key once");
#define TQX 430
#define TAML 1590
#define TGA 0
#define APL 0
#define AHW 0
#define ROT 1
#define ADV 10
#define SDL 0
#define TQA 0
#define PP 0
#define PSL 4
#define NOTW 1
#define FST 11
#define TSL 1
#define PADN 230
constexpr float C2 = 0.08838834764831845f * 1.4426950408889634f;
constexpr u64 GOLD = 0x9E3779B97F4A7C15ull;

constexpr unsigned WS_EP = 0;
constexpr unsigned WS_QX = 4096;
constexpr unsigned WS_QA = WS_QX + 2 * 2 * 32 * 64 * 8;
constexpr unsigned WS_AML = WS_QA + 2 * 32 * 256 * 8;
constexpr unsigned WS_APO = WS_AML + 64 * 32 * 16;
constexpr unsigned WS_GB = WS_APO + 32 * 64 * 512 * 2;
constexpr unsigned WS_GA = WS_GB + 2 * 128 * 128 * 16;
constexpr unsigned WS_GZ = WS_GA + 2 * 64 * 128 * 16;
constexpr unsigned WS_END = WS_GZ + 256 * 128 + 128;
#define STAMP(i) do {} while (0)
#define STAMP_DECL do {} while (0)
#define STAMP_FLUSH(grp) do {} while (0)

#define TR_DECL do {} while (0)
#define TR() do {} while (0)
#define TR_FLUSH() do {} while (0)
DI float bflo(unsigned u) { return __uint_as_float(u << 16); }
DI float bfhi(unsigned u) { return __uint_as_float(u & 0xffff0000u); }
DI unsigned pk_bf(float a, float b) { const f32x2 v = {a, b}; return __builtin_bit_cast(unsigned, __builtin_convertvector(v, bf16x2_t)); }
DI float dot2(unsigned k, unsigned q, float acc) {
  return __builtin_amdgcn_fdot2_f32_bf16(__builtin_bit_cast(bf16x2_t, k), __builtin_bit_cast(bf16x2_t, q), acc, false);
}
DI unsigned plo(unsigned w0, unsigned w1) { return __builtin_amdgcn_perm(w1, w0, 0x05040100u); }
DI unsigned phi(unsigned w0, unsigned w1) { return __builtin_amdgcn_perm(w1, w0, 0x07060302u); }
template <int CTRL> DI float dpp(float x) {
  return __builtin_bit_cast(float, __builtin_amdgcn_mov_dpp(__builtin_bit_cast(int, x), CTRL, 0xf, 0xf, true));
}
DI float row16_sum(float x) { x += dpp<0xB1>(x); x += dpp<0x4E>(x); x += dpp<0x141>(x); x += dpp<0x128>(x); return x; }
DI float row16_max(float x) { x = fmaxf(x, dpp<0xB1>(x)); x = fmaxf(x, dpp<0x4E>(x)); x = fmaxf(x, dpp<0x141>(x)); x = fmaxf(x, dpp<0x128>(x)); return x; }
DI float x16_sum(float x) { auto s = __builtin_amdgcn_permlane16_swap(__float_as_uint(x), __float_as_uint(x), false, false); return __uint_as_float(s[0]) + __uint_as_float(s[1]); }
DI float x16_max(float x) { auto s = __builtin_amdgcn_permlane16_swap(__float_as_uint(x), __float_as_uint(x), false, false); return fmaxf(__uint_as_float(s[0]), __uint_as_float(s[1])); }
DI float xhalf_max(float x) { auto t = __builtin_amdgcn_permlane32_swap(__float_as_uint(x), __float_as_uint(x), false, false); return fmaxf(__uint_as_float(t[0]), __uint_as_float(t[1])); }
DI float xhalf_sum(float x) { auto t = __builtin_amdgcn_permlane32_swap(__float_as_uint(x), __float_as_uint(x), false, false); return __uint_as_float(t[0]) + __uint_as_float(t[1]); }
DI u32x4 ld16(const void* p) { return *reinterpret_cast<const u32x4*>(p); }
DI u32x4 ld16nt(const void* p) { return __builtin_nontemporal_load(reinterpret_cast<const u32x4*>(p)); }
DI float ex2(float x) { return __builtin_amdgcn_exp2f(x); }
#define SCHED() __builtin_amdgcn_sched_barrier(0)

typedef __attribute__((address_space(1))) u64 gu64;
typedef __amdgpu_buffer_rsrc_t rsrc_t;
DI u32x4 ld16c(rsrc_t rs, unsigned off) { return __builtin_amdgcn_raw_buffer_load_b128(rs, (int)off, 0, 16); }
DI u32x2 ld8c(rsrc_t rs, unsigned off) { return __builtin_amdgcn_raw_buffer_load_b64(rs, (int)off, 0, 16); }
DI void st8c(rsrc_t rs, unsigned off, u32x2 v) { __builtin_amdgcn_raw_buffer_store_b64(v, rs, (int)off, 0, 16); }
DI void st16c(rsrc_t rs, unsigned off, u32x4 v) { __builtin_amdgcn_raw_buffer_store_b128(v, rs, (int)off, 0, 16); }
constexpr int SPIN_MAX = 1 << 20;
constexpr unsigned OOB = 0x7ffffff0u;
DI void rawbar() { __builtin_amdgcn_sched_barrier(0); __builtin_amdgcn_s_barrier(); __builtin_amdgcn_sched_barrier(0); }
struct Tag { unsigned hi, lo; };
#define COMPILER_FENCE() asm volatile("" ::: "memory")

template <int N> struct IC { static constexpr int v = N; };
template <int I, int N, class F> DI void sfor(F&& f) { if constexpr (I < N) { f(IC<I>{}); sfor<I + 1, N>(f); } }
typedef __attribute__((address_space(3))) unsigned lu32;
DI void lds_wait(unsigned* cnt, unsigned target) {
#pragma unroll 1
  for (int spin = 0; spin < SPIN_MAX; ++spin) {
    if (__builtin_amdgcn_readfirstlane(__hip_atomic_load((lu32*)cnt, __ATOMIC_RELAXED, __HIP_MEMORY_SCOPE_WORKGROUP)) >= target) break;
    __builtin_amdgcn_s_sleep(1);
  }
  COMPILER_FENCE();
}
DI void lds_hold(unsigned* flag) {
#pragma unroll 1
  for (int spin = 0; spin < SPIN_MAX; ++spin) {
    if (__builtin_amdgcn_readfirstlane(__hip_atomic_load((lu32*)flag, __ATOMIC_RELAXED, __HIP_MEMORY_SCOPE_WORKGROUP)) == 0u) break;
    __builtin_amdgcn_s_sleep(2);
  }
}
DI unsigned lds_arrive_nw(unsigned addr) {
  unsigned old, one = 1u;
  asm volatile("s_mov_b64 exec, 1\n\tds_add_rtn_u32 %0, %1, %2\n\ts_mov_b64 exec, -1" : "=v"(old) : "v"(addr), "v"(one) : "memory");
  return old;
}
DI unsigned lds_done(unsigned old) { asm volatile("s_waitcnt lgkmcnt(0)" : "+v"(old) :: "memory"); return __builtin_amdgcn_readfirstlane(old); }
DI unsigned lds_arrive(unsigned* cnt, int) { return lds_done(lds_arrive_nw((unsigned)(size_t)(lu32*)cnt)); }
DI void lds_add(unsigned* cnt, unsigned v, int) {
  asm volatile("s_mov_b64 exec, 1\n\tds_add_u32 %0, %1\n\ts_mov_b64 exec, -1" :: "v"((unsigned)(size_t)(lu32*)cnt), "v"(v) : "memory");
}
DI void sbar4(unsigned* cnt, int lane) { if (lds_arrive(cnt, lane) < 3u) lds_wait(cnt, 4u); }
DI float rs32(float a, float b) { auto t = __builtin_amdgcn_permlane32_swap(__float_as_uint(a), __float_as_uint(b), false, false); return __uint_as_float(t[0]) + __uint_as_float(t[1]); }
DI float rs16(float a, float b) { auto t = __builtin_amdgcn_permlane16_swap(__float_as_uint(a), __float_as_uint(b), false, false); return __uint_as_float(t[0]) + __uint_as_float(t[1]); }
template <int N> DI unsigned rowbcast(unsigned x) { return (unsigned)__builtin_amdgcn_mov_dpp((int)x, 0x150 + N, 0xf, 0xf, true); }
DI void pin2(u32x4& a, u32x4& b) { asm volatile("" : "+v"(a), "+v"(b)); }
DI void pin2w(u32x8& a, u32x8& b) { asm volatile("" : "+v"(a), "+v"(b)); }
DI float rdlane(float x, int l) { return __builtin_bit_cast(float, __builtin_amdgcn_readlane(__builtin_bit_cast(int, x), l)); }

DI unsigned toff(unsigned row, unsigned ch) { return 256u * row + 16u * (ch ^ (((row & 3) << 2) | ((row >> 2) & 3))); }
constexpr int SMEM = 160 * 1024;
constexpr int L_SCR = 131072;
constexpr int L_MRG = 65536;
constexpr int L_CNT = L_SCR + 32 * 1008;
constexpr int L_ML = L_CNT + 64;

struct KV { u32x4 r[8]; };

typedef const __attribute__((address_space(1))) char* gptr;
struct Args { gptr t[5][4]; gptr w_o; gptr out; };

__global__ __launch_bounds__(512, 2) __attribute__((aligned(4096))) void kmain(Args a) {
  __shared__ __attribute__((aligned(16))) char smem[SMEM];
  const int tid = threadIdx.x, blk = blockIdx.x, lane = tid & 63;
  const int wave = __builtin_amdgcn_readfirstlane(tid >> 6), w4 = wave & 3;
  const int cls = (blk >> 3) & 3;
  const int arow = wave < 4 ? cls : 4;
  gptr p0 = a.t[arow][0], p1 = a.t[arow][1], p2 = a.t[arow][2], p3 = a.t[arow][3], p4 = a.w_o, p5 = a.out;
  SCHED();
  int idx = 8 * (blk & 7) + (blk >> 5);
  if (ROT && wave < 4) idx = (idx & ~7) | ((idx + (cls == 3 ? 2 : (cls == 2 ? 5 : 0))) & 7);
  const int h = idx >> 1, b = idx & 1;
  const int r4 = lane >> 4, c16 = lane & 15;
  unsigned* cnt = reinterpret_cast<unsigned*>(smem + L_CNT);
  STAMP_DECL;
  TR_DECL;
  STAMP(0);
  Tag tg;
  if (wave >= 4) {
    if (tid - 256 < 16) cnt[tid - 256] = 0u;
    const int nr = cls < 2 ? 8 : 10;
    unsigned tab;
    {
      int j = 2 * nr - 1 - lane;
      j = lane < nr ? lane : (j > 2 ? j : (j == 2 ? 0 : j + 1));
      int n = j + (h * nr >> 5);
      n = n >= nr ? n - nr : n;
      int off = 0, wd = 0;
#pragma unroll
      for (int d = 0; d < 4; ++d) {
        const int k = ((2 * n + 1) * NR[d] + (d < cls) - 1 + nr) * (nr == 8 ? 4096 : 3277) >> 16;
        off += k * (SH[d] / NR[d]) + min(k, SH[d] % NR[d]);
        wd = d == cls ? SH[d] / NR[d] + (n < SH[d] % NR[d]) : wd;
      }
      tab = lane < 2 * nr ? (unsigned)(4 * off + w4 * wd) << 10 | (unsigned)wd : 0u;
    }
    asm volatile("" : "+v"(tab));
    asm volatile("" : "+s"(p0), "+s"(p1), "+s"(p2), "+s"(p3));
    SCHED();
    char* ws = (char*)p3;
    const rsrc_t rs = __builtin_amdgcn_make_buffer_rsrc(ws, 0, (int)WS_END, 0x00020000);
    const char* kb = (const char*)p0 + ((size_t)(b * 32 + h) << 20);
    const char* vb = (const char*)p1 + ((size_t)(b * 32 + h) << 20);
    const unsigned lo = lane * 16u, lo2 = lo + 4096u;
    int nx = 0;
    auto nexts = [&]() {
      const unsigned e = __builtin_amdgcn_readlane(tab, nx);
      const rsrc_t r = __builtin_amdgcn_make_buffer_rsrc(const_cast<char*>((nx < nr ? kb : vb) + (e & ~1023u)), 0, (int)((e & 15u) << 10), 0x00020000);
      ++nx;
      return r;
    };
    const rsrc_t s0 = nexts();
    const int rot = cls;
    const char* wb = (const char*)p2 + h * 131072;
    unsigned wl = r4 * 8192 + w4 * 256 + c16 * 16;
    asm volatile("s_waitcnt lgkmcnt(0)" : "+v"(wl), "+v"(tab) :: "memory");
    if (SDL) __builtin_amdgcn_s_sleep(SDL);
    rawbar();
    KV A, B, X;
    u32x4 W[8][4];
    u32x8 T[2][8];
    auto ldp = [&](KV& S, auto P, rsrc_t q) {
      constexpr int p = P.v;
      S.r[2 * p] = __builtin_amdgcn_raw_buffer_load_b128(q, (int)((p < 2 ? lo : lo2) + (p & 1) * 2048), 0, 2);
      S.r[2 * p + 1] = __builtin_amdgcn_raw_buffer_load_b128(q, (int)((p < 2 ? lo : lo2) + (p & 1) * 2048 + 1024), 0, 2);
    };
    auto landed = [&](KV& S, auto P) { asm volatile("" : "+v"(S.r[2 * P.v]), "+v"(S.r[2 * P.v + 1])); };
    auto ldw = [&](auto G, auto J) { W[G.v][J.v] = ld16(wb + ((((G.v >> 1) + rot) & 3) * 32768 + (G.v & 1) * 4096) + wl + J.v * 1024); };
    auto conv = [&](auto G) {
      constexpr int t = G.v >> 2, j0 = (G.v & 3) * 2;
#pragma unroll
      for (int jj = 0; jj < 2; ++jj)
#pragma unroll
        for (int e = 0; e < 4; ++e) {
          const unsigned a_ = W[G.v][2 * jj][e], b_ = W[G.v][2 * jj + 1][e];
          T[t][2 * e][j0 + jj] = plo(a_, b_); T[t][2 * e + 1][j0 + jj] = phi(a_, b_);
        }
#pragma unroll
      for (int e = 0; e < 8; e += 2) pin2w(T[t][e], T[t][e + 1]);
    };
    const unsigned qo = WS_QX + (((2 + b) * 32 + h) * 64 + 4 * c16) * 8;
    u32x4 g0, g1;
    u32x2 tw;
    {
      const rsrc_t s1 = nexts(), s2 = nexts();
      sfor<0, 12>([&](auto I) {
        constexpr int i = I.v;
        if constexpr (i >= 4) { if constexpr (i < 8) landed(X, IC<i & 3>{}); else landed(B, IC<i & 3>{}); }
        if constexpr (i == 7 || i == 11) TR();
        if constexpr (i == 8) tw = ld8c(rs, lane == 0 ? (NOTW ? WS_END - 128 : WS_GB + (b * 128 + h * 4 + cls) * 2048 + 512 * w4) : OOB);
        if constexpr (i == 9) { g0 = ld16c(rs, qo); g1 = ld16c(rs, qo + 16); }
        if constexpr (i < 4) { ldp(X, IC<i & 3>{}, s0); ldw(IC<0>{}, IC<i & 3>{}); }
        else if constexpr (i < 8) { ldp(B, IC<i & 3>{}, s1); ldw(IC<1>{}, IC<i & 3>{}); }
        else { ldp(A, IC<i & 3>{}, s2); ldw(IC<2>{}, IC<i & 3>{}); }
        SCHED();
        if constexpr (i == 8) { conv(IC<0>{}); SCHED(); }
      });
    }
    lds_wait(cnt + 15, 1u);
    tg.hi = __builtin_amdgcn_readfirstlane(cnt[13]); tg.lo = __builtin_amdgcn_readfirstlane(cnt[14]);
    asm volatile("" :: "v"(tw));
    u32x4 qpk;
    {
      SCHED();
      conv(IC<1>{});
      SCHED();
      if (!__all((g0[1] == tg.hi) & (g0[3] == tg.hi) & (g1[1] == tg.hi) & (g1[3] == tg.hi))) {
#pragma unroll 1
        for (int spin = 0; spin < SPIN_MAX; ++spin) {
          COMPILER_FENCE();
          g0 = ld16c(rs, qo); g1 = ld16c(rs, qo + 16);
          if (__all((g0[1] == tg.hi) & (g0[3] == tg.hi) & (g1[1] == tg.hi) & (g1[3] == tg.hi))) break;
        }
      }
      qpk = u32x4{g0[0], g0[2], g1[0], g1[2]};
    }
    STAMP(7);
    const bool holds = cls == 0;
    float sc[10], acc[8];
#pragma unroll
    for (int k = 0; k < 10; ++k) sc[k] = 0.f;
    auto point = [&](KV& S, KV& N, rsrc_t sn, auto P, auto REFILL, auto AH, auto WG) {
      constexpr int ah = AH.v;
      if constexpr (REFILL.v) {
        if constexpr (P.v == 0 && ah > 0) { if (holds) lds_hold(cnt + 8); sfor<0, ah>([&](auto Q) { ldp(N, Q, sn); }); SCHED(); }
        landed(S, P);
        if constexpr (P.v + ah < 4) { if constexpr ((P.v & 1) == 0) { if (holds) lds_hold(cnt + 8); } ldp(N, IC<(P.v + ah) & 3>{}, sn); }
        if constexpr (WG.v >= 0) ldw(WG, P);
        SCHED();
      }
    };
    auto step = [&](KV& S, KV& N, auto REFILL, auto WG, auto ISV) {
      rsrc_t sn = s0;
      if constexpr (REFILL.v) sn = nexts();
      sfor<0, 4>([&](auto P) {
        point(S, N, sn, P, REFILL, IC<(ISV.v || WG.v < 0 ? 1 : AHW)>{}, WG);
        if constexpr (!ISV.v) {
#pragma unroll
          for (int t = 2 * P.v; t < 2 * P.v + 2; ++t) {
            acc[t] = 0.f;
#pragma unroll
            for (int k = 0; k < 4; ++k) acc[t] = dot2(S.r[t][k], qpk[k], acc[t]);
          }
          asm volatile("" : "+v"(acc[2 * P.v]), "+v"(acc[2 * P.v + 1]));
        } else {
          const unsigned pp = rowbcast<4 * P.v>(__float_as_uint(sc[0]));
#pragma unroll
          for (int k = 0; k < 4; ++k) { acc[2 * k] = dot2(plo(S.r[2 * P.v][k], S.r[2 * P.v + 1][k]), pp, acc[2 * k]); acc[2 * k + 1] = dot2(phi(S.r[2 * P.v][k], S.r[2 * P.v + 1][k]), pp, acc[2 * k + 1]); }
          asm volatile("" : "+v"(acc[0]), "+v"(acc[1]), "+v"(acc[2]), "+v"(acc[3]), "+v"(acc[4]), "+v"(acc[5]), "+v"(acc[6]), "+v"(acc[7]));
        }
        SCHED();
      });
      if constexpr (!ISV.v) {
        float u[4], v[2];
#pragma unroll
        for (int i = 0; i < 4; ++i) u[i] = (c16 & 8 ? acc[i + 4] : acc[i]) + dpp<0x128>(c16 & 8 ? acc[i] : acc[i + 4]);
#pragma unroll
        for (int i = 0; i < 2; ++i) v[i] = (c16 & 4 ? u[i + 2] : u[i]) + dpp<0x141>(c16 & 4 ? u[i] : u[i + 2]);
        float x = (c16 & 2 ? v[1] : v[0]) + dpp<0x4E>(c16 & 2 ? v[0] : v[1]);
        x += dpp<0xB1>(x);
#pragma unroll
        for (int k = 9; k > 0; --k) sc[k] = sc[k - 1];
        sc[0] = x;
      } else {
#pragma unroll
        for (int k = 0; k < 9; ++k) sc[k] = sc[k + 1];
      }
      SCHED();
      if constexpr (REFILL.v || ISV.v) TR();
    };
    step(B, B, IC<0>{}, IC<-1>{}, IC<0>{});
    step(A, B, IC<1>{}, IC<3>{}, IC<0>{});
    step(X, X, IC<0>{}, IC<-1>{}, IC<0>{});
    conv(IC<2>{});
    sfor<3, 6>([&](auto I) {
      if constexpr (I.v & 1) step(B, A, IC<1>{}, IC<I.v + 1>{}, IC<0>{}); else step(A, B, IC<1>{}, IC<I.v + 1>{}, IC<0>{});
      conv(I);
    });
#pragma unroll 1
    for (int i = 6; i < nr; i += 2) {
      step(A, B, IC<1>{}, IC<-1>{}, IC<0>{});
      step(B, A, IC<1>{}, IC<-1>{}, IC<0>{});
    }
    float m = -INFINITY, l = 0.f;
#pragma unroll
    for (int k = 0; k < 10; ++k) { sc[k] = (unsigned)(c16 >> 1) < (__builtin_amdgcn_readlane(tab, nr + k) & 15u) ? sc[k] : -INFINITY; m = fmaxf(m, sc[k]); }
    m = row16_max(m);
    {
      const float mc = m * C2;
#pragma unroll
      for (int k = 0; k < 10; ++k) {
        const float p = ex2(fmaf(sc[k], C2, -mc));
        const unsigned pk = pk_bf(dpp<0x00>(p), dpp<0xAA>(p));
        l = dot2(pk, 0x3f803f80u, l);
        sc[k] = __uint_as_float(pk);
      }
      l += dpp<0x124>(l); l += dpp<0x128>(l);
      *reinterpret_cast<float*>(smem + L_ML + ((c16 & 1) * 16 + w4 * 4 + r4) * 4) = (c16 & 1) ? l : m;
      COMPILER_FENCE();
      const unsigned got = lds_arrive_nw(L_CNT + 40);
      const float mr = *reinterpret_cast<const float*>(smem + L_ML + c16 * 4), lr = *reinterpret_cast<const float*>(smem + L_ML + 64 + c16 * 4);
      SCHED();
      const bool last = lds_done(got) == 3u;
      const float M = row16_max(mr);
      st8c(rs, last && lane == 0 ? WS_GZ + (b * 128 + h * 4 + cls) * 128 : OOB, u32x2{__float_as_uint(fmaf(M, C2, __builtin_amdgcn_logf(row16_sum(ex2((mr - M) * C2) * lr)))), tg.hi});
    }
#pragma unroll
    for (int k = 0; k < 8; ++k) acc[k] = 0.f;
    SCHED();
    STAMP(8);
    unsigned yoff = (r4 & 1) ? OOB : WS_GB + (b * 128 + h * 4 + cls) * 2048 + 512 * w4 + 32 * c16 + 16 * (lane >> 5), zoff = WS_GZ + (b * 128 + h * 4 + (lane & 3)) * 128;
    unsigned rwo = L_MRG + (w4 * 4 + r4) * 144 + ((c16 - 4 * rot) & 15) * 8, rro = L_MRG + c16 * 144 + r4 * 16;
    asm volatile("" : "+v"(yoff), "+v"(zoff), "+v"(rwo), "+v"(rro));
    u32x2 zq;
    step(A, B, IC<1>{}, IC<7>{}, IC<1>{});
    conv(IC<6>{});
#pragma unroll 1
    for (int i = 2;;) {
      step(B, A, IC<1>{}, IC<-1>{}, IC<1>{});
      i += 2;
      zq = ld8c(rs, i >= nr ? zoff : OOB);
      SCHED();
      step(A, B, IC<1>{}, IC<-1>{}, IC<1>{});
      if (i >= nr) break;
    }
    conv(IC<7>{});
    if (!__all(zq[1] == tg.hi)) {
#pragma unroll 1
      for (int spin = 0; spin < SPIN_MAX; ++spin) {
        COMPILER_FENCE();
        zq = ld8c(rs, zoff);
        if (__all(zq[1] == tg.hi)) break;
        __builtin_amdgcn_s_sleep(8);
      }
    }
    float wt;
    int six = (lane & 1) ? 0xEEEE : 0x4444;
    {
      const float z = __uint_as_float(zq[0]);
      float Z = fmaxf(z, dpp<0xB1>(z)); Z = fmaxf(Z, dpp<0x4E>(Z));
      float L = ex2(z - Z); L += dpp<0xB1>(L); L += dpp<0x4E>(L);
      Z += __builtin_amdgcn_logf(L);
      wt = ex2(fmaf(m, C2, -Z));
      asm volatile("" : "+v"(wt), "+v"(six));
    }
    f32x4 d[8];
#pragma unroll
    for (int e = 0; e < 8; ++e) { d[e] = f32x4{0.f, 0.f, 0.f, 0.f}; asm volatile("" : "+v"(d[e])); }
    if (cls == 0) lds_wait(cnt + 1, 4u);
    SCHED();
    step(B, B, IC<0>{}, IC<-1>{}, IC<1>{});
    STAMP(1);
#pragma unroll
    for (int k = 0; k < 8; ++k) acc[k] *= wt;
    *reinterpret_cast<u32x2*>(smem + rwo) = u32x2{pk_bf(rs16(acc[0], acc[2]), rs16(acc[1], acc[3])), pk_bf(rs16(acc[4], acc[6]), rs16(acc[5], acc[7]))};
    {
      u32x4 af[2];
      unsigned got = lds_arrive_nw(L_CNT), need = 3u;
#pragma unroll 1
      for (int spin = 0; spin < SPIN_MAX; ++spin) {
#pragma unroll
        for (int t = 0; t < 2; ++t) af[t] = *reinterpret_cast<const u32x4*>(smem + rro + t * 64);
        SCHED();
        if (lds_done(got) >= need) break;
        need = 4u;
        if (TSL) __builtin_amdgcn_s_sleep(TSL);
        asm volatile("ds_read_b32 %0, %1" : "=v"(got) : "v"((unsigned)L_CNT) : "memory");
      }
      SCHED();
      STAMP(3);
      float v4[4];
#pragma unroll
      for (int k = 0; k < 4; ++k)
#pragma unroll
        for (int t = 0; t < 2; ++t) {
          d[k] = __builtin_amdgcn_smfmac_f32_16x16x64_bf16(__builtin_bit_cast(bf16x8_t, af[t]), __builtin_bit_cast(bf16x16_t, T[t][k]), d[k], six, 0, 0);
          d[k + 4] = __builtin_amdgcn_smfmac_f32_16x16x64_bf16(__builtin_bit_cast(bf16x8_t, af[t]), __builtin_bit_cast(bf16x16_t, T[t][k + 4]), d[k + 4], six, 0, 0);
        }
#pragma unroll
      for (int k = 0; k < 4; ++k) {
        const f32x2 x = d[k].xy + d[k].zw, y = d[k + 4].xy + d[k + 4].zw;
        v4[k] = x16_sum(rs32(x[0] + x[1], y[0] + y[1]));
      }
      STAMP(4);
      st16c(rs, yoff, u32x4{pk_bf(v4[0], v4[1]), tg.hi, pk_bf(v4[2], v4[3]), tg.lo});
      SCHED();
      lds_add(cnt + 11, 1u, lane);
    }
    STAMP(2);
    STAMP_FLUSH(1); TR_FLUSH();
    return;
  }
  u32x4 Wa[16], Wb[16], xv[4];
  unsigned pf;
  u64 epw, tstart;
  {
    unsigned oa[16];
    oa[0] = (cls & 1) ? (w4 * 128 + 16 * (lane >> 3)) * 8192 + idx * 128 + 16 * (lane & 7) : (cls == 2 ? (b * 256 + w4 * 64 + 16 * r4) * 8192 + h * 256 + 16 * c16 : (idx * 128 + w4) * 1024 + 16 * lane);
#pragma unroll
    for (int i = 1; i < 16; ++i) oa[i] = oa[i - 1] + (cls ? 8192 : 4096);
    unsigned op = (((blk >> 5) * 4 + w4) * 16 + c16) * 8192 + ((idx & 56) + 3 + (cls == 0 ? -1 : (idx >= 56 ? 4 : 0)) * (lane >> 5)) * 128 + (r4 & 1) * 64;
    unsigned ox = w4 * 256 + 32 * (lane >> 3);
#pragma unroll
    for (int i = 0; i < 16; i += 8) asm volatile("" : "+v"(oa[i]), "+v"(oa[i + 1]), "+v"(oa[i + 2]), "+v"(oa[i + 3]), "+v"(oa[i + 4]), "+v"(oa[i + 5]), "+v"(oa[i + 6]), "+v"(oa[i + 7]));
    asm volatile("" : "+v"(op), "+v"(ox));
    asm volatile("" : "+s"(p0), "+s"(p1), "+s"(p2), "+s"(p3));
    SCHED();
    tstart = wall_clock64();
    SCHED();
    pf = *reinterpret_cast<const unsigned*>((const char*)p1 + op);
    epw = __hip_atomic_load((gu64*)p3, __ATOMIC_RELAXED, __HIP_MEMORY_SCOPE_AGENT);
#pragma unroll
    for (int i = 0; i < 4; ++i) xv[i] = ld16((const char*)p2 + ox + (i >> 1) * 1024 + 16 * (i & 1));
    SCHED();
#pragma unroll
    for (int i = 0; i < 16; ++i) { Wa[i] = ld16nt((const char*)p0 + oa[i]); SCHED(); }
  }
  char* ws = (char*)p3;
  const rsrc_t rs = __builtin_amdgcn_make_buffer_rsrc(ws, 0, (int)WS_END, 0x00020000);
  u16* out = (u16*)p5;
  {
    rawbar();
    const u64 t64 = (((u64)(unsigned)__builtin_amdgcn_readfirstlane((unsigned)(epw >> 32)) << 32) | (unsigned)__builtin_amdgcn_readfirstlane((unsigned)epw)) + GOLD;
    tg.hi = (unsigned)(t64 >> 32); tg.lo = (unsigned)t64;
    asm volatile("" : "+s"(tg.hi), "+s"(tg.lo) : "v"(pf) : "memory");
    if (w4 == 0) { cnt[13] = tg.hi; cnt[14] = tg.lo; COMPILER_FENCE(); lds_add(cnt + 15, 1u, lane); }
    STAMP(13);
  }
  float* red = reinterpret_cast<float*>(smem);
  auto gemv = [&](float* dst) {
    float acc[2][8];
#pragma unroll
    for (int bb = 0; bb < 2; ++bb)
#pragma unroll
      for (int k = 0; k < 8; ++k) acc[bb][k] = 0.f;
#pragma unroll
    for (int ip = 0; ip < 8; ++ip) {
      const unsigned xp0 = xv[ip >> 2][ip & 3], xp1 = xv[2 + (ip >> 2)][ip & 3];
#pragma unroll
      for (int k = 0; k < 4; ++k) {
        const unsigned lo = plo(Wa[2 * ip][k], Wa[2 * ip + 1][k]), hi = phi(Wa[2 * ip][k], Wa[2 * ip + 1][k]);
        acc[0][2 * k] = dot2(lo, xp0, acc[0][2 * k]); acc[0][2 * k + 1] = dot2(hi, xp0, acc[0][2 * k + 1]);
        acc[1][2 * k] = dot2(lo, xp1, acc[1][2 * k]); acc[1][2 * k + 1] = dot2(hi, xp1, acc[1][2 * k + 1]);
      }
    }
    float v8[8], v4[4];
#pragma unroll
    for (int k = 0; k < 8; ++k) v8[k] = rs32(acc[0][k], acc[1][k]);
#pragma unroll
    for (int k = 0; k < 4; ++k) { const float t = rs16(v8[k], v8[k + 4]); v4[k] = t + dpp<0x128>(t); }
    *reinterpret_cast<f32x4*>(dst + (lane >> 5) * 64 + 8 * (lane & 7) + 4 * (r4 & 1)) = f32x4{v4[0], v4[1], v4[2], v4[3]};
    COMPILER_FENCE();
  };
  if (cls & 1) {
    gemv(red + w4 * 128);
    const unsigned got = lds_arrive_nw(L_CNT + 8);
    f32x4 t4[4];
#pragma unroll
    for (int w = 0; w < 4; ++w) t4[w] = *reinterpret_cast<const f32x4*>(red + w * 128 + 4 * (lane & 31));
    SCHED();
    if (lds_done(got) == 3u) {
      const f32x4 t = (t4[0] + t4[1]) + (t4[2] + t4[3]);
      st16c(rs, lane < 32 ? WS_QX + ((((cls >> 1) * 2 + r4) * 32 + h) * 64 + b * 32 + 2 * c16) * 8 : OOB, u32x4{pk_bf(t[0], t[1]), tg.hi, pk_bf(t[2], t[3]), tg.hi});
      SCHED();
      lds_add(cnt + 2, 1u, lane);
    } else lds_wait(cnt + 2, 5u);
    STAMP(8);
  } else if (cls == 2) {
    while (wall_clock64() - tstart < TQX) __builtin_amdgcn_s_sleep(4);
    u32x4 q0, q1;
    const unsigned qo = WS_QX + (h * 64 + 4 * c16) * 8;
#pragma unroll 1
    for (int spin = 0; spin < SPIN_MAX; ++spin) {
      COMPILER_FENCE();
      const u32x4 g0 = ld16c(rs, qo), g1 = ld16c(rs, qo + 16), g2 = ld16c(rs, qo + 16384), g3 = ld16c(rs, qo + 16400);
      q0 = u32x4{g0[0], g0[2], g1[0], g1[2]}; q1 = u32x4{g2[0], g2[2], g3[0], g3[2]};
      if (__all((g0[1] == tg.hi) & (g0[3] == tg.hi) & (g1[1] == tg.hi) & (g1[3] == tg.hi) & (g2[1] == tg.hi) & (g2[3] == tg.hi) & (g3[1] == tg.hi) & (g3[3] == tg.hi))) break;
      __builtin_amdgcn_s_sleep(1);
    }
    STAMP(9);
    float v[4];
    {
      float u[16], t8[8];
#pragma unroll
      for (int i = 0; i < 16; ++i) {
        float a = 0.f, c = 0.f;
#pragma unroll
        for (int k = 0; k < 4; ++k) { a = dot2(Wa[i][k], q0[k], a); c = dot2(Wa[i][k], q1[k], c); }
        u[i] = (c16 & 4 ? c : a) + dpp<0x141>(c16 & 4 ? a : c);
        asm volatile("" : "+v"(u[i]));
      }
#pragma unroll
      for (int i = 0; i < 8; ++i) { t8[i] = (c16 & 2 ? u[i + 8] : u[i]) + dpp<0x4E>(c16 & 2 ? u[i] : u[i + 8]); asm volatile("" : "+v"(t8[i])); }
#pragma unroll
      for (int i = 0; i < 4; ++i) {
        float t = (c16 & 1 ? t8[i + 4] : t8[i]) + dpp<0xB1>(c16 & 1 ? t8[i] : t8[i + 4]);
        asm volatile("" : "+v"(t));
        v[i] = t + dpp<0x128>(t);
      }
    }
    st16c(rs, c16 < 8 ? WS_QA + ((c16 >> 2) * 32 + h) * 2048 + (b * 256 + w4 * 64 + 16 * r4 + 4 * (c16 & 3)) * 4 : OOB, u32x4{pk_bf(v[0], v[1]), tg.hi, pk_bf(v[2], v[3]), tg.hi});
    STAMP(10);
  }
  if (cls >= 2) {
    const unsigned tw = __builtin_nontemporal_load(reinterpret_cast<const unsigned*>(out + (idx >> 5) * 1024 + (cls & 1) * 512 + 16 * (idx & 31)));
    asm volatile("" :: "v"(tw));
  }
  asm volatile("" :: "v"(xv[0]), "v"(xv[1]), "v"(xv[2]), "v"(xv[3]));
  SCHED();
  if (cls >= 2) {
    unsigned d0 = (cls == 3 ? WS_GB + (idx >> 5) * 128 * 2048 : WS_GA + (idx >> 5) * 64 * 2048) + (16 * w4 + (lane >> 2)) * 2048 + (idx & 31) * 64 + (lane & 3) * 16;
    unsigned d1 = cls == 3 ? d0 + 64 * 2048 : d0;
    unsigned fw = (lane & 12) ? 32768 + 256 + lane * 4 : 32768 + (w4 * 16 + 4 * (lane & 3) + r4) * 4, fr = 32768 + 8 * (lane & 7);
    unsigned oo = lane < 8 ? ((idx >> 5) * 1024 + (cls & 1) * 512 + 16 * (idx & 31) + 2 * lane) * 2 : OOB;
    float f1 = cls == 3 ? 1.f : 0.f;
    const rsrc_t ro = __builtin_amdgcn_make_buffer_rsrc(out, 0, 4096, 0x00020000);
    asm volatile("" : "+v"(d0), "+v"(d1), "+v"(fw), "+v"(fr), "+v"(oo), "+v"(f1));
    lds_wait(cnt + FST, 4u);
    if (TGA && cls == 2) { while (wall_clock64() - tstart < TGA) __builtin_amdgcn_s_sleep(2); }
    STAMP(3);
    u32x4 x, y;
    bool ok = false;
#pragma unroll 1
    for (int spin = 0; spin < SPIN_MAX; ++spin) {
      COMPILER_FENCE();
      if (!ok) { x = ld16c(rs, d0); y = ld16c(rs, d1); }
      ok = (((x[1] ^ tg.hi) | (y[1] ^ tg.hi)) | ((x[3] ^ tg.lo) | (y[3] ^ tg.lo))) == 0u;
      if (__all(ok)) break;
    }
    STAMP(14);
    float s = rs16(rs32(fmaf(f1, bflo(y[0]), bflo(x[0])), fmaf(f1, bflo(y[2]), bflo(x[2]))), rs32(fmaf(f1, bfhi(y[0]), bfhi(x[0])), fmaf(f1, bfhi(y[2]), bfhi(x[2]))));
    s += dpp<0x124>(s); s += dpp<0x128>(s);
    *reinterpret_cast<float*>(smem + fw) = s;
    COMPILER_FENCE();
    const unsigned got = lds_arrive_nw(L_CNT + 24);
    f32x2 t[4];
#pragma unroll
    for (int w = 0; w < 4; ++w) t[w] = *reinterpret_cast<const f32x2*>(smem + fr + w * 64);
    SCHED();
    if (lds_done(got) == 3u) {
      const f32x2 r = (t[0] + t[1]) + (t[2] + t[3]);
      __builtin_amdgcn_raw_buffer_store_b32(pk_bf(r[0], r[1]), ro, (int)oo, 0, 0);
    }
    STAMP(6);
    STAMP_FLUSH(0); TR_FLUSH();
    return;
  }
  if (cls == 0) {
    {
      const u16* pb = (const u16*)p0 + (idx * 128 + 64 + w4) * 512 + 8 * lane;
#pragma unroll
      for (int i = 0; i < 16; ++i) {
        Wb[i] = ld16nt(pb + i * 2048);
        if (i >= 4 && (i & 1)) asm volatile("" : "+v"(Wb[i - 4]));
        if (i == 9) TR();
        SCHED();
      }
    }
    if (TQA) { while (wall_clock64() - tstart < TQA) __builtin_amdgcn_s_sleep(2); }
    lds_add(cnt + 8, 1u, lane);
    char* scr = smem + L_SCR;
    const int ab = idx >> 5, r = lane & 31, hh = lane >> 5;
#pragma unroll
    for (int i = 0; i < 16; ++i) *reinterpret_cast<u32x4*>(smem + ((i >> 3) * 4 + r4) * 8192 + toff(4 * (i & 7) + w4, c16)) = Wa[i];
#pragma unroll
    for (int i = 0; i < 16; ++i) *reinterpret_cast<u32x4*>(smem + ((2 + (i >> 3)) * 4 + r4) * 8192 + toff(4 * (i & 7) + w4, c16)) = Wb[i];
    STAMP(8);
    TR();
    u32x4 qlast = {0u, 0u, 0u, 0u};
    {
      u32x4 g[18];
      const int fl = (idx & 1) * 8;
      const unsigned o8 = WS_QA + (ab * 32 + w4 * 8) * 2048 + lane * 16, ol = WS_QA + (ab * 32 + r) * 2048 + 1984 + 32 * hh;
      unsigned pend = 0x3ffffu;
#pragma unroll 1
      for (int spin = 0; spin < SPIN_MAX && pend; ++spin) {
        COMPILER_FENCE();
#pragma unroll
        for (int i = 0; i < 18; ++i) g[i] = ld16c(rs, (pend >> i & 1) ? (i < 16 ? o8 + (i ^ fl) * 1024 : ol + 16 * (i - 16)) : OOB);
        if (spin == 0) { SCHED(); lds_add(cnt + 8, ~0u, lane); SCHED(); }
#pragma unroll
        for (int i = 0; i < 18; ++i) {
          if ((pend >> i & 1) && __all((g[i][1] == tg.hi) & (g[i][3] == tg.hi))) {
            pend &= ~(1u << i);
            if (i >= 16) { qlast[2 * (i - 16)] = g[i][0]; qlast[2 * (i - 16) + 1] = g[i][2]; }
            else if (!(i & 1) || lane < 60) *reinterpret_cast<u32x2*>(scr + (w4 * 8 + ((i ^ fl) >> 1)) * 1008 + (i & 1) * 512 + 8 * lane) = u32x2{g[i][0], g[i][2]};
          }
        }
        if (pend) __builtin_amdgcn_s_sleep(8);
      }
    }
    sbar4(cnt + 3, lane);
    STAMP(10);
    f32x16 sacc;
#pragma unroll
    for (int i = 0; i < 16; ++i) sacc[i] = 0.f;
#pragma unroll
    for (int jj = 0; jj < 32; ++jj) {
      const u32x4 af = *reinterpret_cast<const u32x4*>(smem + (w4 * 4 + (jj >> 3)) * 8192 + toff(r, 2 * (jj & 7) + hh));
      const u32x4 bf = jj < 31 ? *reinterpret_cast<const u32x4*>(scr + r * 1008 + 16 * (2 * (jj < 31 ? jj : 0) + hh)) : qlast;
      sacc = __builtin_amdgcn_mfma_f32_32x32x16_bf16(__builtin_bit_cast(bf16x8_t, af), __builtin_bit_cast(bf16x8_t, bf), sacc, 0, 0, 0);
    }
    float m = sacc[0];
#pragma unroll
    for (int i = 1; i < 16; ++i) m = fmaxf(m, sacc[i]);
    m = xhalf_max(m);
    if (hh == 0) *reinterpret_cast<float*>(scr + r * 1008 + 992 + 4 * w4) = m;
    sbar4(cnt + 4, lane);
    STAMP(11);
    {
      const f32x4 m4 = *reinterpret_cast<const f32x4*>(scr + r * 1008 + 992);
      m = fmaxf(fmaxf(m4[0], m4[1]), fmaxf(m4[2], m4[3]));
    }
    const float mc = m * C2;
    float p[16], ls = 0.f;
#pragma unroll
    for (int i = 0; i < 16; ++i) { p[i] = ex2(fmaf(sacc[i], C2, -mc)); ls += p[i]; }
    ls = xhalf_sum(ls);
    sbar4(cnt + 5, lane);
    u32x4* pf = reinterpret_cast<u32x4*>(scr);
    float* wl = reinterpret_cast<float*>(scr + 8192);
    if (hh == 0) wl[r * 4 + w4] = ls;
    pf[(2 * w4) * 64 + lane] = u32x4{pk_bf(p[0], p[1]), pk_bf(p[2], p[3]), pk_bf(p[4], p[5]), pk_bf(p[6], p[7])};
    pf[(2 * w4 + 1) * 64 + lane] = u32x4{pk_bf(p[8], p[9]), pk_bf(p[10], p[11]), pk_bf(p[12], p[13]), pk_bf(p[14], p[15])};
    sbar4(cnt + 6, lane);
    u32x4 pb8[8];
#pragma unroll
    for (int ks = 0; ks < 8; ++ks) pb8[ks] = pf[ks * 64 + lane];
    const f32x4 l4 = *reinterpret_cast<const f32x4*>(wl + r * 4);
    const int qq = (lane & 15) >> 2, pp = lane & 3, blk2 = (lane >> 4) & 1;
    char* ot = scr + 12288 + w4 * 4096;
#pragma unroll
    for (int c2 = 0; c2 < 2; ++c2) {
      f32x16 oacc[2];
#pragma unroll
      for (int ct = 0; ct < 2; ++ct)
#pragma unroll
        for (int i = 0; i < 16; ++i) oacc[ct][i] = 0.f;
#pragma unroll
      for (int ks = 0; ks < 8; ++ks)
#pragma unroll
        for (int ct = 0; ct < 2; ++ct) {
          typedef __attribute__((address_space(3))) s16x4* lp;
          const char* tile = smem + ((ks >> 1) * 4 + w4) * 8192;
          const int cc = 2 * c2 + ct;
          const s16x4 lo = __builtin_amdgcn_ds_read_tr16_b64_v4i16((lp)(tile + toff(16 * (ks & 1) + 4 * hh + qq, 4 * cc + 2 * blk2 + (pp >> 1)) + 8 * (pp & 1)));
          const s16x4 hi = __builtin_amdgcn_ds_read_tr16_b64_v4i16((lp)(tile + toff(16 * (ks & 1) + 8 + 4 * hh + qq, 4 * cc + 2 * blk2 + (pp >> 1)) + 8 * (pp & 1)));
          const s16x8 afr = __builtin_shufflevector(lo, hi, 0, 1, 2, 3, 4, 5, 6, 7);
          oacc[ct] = __builtin_amdgcn_mfma_f32_32x32x16_bf16(__builtin_bit_cast(bf16x8_t, afr), __builtin_bit_cast(bf16x8_t, pb8[ks]), oacc[ct], 0, 0, 0);
        }
#pragma unroll
      for (int ct = 0; ct < 2; ++ct)
#pragma unroll
        for (int g = 0; g < 4; ++g)
          *reinterpret_cast<u32x2*>(ot + r * 128 + 16 * ((4 * ct + g) ^ (r & 7)) + 8 * hh) = u32x2{pk_bf(oacc[ct][4 * g], oacc[ct][4 * g + 1]), pk_bf(oacc[ct][4 * g + 2], oacc[ct][4 * g + 3])};
      if (c2 == 0) lds_add(cnt + 8, 1u, lane);
      __builtin_amdgcn_wave_barrier();
#pragma unroll
      for (int i = 0; i < 4; ++i) {
        const int hd = 8 * i + (lane >> 3), pc = lane & 7;
        const u32x4 vv = *reinterpret_cast<const u32x4*>(ot + hd * 128 + 16 * (pc ^ (hd & 7)));
        st16c(rs, WS_APO + ((APL ? idx * 32 + hd : hd * 64 + idx) * 512 + w4 * 128 + 64 * c2 + 8 * pc) * 2, vv);
      }
      __builtin_amdgcn_wave_barrier();
    }
    lds_arrive(cnt + 1, lane);
    STAMP(1);
    asm volatile("s_waitcnt vmcnt(0)" ::: "memory");
    if (lds_arrive(cnt + 9, lane) == 3u && hh == 0)
      st16c(rs, WS_AML + (idx * 32 + r) * 16, u32x4{__float_as_uint(m), tg.hi, __float_as_uint((l4[0] + l4[1]) + (l4[2] + l4[3])), tg.hi});
    lds_add(cnt + 8, ~0u, lane);
    STAMP(2);
    if (idx == 0 && w4 == 0) {
      lds_wait(cnt + ADV, 4u);
#pragma unroll 1
      for (int spin = 0; spin < SPIN_MAX; ++spin) {
        COMPILER_FENCE();
        const u32x2 t0 = ld8c(rs, WS_AML + lane * 512), t1 = ld8c(rs, WS_QX + (64 * 64 + lane * 32) * 8);
        if (__all((t0[1] == tg.hi) & (t1[1] == tg.hi))) break;
        __builtin_amdgcn_s_sleep(8);
      }
      if (lane == 0) __hip_atomic_store((gu64*)(ws + WS_EP), ((u64)tg.hi << 32) | tg.lo, __ATOMIC_RELAXED, __HIP_MEMORY_SCOPE_AGENT);
    }
    STAMP_FLUSH(0); TR_FLUSH();
    return;
  }
  if (cls == 1) {
    {
      const u16* pa = (const u16*)p1 + (w4 * 128 + 16 * (lane >> 3)) * 4096 + h * 128 + b * 64 + 8 * (lane & 7);
      const u16* pb = (const u16*)p4 + (h * 128 + b * 64 + w4 * 2) * 512 + 8 * lane;
#pragma unroll
      for (int i = 0; i < 16; ++i) {
        Wa[i] = ld16nt(pa + i * 4096);
        SCHED();
        if ((i & 1) && !((idx & 7) == 3 || idx == 63)) asm volatile("s_waitcnt vmcnt(6)" ::: "memory");
        if ((i & 7) == 7) TR();
        SCHED();
      }
#pragma unroll
      for (int i = 0; i < 16; ++i) {
        Wb[i] = ld16nt(pb + (i >> 1) * 4096 + (i & 1) * 512);
        SCHED();
        if (i & 1) asm volatile("s_waitcnt vmcnt(6)" ::: "memory");
        if ((i & 7) == 7) TR();
        SCHED();
      }
    }
    float* red2 = red + 2048;
    float* wsh = red + 3072;
    if (w4 == 0) {
      const unsigned fo = WS_AML + (lane * 32 + h) * 16;
      while (wall_clock64() - tstart < TAML) __builtin_amdgcn_s_sleep(8);
      u32x4 g;
      bool ok = false;
#pragma unroll 1
      for (int spin = 0; spin < SPIN_MAX; ++spin) {
        COMPILER_FENCE();
        if (!ok) g = ld16c(rs, fo);
        ok = (g[1] == tg.hi) & (g[3] == tg.hi);
        if (__all(ok)) break;
        __builtin_amdgcn_s_sleep(2);
      }
      lds_add(cnt + 7, 1u, lane);
      const float mu = __uint_as_float(g[0]), lu = __uint_as_float(g[2]);
      const float M = x16_max(row16_max(mu));
      const float wl = ex2((mu - M) * C2);
      wsh[lane] = wl / x16_sum(row16_sum(wl * lu));
      lds_arrive(cnt + 7, lane);
    } else lds_wait(cnt + 7, 1u);
    COMPILER_FENCE();
    STAMP(3);
    u32x4 pp[16];
#pragma unroll
    for (int i = 0; i < 16; ++i) { pp[i] = ld16c(rs, WS_APO + (APL ? (w4 * 16 + (i ^ (8 * b))) * 32 + h : h * 64 + w4 * 16 + (i ^ (8 * b))) * 1024 + lane * 16); SCHED(); }
    lds_wait(cnt + 7, 2u);
    float a8[8];
#pragma unroll
    for (int k = 0; k < 8; ++k) a8[k] = 0.f;
    const float wv = wsh[w4 * 16 + (c16 ^ (8 * b))];
#pragma unroll
    for (int i = 0; i < 16; ++i) {
      const float wt = rdlane(wv, i);
#pragma unroll
      for (int k = 0; k < 4; ++k) { a8[2 * k] = fmaf(wt, bflo(pp[i][k]), a8[2 * k]); a8[2 * k + 1] = fmaf(wt, bfhi(pp[i][k]), a8[2 * k + 1]); }
      asm volatile("" : "+v"(a8[0]), "+v"(a8[1]), "+v"(a8[2]), "+v"(a8[3]), "+v"(a8[4]), "+v"(a8[5]), "+v"(a8[6]), "+v"(a8[7]));
      SCHED();
    }
    *reinterpret_cast<f32x4*>(red + w4 * 512 + 8 * lane) = f32x4{a8[0], a8[1], a8[2], a8[3]};
    *reinterpret_cast<f32x4*>(red + w4 * 512 + 8 * lane + 4) = f32x4{a8[4], a8[5], a8[6], a8[7]};
    sbar4(cnt + 3, lane);
    STAMP(4);
#pragma unroll
    for (int i = 0; i < 4; ++i) {
      const float* src = red + (i >> 1) * 1024 + w4 * 128 + 16 * (lane >> 3) + 8 * (i & 1);
      const f32x4 t0 = *reinterpret_cast<const f32x4*>(src) + *reinterpret_cast<const f32x4*>(src + 512), t1 = *reinterpret_cast<const f32x4*>(src + 4) + *reinterpret_cast<const f32x4*>(src + 516);
      xv[i] = u32x4{pk_bf(t0[0], t0[1]), pk_bf(t0[2], t0[3]), pk_bf(t1[0], t1[1]), pk_bf(t1[2], t1[3])};
    }
    gemv(red2 + w4 * 128);
    sbar4(cnt + 4, lane);
    STAMP(12);
    unsigned op;
    {
      f32x2 t = {0.f, 0.f};
#pragma unroll
      for (int w = 0; w < 4; ++w) t += *reinterpret_cast<const f32x2*>(red2 + w * 128 + (lane >> 5) * 64 + 8 * (lane & 7) + 2 * w4);
      op = pk_bf(t[0], t[1]);
    }
    float* red3 = red + 4096;
    float a[2][8];
#pragma unroll
    for (int bb = 0; bb < 2; ++bb)
#pragma unroll
      for (int k = 0; k < 8; ++k) a[bb][k] = 0.f;
#pragma unroll
    for (int ip = 0; ip < 8; ++ip) {
      const unsigned v0 = __builtin_amdgcn_readlane(op, ip), v1 = __builtin_amdgcn_readlane(op, 32 + ip);
#pragma unroll
      for (int k = 0; k < 4; ++k) {
        const unsigned lo = plo(Wb[2 * ip][k], Wb[2 * ip + 1][k]), hi = phi(Wb[2 * ip][k], Wb[2 * ip + 1][k]);
        a[0][2 * k] = dot2(lo, v0, a[0][2 * k]); a[0][2 * k + 1] = dot2(hi, v0, a[0][2 * k + 1]);
        a[1][2 * k] = dot2(lo, v1, a[1][2 * k]); a[1][2 * k + 1] = dot2(hi, v1, a[1][2 * k + 1]);
      }
    }
#pragma unroll
    for (int bb = 0; bb < 2; ++bb) {
      *reinterpret_cast<f32x4*>(red3 + (w4 * 2 + bb) * 512 + 8 * lane) = f32x4{a[bb][0], a[bb][1], a[bb][2], a[bb][3]};
      *reinterpret_cast<f32x4*>(red3 + (w4 * 2 + bb) * 512 + 8 * lane + 4) = f32x4{a[bb][4], a[bb][5], a[bb][6], a[bb][7]};
    }
    sbar4(cnt + 5, lane);
    {
      f32x4 t = {0.f, 0.f, 0.f, 0.f};
#pragma unroll
      for (int w = 0; w < 4; ++w) t += *reinterpret_cast<const f32x4*>(red3 + w * 1024 + 4 * tid);
      st16c(rs, WS_GA + ((tid >> 7) * 64 + idx) * 2048 + (tid & 127) * 16, u32x4{pk_bf(t[0], t[1]), tg.hi, pk_bf(t[2], t[3]), tg.lo});
    }
    STAMP(5);
    STAMP_FLUSH(0); TR_FLUSH();
    if (PADN) asm volatile("s_endpgm\n\t.fill %0,4,0xBF800000" :: "n"(PADN));
    return;
  }
}

extern "C" void kernel_launch(void* const* d_in, const int* in_sizes, int n_in, void* d_out, int out_size, void* d_ws, size_t ws_size, hipStream_t stream) {
  const int sel[5][3] = {{1, 3, 6}, {2, 4, 0}, {3, 2, 6}, {9, 9, 6}, {7, 8, 10}};
  Args a;
  for (int r = 0; r < 5; ++r) {
    for (int c = 0; c < 3; ++c) a.t[r][c] = (gptr)d_in[sel[r][c]];
    a.t[r][3] = (gptr)d_ws;
  }
  a.w_o = (gptr)d_in[5];
  a.out = (gptr)d_out;
  kmain<<<256, 512, 0, stream>>>(a);
}
```

```cpp
#include <hip/hip_runtime.h>
#include <hip/hip_bf16.h>
#include <stdint.h>

#define DI __device__ __forceinline__
typedef __attribute__((ext_vector_type(4))) unsigned u32x4;
typedef __attribute__((ext_vector_type(2))) unsigned u32x2;
typedef __attribute__((ext_vector_type(8))) unsigned u32x8;
typedef __attribute__((ext_vector_type(16))) __bf16 bf16x16_t;
typedef __attribute__((ext_vector_type(4))) float f32x4;
typedef __attribute__((ext_vector_type(2))) float f32x2;
typedef __attribute__((ext_vector_type(16))) float f32x16;
typedef __attribute__((ext_vector_type(8))) __bf16 bf16x8_t;
typedef __attribute__((ext_vector_type(2))) __bf16 bf16x2_t;
typedef __attribute__((ext_vector_type(4))) short s16x4;
typedef __attribute__((ext_vector_type(8))) short s16x8;
typedef unsigned short u16;
typedef unsigned long long u64;

#define SHARES 48, 48, 80, 80
constexpr int SH[4] = {SHARES};
constexpr int NR[4] = {8, 8, 10, 10};
static_assert(SH[0] + SH[1] + SH[2] + SH[3] == 256 && SH[0] <= 64 && SH[1] <= 64 && SH[2] <= 80 && SH[3] <= 80 && SH[0] >= 8 && SH[1] >= 8, "every key once");
#define TQX 430
#define TQA 840
#define TAML 1680
#define PADN 200
constexpr float C2 = 0.08838834764831845f * 1.4426950408889634f;
constexpr u64 GOLD = 0x9E3779B97F4A7C15ull;

constexpr unsigned WS_EP = 0;
constexpr unsigned WS_QX = 4096;
constexpr unsigned WS_QA = WS_QX + 2 * 2 * 32 * 64 * 8;
constexpr unsigned WS_AML = WS_QA + 2 * 32 * 256 * 8;
constexpr unsigned WS_APO = WS_AML + 64 * 32 * 16;
constexpr unsigned WS_GB = WS_APO + 32 * 64 * 512 * 2;
constexpr unsigned WS_GA = WS_GB + 2 * 128 * 128 * 16;
constexpr unsigned WS_GZ = WS_GA + 2 * 64 * 128 * 16;
constexpr unsigned WS_END = WS_GZ + 256 * 128 + 128;
#define STAMP(i) do {} while (0)
#define STAMP_DECL do {} while (0)
#define STAMP_FLUSH(grp) do {} while (0)

#define TR_DECL do {} while (0)
#define TR() do {} while (0)
#define TR_FLUSH() do {} while (0)
DI float bflo(unsigned u) { return __uint_as_float(u << 16); }
DI float bfhi(unsigned u) { return __uint_as_float(u & 0xffff0000u); }
DI unsigned pk_bf(float a, float b) { const f32x2 v = {a, b}; return __builtin_bit_cast(unsigned, __builtin_convertvector(v, bf16x2_t)); }
DI float dot2(unsigned k, unsigned q, float acc) {
  return __builtin_amdgcn_fdot2_f32_bf16(__builtin_bit_cast(bf16x2_t, k), __builtin_bit_cast(bf16x2_t, q), acc, false);
}
DI unsigned plo(unsigned w0, unsigned w1) { return __builtin_amdgcn_perm(w1, w0, 0x05040100u); }
DI unsigned phi(unsigned w0, unsigned w1) { return __builtin_amdgcn_perm(w1, w0, 0x07060302u); }
template <int CTRL> DI float dpp(float x) {
  return __builtin_bit_cast(float, __builtin_amdgcn_mov_dpp(__builtin_bit_cast(int, x), CTRL, 0xf, 0xf, true));
}
DI float row16_sum(float x) { x += dpp<0xB1>(x); x += dpp<0x4E>(x); x += dpp<0x141>(x); x += dpp<0x128>(x); return x; }
DI float row16_max(float x) { x = fmaxf(x, dpp<0xB1>(x)); x = fmaxf(x, dpp<0x4E>(x)); x = fmaxf(x, dpp<0x141>(x)); x = fmaxf(x, dpp<0x128>(x)); return x; }
DI float x16_sum(float x) { auto s = __builtin_amdgcn_permlane16_swap(__float_as_uint(x), __float_as_uint(x), false, false); return __uint_as_float(s[0]) + __uint_as_float(s[1]); }
DI float x16_max(float x) { auto s = __builtin_amdgcn_permlane16_swap(__float_as_uint(x), __float_as_uint(x), false, false); return fmaxf(__uint_as_float(s[0]), __uint_as_float(s[1])); }
DI float xhalf_max(float x) { auto t = __builtin_amdgcn_permlane32_swap(__float_as_uint(x), __float_as_uint(x), false, false); return fmaxf(__uint_as_float(t[0]), __uint_as_float(t[1])); }
DI float xhalf_sum(float x) { auto t = __builtin_amdgcn_permlane32_swap(__float_as_uint(x), __float_as_uint(x), false, false); return __uint_as_float(t[0]) + __uint_as_float(t[1]); }
DI u32x4 ld16(const void* p) { return *reinterpret_cast<const u32x4*>(p); }
DI u32x4 ld16nt(const void* p) { return __builtin_nontemporal_load(reinterpret_cast<const u32x4*>(p)); }
DI float ex2(float x) { return __builtin_amdgcn_exp2f(x); }
#define SCHED() __builtin_amdgcn_sched_barrier(0)

typedef __attribute__((address_space(1))) u64 gu64;
typedef __amdgpu_buffer_rsrc_t rsrc_t;
DI u32x4 ld16c(rsrc_t rs, unsigned off) { return __builtin_amdgcn_raw_buffer_load_b128(rs, (int)off, 0, 16); }
DI u32x2 ld8c(rsrc_t rs, unsigned off) { return __builtin_amdgcn_raw_buffer_load_b64(rs, (int)off, 0, 16); }
DI void st8c(rsrc_t rs, unsigned off, u32x2 v) { __builtin_amdgcn_raw_buffer_store_b64(v, rs, (int)off, 0, 16); }
DI void st16c(rsrc_t rs, unsigned off, u32x4 v) { __builtin_amdgcn_raw_buffer_store_b128(v, rs, (int)off, 0, 16); }
constexpr int SPIN_MAX = 1 << 20;
constexpr unsigned OOB = 0x7ffffff0u;
DI void rawbar() { __builtin_amdgcn_sched_barrier(0); __builtin_amdgcn_s_barrier(); __builtin_amdgcn_sched_barrier(0); }
struct Tag { unsigned hi, lo; };
#define COMPILER_FENCE() asm volatile("" ::: "memory")

template <int N> struct IC { static constexpr int v = N; };
template <int I, int N, class F> DI void sfor(F&& f) { if constexpr (I < N) { f(IC<I>{}); sfor<I + 1, N>(f); } }
typedef __attribute__((address_space(3))) unsigned lu32;
DI void lds_wait(unsigned* cnt, unsigned target) {
#pragma unroll 1
  for (int spin = 0; spin < SPIN_MAX; ++spin) {
    if (__builtin_amdgcn_readfirstlane(__hip_atomic_load((lu32*)cnt, __ATOMIC_RELAXED, __HIP_MEMORY_SCOPE_WORKGROUP)) >= target) break;
    __builtin_amdgcn_s_sleep(1);
  }
  COMPILER_FENCE();
}
DI void lds_hold(unsigned* flag) {
#pragma unroll 1
  for (int spin = 0; spin < SPIN_MAX; ++spin) {
    if (__builtin_amdgcn_readfirstlane(__hip_atomic_load((lu32*)flag, __ATOMIC_RELAXED, __HIP_MEMORY_SCOPE_WORKGROUP)) == 0u) break;
    __builtin_amdgcn_s_sleep(2);
  }
}
DI unsigned lds_arrive_nw(unsigned addr) {
  unsigned old, one = 1u;
  asm volatile("s_mov_b64 exec, 1\n\tds_add_rtn_u32 %0, %1, %2\n\ts_mov_b64 exec, -1" : "=v"(old) : "v"(addr), "v"(one) : "memory");
  return old;
}
DI unsigned lds_done(unsigned old) { asm volatile("s_waitcnt lgkmcnt(0)" : "+v"(old) :: "memory"); return __builtin_amdgcn_readfirstlane(old); }
DI unsigned lds_arrive(unsigned* cnt, int) { return lds_done(lds_arrive_nw((unsigned)(size_t)(lu32*)cnt)); }
DI void lds_add(unsigned* cnt, unsigned v, int) {
  asm volatile("s_mov_b64 exec, 1\n\tds_add_u32 %0, %1\n\ts_mov_b64 exec, -1" :: "v"((unsigned)(size_t)(lu32*)cnt), "v"(v) : "memory");
}
DI void sbar4(unsigned* cnt, int lane) { if (lds_arrive(cnt, lane) < 3u) lds_wait(cnt, 4u); }
DI float rs32(float a, float b) { auto t = __builtin_amdgcn_permlane32_swap(__float_as_uint(a), __float_as_uint(b), false, false); return __uint_as_float(t[0]) + __uint_as_float(t[1]); }
DI float rs16(float a, float b) { auto t = __builtin_amdgcn_permlane16_swap(__float_as_uint(a), __float_as_uint(b), false, false); return __uint_as_float(t[0]) + __uint_as_float(t[1]); }
template <int N> DI unsigned rowbcast(unsigned x) { return (unsigned)__builtin_amdgcn_mov_dpp((int)x, 0x150 + N, 0xf, 0xf, true); }
DI void pin2w(u32x8& a, u32x8& b) { asm volatile("" : "+v"(a), "+v"(b)); }
DI float rdlane(float x, int l) { return __builtin_bit_cast(float, __builtin_amdgcn_readlane(__builtin_bit_cast(int, x), l)); }

DI unsigned toff(unsigned row, unsigned ch) { return 256u * row + 16u * (ch ^ (((row & 3) << 2) | ((row >> 2) & 3))); }
constexpr int SMEM = 160 * 1024;
constexpr int L_SCR = 131072;
constexpr int L_MRG = 65536;
constexpr int L_CNT = L_SCR + 32 * 1008;
constexpr int L_ML = L_CNT + 64;

struct KV { u32x4 r[8]; };

typedef const __attribute__((address_space(1))) char* gptr;
struct Args { gptr t[5][4]; gptr w_o; gptr out; };

__global__ __launch_bounds__(512, 2) __attribute__((aligned(4096))) void kmain(Args a) {
  __shared__ __attribute__((aligned(16))) char smem[SMEM];
  const int tid = threadIdx.x, blk = blockIdx.x, lane = tid & 63;
  const int wave = __builtin_amdgcn_readfirstlane(tid >> 6), w4 = wave & 3;
  const int cls = ((blk >> 3) + (blk >> 5)) & 3;
  const int arow = wave < 4 ? cls : 4;
  gptr p0 = a.t[arow][0], p1 = a.t[arow][1], p2 = a.t[arow][2], p3 = a.t[arow][3], p4 = a.w_o, p5 = a.out;
  SCHED();
  int idx = 8 * (blk & 7) + (blk >> 5);
  if (wave < 4) idx = (idx & ~7) | ((idx + (cls == 3 ? 2 : (cls == 2 ? 5 : 0))) & 7);
  const int h = idx >> 1, b = idx & 1;
  const int r4 = lane >> 4, c16 = lane & 15;
  unsigned* cnt = reinterpret_cast<unsigned*>(smem + L_CNT);
  STAMP_DECL;
  TR_DECL;
  STAMP(0);
  Tag tg;
  if (wave >= 4) {
    if (tid - 256 < 16) cnt[tid - 256] = 0u;
    const int nr = cls < 2 ? 8 : 10;
    unsigned tab;
    {
      int j = 2 * nr - 1 - lane;
      j = lane < nr ? lane : (j > 2 ? j : (j == 2 ? 0 : j + 1));
      int n = j + (h * nr >> 5);
      n = n >= nr ? n - nr : n;
      int off = 0, wd = 0;
#pragma unroll
      for (int d = 0; d < 4; ++d) {
        const int k = ((2 * n + 1) * NR[d] + (d < cls) - 1 + nr) * (nr == 8 ? 4096 : 3277) >> 16;
        off += k * (SH[d] / NR[d]) + min(k, SH[d] % NR[d]);
        wd = d == cls ? SH[d] / NR[d] + (n < SH[d] % NR[d]) : wd;
      }
      tab = lane < 2 * nr ? (unsigned)(4 * off + w4 * wd) << 10 | (unsigned)wd : 0u;
    }
    asm volatile("" : "+v"(tab));
    asm volatile("" : "+s"(p0), "+s"(p1), "+s"(p2), "+s"(p3));
    SCHED();
    char* ws = (char*)p3;
    const rsrc_t rs = __builtin_amdgcn_make_buffer_rsrc(ws, 0, (int)WS_END, 0x00020000);
    const char* kb = (const char*)p0 + ((size_t)(b * 32 + h) << 20);
    const char* vb = (const char*)p1 + ((size_t)(b * 32 + h) << 20);
    const unsigned lo = lane * 16u, lo2 = lo + 4096u;
    int nx = 0;
    auto nexts = [&]() {
      const unsigned e = __builtin_amdgcn_readlane(tab, nx);
      const rsrc_t r = __builtin_amdgcn_make_buffer_rsrc(const_cast<char*>((nx < nr ? kb : vb) + (e & ~1023u)), 0, (int)((e & 15u) << 10), 0x00020000);
      ++nx;
      return r;
    };
    const rsrc_t s0 = nexts();
    const int rot = cls;
    const char* wb = (const char*)p2 + h * 131072;
    unsigned wl = r4 * 8192 + w4 * 256 + c16 * 16;
    asm volatile("s_waitcnt lgkmcnt(0)" : "+v"(wl), "+v"(tab) :: "memory");
    rawbar();
    KV A, B, X;
    u32x4 W[8][4];
    u32x8 T[2][8];
    auto ldp = [&](KV& S, auto P, rsrc_t q) {
      constexpr int p = P.v;
      S.r[2 * p] = __builtin_amdgcn_raw_buffer_load_b128(q, (int)((p < 2 ? lo : lo2) + (p & 1) * 2048), 0, 2);
      S.r[2 * p + 1] = __builtin_amdgcn_raw_buffer_load_b128(q, (int)((p < 2 ? lo : lo2) + (p & 1) * 2048 + 1024), 0, 2);
    };
    auto landed = [&](KV& S, auto P) { asm volatile("" : "+v"(S.r[2 * P.v]), "+v"(S.r[2 * P.v + 1])); };
    auto ldw = [&](auto G, auto J) { W[G.v][J.v] = ld16(wb + ((((G.v >> 1) + rot) & 3) * 32768 + (G.v & 1) * 4096) + wl + J.v * 1024); };
    auto conv = [&](auto G) {
      constexpr int t = G.v >> 2, j0 = (G.v & 3) * 2;
#pragma unroll
      for (int jj = 0; jj < 2; ++jj)
#pragma unroll
        for (int e = 0; e < 4; ++e) {
          const unsigned a_ = W[G.v][2 * jj][e], b_ = W[G.v][2 * jj + 1][e];
          T[t][2 * e][j0 + jj] = plo(a_, b_); T[t][2 * e + 1][j0 + jj] = phi(a_, b_);
        }
#pragma unroll
      for (int e = 0; e < 8; e += 2) pin2w(T[t][e], T[t][e + 1]);
    };
    const unsigned qo = WS_QX + (((2 + b) * 32 + h) * 64 + 4 * c16) * 8;
    u32x4 g0, g1;
    u32x2 tw;
    {
      const rsrc_t s1 = nexts(), s2 = nexts();
      sfor<0, 12>([&](auto I) {
        constexpr int i = I.v;
        if constexpr (i >= 4) { if constexpr (i < 8) landed(X, IC<i & 3>{}); else landed(B, IC<i & 3>{}); }
        if constexpr (i == 7 || i == 11) TR();
        if constexpr (i == 8) tw = ld8c(rs, lane == 0 ? WS_END - 128 : OOB);
        if constexpr (i == 9) { g0 = ld16c(rs, qo); g1 = ld16c(rs, qo + 16); }
        if constexpr (i < 4) { ldp(X, IC<i & 3>{}, s0); ldw(IC<0>{}, IC<i & 3>{}); }
        else if constexpr (i < 8) { ldp(B, IC<i & 3>{}, s1); ldw(IC<1>{}, IC<i & 3>{}); }
        else { ldp(A, IC<i & 3>{}, s2); ldw(IC<2>{}, IC<i & 3>{}); }
        SCHED();
        if constexpr (i == 8) { conv(IC<0>{}); SCHED(); }
      });
    }
    lds_wait(cnt + 15, 1u);
    tg.hi = __builtin_amdgcn_readfirstlane(cnt[13]); tg.lo = __builtin_amdgcn_readfirstlane(cnt[14]);
    asm volatile("" :: "v"(tw));
    u32x4 qpk;
    {
      SCHED();
      conv(IC<1>{});
      SCHED();
      if (!__all((g0[1] == tg.hi) & (g0[3] == tg.hi) & (g1[1] == tg.hi) & (g1[3] == tg.hi))) {
#pragma unroll 1
        for (int spin = 0; spin < SPIN_MAX; ++spin) {
          COMPILER_FENCE();
          g0 = ld16c(rs, qo); g1 = ld16c(rs, qo + 16);
          if (__all((g0[1] == tg.hi) & (g0[3] == tg.hi) & (g1[1] == tg.hi) & (g1[3] == tg.hi))) break;
        }
      }
      qpk = u32x4{g0[0], g0[2], g1[0], g1[2]};
    }
    STAMP(7);
    const bool holds = cls == 0;
    float sc[10], acc[8];
#pragma unroll
    for (int k = 0; k < 10; ++k) sc[k] = 0.f;
    auto point = [&](KV& S, KV& N, rsrc_t sn, auto P, auto REFILL, auto AH, auto WG) {
      constexpr int ah = AH.v;
      if constexpr (REFILL.v) {
        if constexpr (P.v == 0 && ah > 0) { if (holds) lds_hold(cnt + 8); sfor<0, ah>([&](auto Q) { ldp(N, Q, sn); }); SCHED(); }
        landed(S, P);
        if constexpr (P.v + ah < 4) { if constexpr ((P.v & 1) == 0) { if (holds) lds_hold(cnt + 8); } ldp(N, IC<(P.v + ah) & 3>{}, sn); }
        if constexpr (WG.v >= 0) ldw(WG, P);
        SCHED();
      }
    };
    auto step = [&](KV& S, KV& N, auto REFILL, auto WG, auto ISV) {
      rsrc_t sn = s0;
      if constexpr (REFILL.v) sn = nexts();
      sfor<0, 4>([&](auto P) {
        point(S, N, sn, P, REFILL, IC<(ISV.v || WG.v < 0 ? 1 : 0)>{}, WG);
        if constexpr (!ISV.v) {
#pragma unroll
          for (int t = 2 * P.v; t < 2 * P.v + 2; ++t) {
            acc[t] = 0.f;
#pragma unroll
            for (int k = 0; k < 4; ++k) acc[t] = dot2(S.r[t][k], qpk[k], acc[t]);
          }
          asm volatile("" : "+v"(acc[2 * P.v]), "+v"(acc[2 * P.v + 1]));
        } else {
          const unsigned pp = rowbcast<4 * P.v>(__float_as_uint(sc[0]));
#pragma unroll
          for (int k = 0; k < 4; ++k) { acc[2 * k] = dot2(plo(S.r[2 * P.v][k], S.r[2 * P.v + 1][k]), pp, acc[2 * k]); acc[2 * k + 1] = dot2(phi(S.r[2 * P.v][k], S.r[2 * P.v + 1][k]), pp, acc[2 * k + 1]); }
          asm volatile("" : "+v"(acc[0]), "+v"(acc[1]), "+v"(acc[2]), "+v"(acc[3]), "+v"(acc[4]), "+v"(acc[5]), "+v"(acc[6]), "+v"(acc[7]));
        }
        SCHED();
      });
      if constexpr (!ISV.v) {
        float u[4], v[2];
#pragma unroll
        for (int i = 0; i < 4; ++i) u[i] = (c16 & 8 ? acc[i + 4] : acc[i]) + dpp<0x128>(c16 & 8 ? acc[i] : acc[i + 4]);
#pragma unroll
        for (int i = 0; i < 2; ++i) v[i] = (c16 & 4 ? u[i + 2] : u[i]) + dpp<0x141>(c16 & 4 ? u[i] : u[i + 2]);
        float x = (c16 & 2 ? v[1] : v[0]) + dpp<0x4E>(c16 & 2 ? v[0] : v[1]);
        x += dpp<0xB1>(x);
#pragma unroll
        for (int k = 9; k > 0; --k) sc[k] = sc[k - 1];
        sc[0] = x;
      } else {
#pragma unroll
        for (int k = 0; k < 9; ++k) sc[k] = sc[k + 1];
      }
      SCHED();
      if constexpr (REFILL.v || ISV.v) TR();
    };
    step(B, B, IC<0>{}, IC<-1>{}, IC<0>{});
    step(A, B, IC<1>{}, IC<3>{}, IC<0>{});
    step(X, X, IC<0>{}, IC<-1>{}, IC<0>{});
    conv(IC<2>{});
    sfor<3, 6>([&](auto I) {
      if constexpr (I.v & 1) step(B, A, IC<1>{}, IC<I.v + 1>{}, IC<0>{}); else step(A, B, IC<1>{}, IC<I.v + 1>{}, IC<0>{});
      conv(I);
    });
#pragma unroll 1
    for (int i = 6; i < nr; i += 2) {
      step(A, B, IC<1>{}, IC<-1>{}, IC<0>{});
      step(B, A, IC<1>{}, IC<-1>{}, IC<0>{});
    }
    float m = -INFINITY, l = 0.f;
#pragma unroll
    for (int k = 0; k < 10; ++k) { sc[k] = (unsigned)(c16 >> 1) < (__builtin_amdgcn_readlane(tab, nr + k) & 15u) ? sc[k] : -INFINITY; m = fmaxf(m, sc[k]); }
    m = row16_max(m);
    {
      const float mc = m * C2;
#pragma unroll
      for (int k = 0; k < 10; ++k) {
        const float p = ex2(fmaf(sc[k], C2, -mc));
        const unsigned pk = pk_bf(dpp<0x00>(p), dpp<0xAA>(p));
        l = dot2(pk, 0x3f803f80u, l);
        sc[k] = __uint_as_float(pk);
      }
      l += dpp<0x124>(l); l += dpp<0x128>(l);
      *reinterpret_cast<float*>(smem + L_ML + ((c16 & 1) * 16 + w4 * 4 + r4) * 4) = (c16 & 1) ? l : m;
      COMPILER_FENCE();
      const unsigned got = lds_arrive_nw(L_CNT + 40);
      const float mr = *reinterpret_cast<const float*>(smem + L_ML + c16 * 4), lr = *reinterpret_cast<const float*>(smem + L_ML + 64 + c16 * 4);
      SCHED();
      const bool last = lds_done(got) == 3u;
      const float M = row16_max(mr);
      st8c(rs, last && lane == 0 ? WS_GZ + (b * 128 + h * 4 + cls) * 128 : OOB, u32x2{__float_as_uint(fmaf(M, C2, __builtin_amdgcn_logf(row16_sum(ex2((mr - M) * C2) * lr)))), tg.hi});
    }
#pragma unroll
    for (int k = 0; k < 8; ++k) acc[k] = 0.f;
    SCHED();
    STAMP(8);
    unsigned yoff = (r4 & 1) ? OOB : WS_GB + (b * 128 + h * 4 + cls) * 2048 + 512 * w4 + 32 * c16 + 16 * (lane >> 5), zoff = WS_GZ + (b * 128 + h * 4 + (lane & 3)) * 128;
    unsigned rwo = L_MRG + (w4 * 4 + r4) * 144 + ((c16 - 4 * rot) & 15) * 8, rro = L_MRG + c16 * 144 + r4 * 16;
    asm volatile("" : "+v"(yoff), "+v"(zoff), "+v"(rwo), "+v"(rro));
    u32x2 zq;
    step(A, B, IC<1>{}, IC<7>{}, IC<1>{});
    conv(IC<6>{});
#pragma unroll 1
    for (int i = 2;;) {
      step(B, A, IC<1>{}, IC<-1>{}, IC<1>{});
      i += 2;
      zq = ld8c(rs, i >= nr ? zoff : OOB);
      SCHED();
      step(A, B, IC<1>{}, IC<-1>{}, IC<1>{});
      if (i >= nr) break;
    }
    conv(IC<7>{});
    if (!__all(zq[1] == tg.hi)) {
#pragma unroll 1
      for (int spin = 0; spin < SPIN_MAX; ++spin) {
        COMPILER_FENCE();
        zq = ld8c(rs, zoff);
        if (__all(zq[1] == tg.hi)) break;
        __builtin_amdgcn_s_sleep(8);
      }
    }
    float wt;
    int six = (lane & 1) ? 0xEEEE : 0x4444;
    {
      const float z = __uint_as_float(zq[0]);
      float Z = fmaxf(z, dpp<0xB1>(z)); Z = fmaxf(Z, dpp<0x4E>(Z));
      float L = ex2(z - Z); L += dpp<0xB1>(L); L += dpp<0x4E>(L);
      Z += __builtin_amdgcn_logf(L);
      wt = ex2(fmaf(m, C2, -Z));
      asm volatile("" : "+v"(wt), "+v"(six));
    }
    f32x4 d[8];
#pragma unroll
    for (int e = 0; e < 8; ++e) { d[e] = f32x4{0.f, 0.f, 0.f, 0.f}; asm volatile("" : "+v"(d[e])); }
    if (cls == 0) lds_wait(cnt + 1, 4u);
    SCHED();
    step(B, B, IC<0>{}, IC<-1>{}, IC<1>{});
    STAMP(1);
#pragma unroll
    for (int k = 0; k < 8; ++k) acc[k] *= wt;
    *reinterpret_cast<u32x2*>(smem + rwo) = u32x2{pk_bf(rs16(acc[0], acc[2]), rs16(acc[1], acc[3])), pk_bf(rs16(acc[4], acc[6]), rs16(acc[5], acc[7]))};
    {
      u32x4 af[2];
      unsigned got = lds_arrive_nw(L_CNT), need = 3u;
#pragma unroll 1
      for (int spin = 0; spin < SPIN_MAX; ++spin) {
#pragma unroll
        for (int t = 0; t < 2; ++t) af[t] = *reinterpret_cast<const u32x4*>(smem + rro + t * 64);
        SCHED();
        if (lds_done(got) >= need) break;
        need = 4u;
        __builtin_amdgcn_s_sleep(1);
        asm volatile("ds_read_b32 %0, %1" : "=v"(got) : "v"((unsigned)L_CNT) : "memory");
      }
      SCHED();
      STAMP(3);
      float v4[4];
#pragma unroll
      for (int k = 0; k < 4; ++k)
#pragma unroll
        for (int t = 0; t < 2; ++t) {
          d[k] = __builtin_amdgcn_smfmac_f32_16x16x64_bf16(__builtin_bit_cast(bf16x8_t, af[t]), __builtin_bit_cast(bf16x16_t, T[t][k]), d[k], six, 0, 0);
          d[k + 4] = __builtin_amdgcn_smfmac_f32_16x16x64_bf16(__builtin_bit_cast(bf16x8_t, af[t]), __builtin_bit_cast(bf16x16_t, T[t][k + 4]), d[k + 4], six, 0, 0);
        }
#pragma unroll
      for (int k = 0; k < 4; ++k) {
        const f32x2 x = d[k].xy + d[k].zw, y = d[k + 4].xy + d[k + 4].zw;
        v4[k] = x16_sum(rs32(x[0] + x[1], y[0] + y[1]));
      }
      STAMP(4);
      st16c(rs, yoff, u32x4{pk_bf(v4[0], v4[1]), tg.hi, pk_bf(v4[2], v4[3]), tg.lo});
      SCHED();
      lds_add(cnt + 11, 1u, lane);
    }
    STAMP(2);
    STAMP_FLUSH(1); TR_FLUSH();
    return;
  }
  u32x4 Wa[16], Wb[16], xv[4];
  unsigned pf;
  u64 epw, tstart;
  {
    unsigned oa[16];
    oa[0] = (cls & 1) ? (w4 * 128 + 16 * (lane >> 3)) * 8192 + idx * 128 + 16 * (lane & 7) : (cls == 2 ? (b * 256 + w4 * 64 + 16 * r4) * 8192 + h * 256 + 16 * c16 : (idx * 128 + w4) * 1024 + 16 * lane);
#pragma unroll
    for (int i = 1; i < 16; ++i) oa[i] = oa[i - 1] + (cls ? 8192 : 4096);
    unsigned op = (((blk >> 5) * 4 + w4) * 16 + c16) * 8192 + ((idx & 56) + 3 + (cls == 0 ? -1 : (idx >= 56 ? 4 : 0)) * (lane >> 5)) * 128 + (r4 & 1) * 64;
    unsigned ox = w4 * 256 + 32 * (lane >> 3);
#pragma unroll
    for (int i = 0; i < 16; i += 8) asm volatile("" : "+v"(oa[i]), "+v"(oa[i + 1]), "+v"(oa[i + 2]), "+v"(oa[i + 3]), "+v"(oa[i + 4]), "+v"(oa[i + 5]), "+v"(oa[i + 6]), "+v"(oa[i + 7]));
    asm volatile("" : "+v"(op), "+v"(ox));
    asm volatile("" : "+s"(p0), "+s"(p1), "+s"(p2), "+s"(p3));
    SCHED();
    tstart = wall_clock64();
    SCHED();
    pf = *reinterpret_cast<const unsigned*>((const char*)p1 + op);
    epw = __hip_atomic_load((gu64*)p3, __ATOMIC_RELAXED, __HIP_MEMORY_SCOPE_AGENT);
#pragma unroll
    for (int i = 0; i < 4; ++i) xv[i] = ld16((const char*)p2 + ox + (i >> 1) * 1024 + 16 * (i & 1));
    SCHED();
#pragma unroll
    for (int i = 0; i < 16; ++i) { Wa[i] = ld16nt((const char*)p0 + oa[i]); SCHED(); }
  }
  char* ws = (char*)p3;
  const rsrc_t rs = __builtin_amdgcn_make_buffer_rsrc(ws, 0, (int)WS_END, 0x00020000);
  u16* out = (u16*)p5;
  {
    rawbar();
    const u64 t64 = (((u64)(unsigned)__builtin_amdgcn_readfirstlane((unsigned)(epw >> 32)) << 32) | (unsigned)__builtin_amdgcn_readfirstlane((unsigned)epw)) + GOLD;
    tg.hi = (unsigned)(t64 >> 32); tg.lo = (unsigned)t64;
    asm volatile("" : "+s"(tg.hi), "+s"(tg.lo) : "v"(pf) : "memory");
    if (w4 == 0) { cnt[13] = tg.hi; cnt[14] = tg.lo; COMPILER_FENCE(); lds_add(cnt + 15, 1u, lane); }
    STAMP(13);
  }
  float* red = reinterpret_cast<float*>(smem);
  auto gemv = [&](float* dst) {
    float acc[2][8];
#pragma unroll
    for (int bb = 0; bb < 2; ++bb)
#pragma unroll
      for (int k = 0; k < 8; ++k) acc[bb][k] = 0.f;
#pragma unroll
    for (int ip = 0; ip < 8; ++ip) {
      const unsigned xp0 = xv[ip >> 2][ip & 3], xp1 = xv[2 + (ip >> 2)][ip & 3];
#pragma unroll
      for (int k = 0; k < 4; ++k) {
        const unsigned lo = plo(Wa[2 * ip][k], Wa[2 * ip + 1][k]), hi = phi(Wa[2 * ip][k], Wa[2 * ip + 1][k]);
        acc[0][2 * k] = dot2(lo, xp0, acc[0][2 * k]); acc[0][2 * k + 1] = dot2(hi, xp0, acc[0][2 * k + 1]);
        acc[1][2 * k] = dot2(lo, xp1, acc[1][2 * k]); acc[1][2 * k + 1] = dot2(hi, xp1, acc[1][2 * k + 1]);
      }
    }
    float v8[8], v4[4];
#pragma unroll
    for (int k = 0; k < 8; ++k) v8[k] = rs32(acc[0][k], acc[1][k]);
#pragma unroll
    for (int k = 0; k < 4; ++k) { const float t = rs16(v8[k], v8[k + 4]); v4[k] = t + dpp<0x128>(t); }
    *reinterpret_cast<f32x4*>(dst + (lane >> 5) * 64 + 8 * (lane & 7) + 4 * (r4 & 1)) = f32x4{v4[0], v4[1], v4[2], v4[3]};
    COMPILER_FENCE();
  };
  if (cls & 1) {
    gemv(red + w4 * 128);
    const unsigned got = lds_arrive_nw(L_CNT + 8);
    f32x4 t4[4];
#pragma unroll
    for (int w = 0; w < 4; ++w) t4[w] = *reinterpret_cast<const f32x4*>(red + w * 128 + 4 * (lane & 31));
    SCHED();
    if (lds_done(got) == 3u) {
      const f32x4 t = (t4[0] + t4[1]) + (t4[2] + t4[3]);
      st16c(rs, lane < 32 ? WS_QX + ((((cls >> 1) * 2 + r4) * 32 + h) * 64 + b * 32 + 2 * c16) * 8 : OOB, u32x4{pk_bf(t[0], t[1]), tg.hi, pk_bf(t[2], t[3]), tg.hi});
      SCHED();
      lds_add(cnt + 2, 1u, lane);
    } else lds_wait(cnt + 2, 5u);
    STAMP(8);
  } else if (cls == 2) {
    while (wall_clock64() - tstart < TQX) __builtin_amdgcn_s_sleep(4);
    u32x4 q0, q1;
    const unsigned qo = WS_QX + (h * 64 + 4 * c16) * 8;
#pragma unroll 1
    for (int spin = 0; spin < SPIN_MAX; ++spin) {
      COMPILER_FENCE();
      const u32x4 g0 = ld16c(rs, qo), g1 = ld16c(rs, qo + 16), g2 = ld16c(rs, qo + 16384), g3 = ld16c(rs, qo + 16400);
      q0 = u32x4{g0[0], g0[2], g1[0], g1[2]}; q1 = u32x4{g2[0], g2[2], g3[0], g3[2]};
      if (__all((g0[1] == tg.hi) & (g0[3] == tg.hi) & (g1[1] == tg.hi) & (g1[3] == tg.hi) & (g2[1] == tg.hi) & (g2[3] == tg.hi) & (g3[1] == tg.hi) & (g3[3] == tg.hi))) break;
      __builtin_amdgcn_s_sleep(1);
    }
    STAMP(9);
    float v[4];
    {
      float u[16], t8[8];
#pragma unroll
      for (int i = 0; i < 16; ++i) {
        float a = 0.f, c = 0.f;
#pragma unroll
        for (int k = 0; k < 4; ++k) { a = dot2(Wa[i][k], q0[k], a); c = dot2(Wa[i][k], q1[k], c); }
        u[i] = (c16 & 4 ? c : a) + dpp<0x141>(c16 & 4 ? a : c);
        asm volatile("" : "+v"(u[i]));
      }
#pragma unroll
      for (int i = 0; i < 8; ++i) { t8[i] = (c16 & 2 ? u[i + 8] : u[i]) + dpp<0x4E>(c16 & 2 ? u[i] : u[i + 8]); asm volatile("" : "+v"(t8[i])); }
#pragma unroll
      for (int i = 0; i < 4; ++i) {
        float t = (c16 & 1 ? t8[i + 4] : t8[i]) + dpp<0xB1>(c16 & 1 ? t8[i] : t8[i + 4]);
        asm volatile("" : "+v"(t));
        v[i] = t + dpp<0x128>(t);
      }
    }
    st16c(rs, c16 < 8 ? WS_QA + ((c16 >> 2) * 32 + h) * 2048 + (b * 256 + w4 * 64 + 16 * r4 + 4 * (c16 & 3)) * 4 : OOB, u32x4{pk_bf(v[0], v[1]), tg.hi, pk_bf(v[2], v[3]), tg.hi});
    STAMP(10);
  } else {
    {
      const u16* pb = (const u16*)p0 + (idx * 128 + 64 + w4) * 512 + 8 * lane;
#pragma unroll
      for (int i = 0; i < 16; ++i) {
        Wb[i] = ld16nt(pb + i * 2048);
        if (i >= 4 && (i & 1)) asm volatile("" : "+v"(Wb[i - 4]));
        if (i == 9) TR();
        SCHED();
      }
    }
    lds_add(cnt + 8, 1u, lane);
#pragma unroll
    for (int i = 0; i < 16; ++i) *reinterpret_cast<u32x4*>(smem + ((i >> 3) * 4 + r4) * 8192 + toff(4 * (i & 7) + w4, c16)) = Wa[i];
#pragma unroll
    for (int i = 0; i < 16; ++i) *reinterpret_cast<u32x4*>(smem + ((2 + (i >> 3)) * 4 + r4) * 8192 + toff(4 * (i & 7) + w4, c16)) = Wb[i];
    STAMP(8);
    TR();
  }
  if (cls >= 2) {
    const unsigned tw = __builtin_nontemporal_load(reinterpret_cast<const unsigned*>(out + (idx >> 5) * 1024 + (cls & 1) * 512 + 16 * (idx & 31)));
    asm volatile("" :: "v"(tw));
  }
  asm volatile("" :: "v"(xv[0]), "v"(xv[1]), "v"(xv[2]), "v"(xv[3]));
  SCHED();
  if (cls >= 2) {
    unsigned d0 = (cls == 3 ? WS_GB + (idx >> 5) * 128 * 2048 : WS_GA + (idx >> 5) * 64 * 2048) + (16 * w4 + (lane >> 2)) * 2048 + (idx & 31) * 64 + (lane & 3) * 16;
    unsigned d1 = cls == 3 ? d0 + 64 * 2048 : d0;
    unsigned fw = (lane & 12) ? 32768 + 256 + lane * 4 : 32768 + (w4 * 16 + 4 * (lane & 3) + r4) * 4, fr = 32768 + 8 * (lane & 7);
    unsigned oo = lane < 8 ? ((idx >> 5) * 1024 + (cls & 1) * 512 + 16 * (idx & 31) + 2 * lane) * 2 : OOB;
    float f1 = cls == 3 ? 1.f : 0.f;
    const rsrc_t ro = __builtin_amdgcn_make_buffer_rsrc(out, 0, 4096, 0x00020000);
    asm volatile("" : "+v"(d0), "+v"(d1), "+v"(fw), "+v"(fr), "+v"(oo), "+v"(f1));
    lds_wait(cnt + 11, 4u);
    STAMP(3);
    u32x4 x, y;
    bool ok = false;
#pragma unroll 1
    for (int spin = 0; spin < SPIN_MAX; ++spin) {
      COMPILER_FENCE();
      if (!ok) { x = ld16c(rs, d0); y = ld16c(rs, d1); }
      ok = (((x[1] ^ tg.hi) | (y[1] ^ tg.hi)) | ((x[3] ^ tg.lo) | (y[3] ^ tg.lo))) == 0u;
      if (__all(ok)) break;
    }
    STAMP(14);
    float s = rs16(rs32(fmaf(f1, bflo(y[0]), bflo(x[0])), fmaf(f1, bflo(y[2]), bflo(x[2]))), rs32(fmaf(f1, bfhi(y[0]), bfhi(x[0])), fmaf(f1, bfhi(y[2]), bfhi(x[2]))));
    s += dpp<0x124>(s); s += dpp<0x128>(s);
    *reinterpret_cast<float*>(smem + fw) = s;
    COMPILER_FENCE();
    const unsigned got = lds_arrive_nw(L_CNT + 24);
    f32x2 t[4];
#pragma unroll
    for (int w = 0; w < 4; ++w) t[w] = *reinterpret_cast<const f32x2*>(smem + fr + w * 64);
    SCHED();
    if (lds_done(got) == 3u) {
      const f32x2 r = (t[0] + t[1]) + (t[2] + t[3]);
      __builtin_amdgcn_raw_buffer_store_b32(pk_bf(r[0], r[1]), ro, (int)oo, 0, 0);
    }
    STAMP(6);
    STAMP_FLUSH(0); TR_FLUSH();
    return;
  }
  if (cls == 0) {
    char* scr = smem + L_SCR;
    const int ab = idx >> 5, r = lane & 31, hh = lane >> 5;
    u32x4 qlast = {0u, 0u, 0u, 0u};
    {
      u32x4 g[18];
      const int fl = (idx & 1) * 8;
      const unsigned o8 = WS_QA + (ab * 32 + w4 * 8) * 2048 + lane * 16, ol = WS_QA + (ab * 32 + r) * 2048 + 1984 + 32 * hh;
      while (wall_clock64() - tstart < TQA) __builtin_amdgcn_s_sleep(2);
      unsigned pend = 0x3ffffu;
#pragma unroll 1
      for (int spin = 0; spin < SPIN_MAX && pend; ++spin) {
        COMPILER_FENCE();
#pragma unroll
        for (int i = 0; i < 18; ++i) g[i] = ld16c(rs, (pend >> i & 1) ? (i < 16 ? o8 + (i ^ fl) * 1024 : ol + 16 * (i - 16)) : OOB);
        if (spin == 0) { SCHED(); lds_add(cnt + 8, ~0u, lane); SCHED(); }
#pragma unroll
        for (int i = 0; i < 18; ++i) {
          if ((pend >> i & 1) && __all((g[i][1] == tg.hi) & (g[i][3] == tg.hi))) {
            pend &= ~(1u << i);
            if (i >= 16) { qlast[2 * (i - 16)] = g[i][0]; qlast[2 * (i - 16) + 1] = g[i][2]; }
            else if (!(i & 1) || lane < 60) *reinterpret_cast<u32x2*>(scr + (w4 * 8 + ((i ^ fl) >> 1)) * 1008 + (i & 1) * 512 + 8 * lane) = u32x2{g[i][0], g[i][2]};
          }
        }
        if (pend) __builtin_amdgcn_s_sleep(8);
      }
    }
    sbar4(cnt + 3, lane);
    STAMP(10);
    f32x16 sacc;
#pragma unroll
    for (int i = 0; i < 16; ++i) sacc[i] = 0.f;
#pragma unroll
    for (int jj = 0; jj < 32; ++jj) {
      const u32x4 af = *reinterpret_cast<const u32x4*>(smem + (w4 * 4 + (jj >> 3)) * 8192 + toff(r, 2 * (jj & 7) + hh));
      const u32x4 bf = jj < 31 ? *reinterpret_cast<const u32x4*>(scr + r * 1008 + 16 * (2 * (jj < 31 ? jj : 0) + hh)) : qlast;
      sacc = __builtin_amdgcn_mfma_f32_32x32x16_bf16(__builtin_bit_cast(bf16x8_t, af), __builtin_bit_cast(bf16x8_t, bf), sacc, 0, 0, 0);
    }
    float m = sacc[0];
#pragma unroll
    for (int i = 1; i < 16; ++i) m = fmaxf(m, sacc[i]);
    m = xhalf_max(m);
    if (hh == 0) *reinterpret_cast<float*>(scr + r * 1008 + 992 + 4 * w4) = m;
    sbar4(cnt + 4, lane);
    STAMP(11);
    {
      const f32x4 m4 = *reinterpret_cast<const f32x4*>(scr + r * 1008 + 992);
      m = fmaxf(fmaxf(m4[0], m4[1]), fmaxf(m4[2], m4[3]));
    }
    const float mc = m * C2;
    float p[16], ls = 0.f;
#pragma unroll
    for (int i = 0; i < 16; ++i) { p[i] = ex2(fmaf(sacc[i], C2, -mc)); ls += p[i]; }
    ls = xhalf_sum(ls);
    sbar4(cnt + 5, lane);
    u32x4* pf = reinterpret_cast<u32x4*>(scr);
    float* wl = reinterpret_cast<float*>(scr + 8192);
    if (hh == 0) wl[r * 4 + w4] = ls;
    pf[(2 * w4) * 64 + lane] = u32x4{pk_bf(p[0], p[1]), pk_bf(p[2], p[3]), pk_bf(p[4], p[5]), pk_bf(p[6], p[7])};
    pf[(2 * w4 + 1) * 64 + lane] = u32x4{pk_bf(p[8], p[9]), pk_bf(p[10], p[11]), pk_bf(p[12], p[13]), pk_bf(p[14], p[15])};
    sbar4(cnt + 6, lane);
    u32x4 pb8[8];
#pragma unroll
    for (int ks = 0; ks < 8; ++ks) pb8[ks] = pf[ks * 64 + lane];
    const f32x4 l4 = *reinterpret_cast<const f32x4*>(wl + r * 4);
    const int qq = (lane & 15) >> 2, pp = lane & 3, blk2 = (lane >> 4) & 1;
    char* ot = scr + 12288 + w4 * 4096;
#pragma unroll
    for (int c2 = 0; c2 < 2; ++c2) {
      f32x16 oacc[2];
#pragma unroll
      for (int ct = 0; ct < 2; ++ct)
#pragma unroll
        for (int i = 0; i < 16; ++i) oacc[ct][i] = 0.f;
#pragma unroll
      for (int ks = 0; ks < 8; ++ks)
#pragma unroll
        for (int ct = 0; ct < 2; ++ct) {
          typedef __attribute__((address_space(3))) s16x4* lp;
          const char* tile = smem + ((ks >> 1) * 4 + w4) * 8192;
          const int cc = 2 * c2 + ct;
          const s16x4 lo = __builtin_amdgcn_ds_read_tr16_b64_v4i16((lp)(tile + toff(16 * (ks & 1) + 4 * hh + qq, 4 * cc + 2 * blk2 + (pp >> 1)) + 8 * (pp & 1)));
          const s16x4 hi = __builtin_amdgcn_ds_read_tr16_b64_v4i16((lp)(tile + toff(16 * (ks & 1) + 8 + 4 * hh + qq, 4 * cc + 2 * blk2 + (pp >> 1)) + 8 * (pp & 1)));
          const s16x8 afr = __builtin_shufflevector(lo, hi, 0, 1, 2, 3, 4, 5, 6, 7);
          oacc[ct] = __builtin_amdgcn_mfma_f32_32x32x16_bf16(__builtin_bit_cast(bf16x8_t, afr), __builtin_bit_cast(bf16x8_t, pb8[ks]), oacc[ct], 0, 0, 0);
        }
#pragma unroll
      for (int ct = 0; ct < 2; ++ct)
#pragma unroll
        for (int g = 0; g < 4; ++g)
          *reinterpret_cast<u32x2*>(ot + r * 128 + 16 * ((4 * ct + g) ^ (r & 7)) + 8 * hh) = u32x2{pk_bf(oacc[ct][4 * g], oacc[ct][4 * g + 1]), pk_bf(oacc[ct][4 * g + 2], oacc[ct][4 * g + 3])};
      if (c2 == 0) lds_add(cnt + 8, 1u, lane);
      __builtin_amdgcn_wave_barrier();
#pragma unroll
      for (int i = 0; i < 4; ++i) {
        const int hd = 8 * i + (lane >> 3), pc = lane & 7;
        const u32x4 vv = *reinterpret_cast<const u32x4*>(ot + hd * 128 + 16 * (pc ^ (hd & 7)));
        st16c(rs, WS_APO + ((hd * 64 + idx) * 512 + w4 * 128 + 64 * c2 + 8 * pc) * 2, vv);
      }
      __builtin_amdgcn_wave_barrier();
    }
    lds_arrive(cnt + 1, lane);
    STAMP(1);
    asm volatile("s_waitcnt vmcnt(0)" ::: "memory");
    if (lds_arrive(cnt + 9, lane) == 3u && hh == 0)
      st16c(rs, WS_AML + (idx * 32 + r) * 16, u32x4{__float_as_uint(m), tg.hi, __float_as_uint((l4[0] + l4[1]) + (l4[2] + l4[3])), tg.hi});
    lds_add(cnt + 8, ~0u, lane);
    STAMP(2);
    if (idx == 0 && w4 == 0) {
      lds_wait(cnt + 10, 4u);
#pragma unroll 1
      for (int spin = 0; spin < SPIN_MAX; ++spin) {
        COMPILER_FENCE();
        const u32x2 t0 = ld8c(rs, WS_AML + lane * 512), t1 = ld8c(rs, WS_QX + (64 * 64 + lane * 32) * 8);
        if (__all((t0[1] == tg.hi) & (t1[1] == tg.hi))) break;
        __builtin_amdgcn_s_sleep(8);
      }
      if (lane == 0) __hip_atomic_store((gu64*)(ws + WS_EP), ((u64)tg.hi << 32) | tg.lo, __ATOMIC_RELAXED, __HIP_MEMORY_SCOPE_AGENT);
    }
    STAMP_FLUSH(0); TR_FLUSH();
    return;
  }
  if (cls == 1) {
    {
      const u16* pa = (const u16*)p1 + (w4 * 128 + 16 * (lane >> 3)) * 4096 + h * 128 + b * 64 + 8 * (lane & 7);
      const u16* pb = (const u16*)p4 + (h * 128 + b * 64 + w4 * 2) * 512 + 8 * lane;
#pragma unroll
      for (int i = 0; i < 16; ++i) {
        Wa[i] = ld16nt(pa + i * 4096);
        SCHED();
        if ((i & 1) && !((idx & 7) == 3 || idx == 63)) asm volatile("s_waitcnt vmcnt(6)" ::: "memory");
        if ((i & 7) == 7) TR();
        SCHED();
      }
#pragma unroll
      for (int i = 0; i < 16; ++i) {
        Wb[i] = ld16nt(pb + (i >> 1) * 4096 + (i & 1) * 512);
        SCHED();
        if (i & 1) asm volatile("s_waitcnt vmcnt(6)" ::: "memory");
        if ((i & 7) == 7) TR();
        SCHED();
      }
    }
    float* red2 = red + 2048;
    float* wsh = red + 3072;
    if (w4 == 0) {
      const unsigned fo = WS_AML + (lane * 32 + h) * 16;
      while (wall_clock64() - tstart < TAML) __builtin_amdgcn_s_sleep(8);
      u32x4 g;
      bool ok = false;
#pragma unroll 1
      for (int spin = 0; spin < SPIN_MAX; ++spin) {
        COMPILER_FENCE();
        if (!ok) g = ld16c(rs, fo);
        ok = (g[1] == tg.hi) & (g[3] == tg.hi);
        if (__all(ok)) break;
        __builtin_amdgcn_s_sleep(2);
      }
      lds_add(cnt + 7, 1u, lane);
      const float mu = __uint_as_float(g[0]), lu = __uint_as_float(g[2]);
      const float M = x16_max(row16_max(mu));
      const float wl = ex2((mu - M) * C2);
      wsh[lane] = wl / x16_sum(row16_sum(wl * lu));
      lds_arrive(cnt + 7, lane);
    } else lds_wait(cnt + 7, 1u);
    COMPILER_FENCE();
    STAMP(3);
    u32x4 pp[16];
#pragma unroll
    for (int i = 0; i < 16; ++i) { pp[i] = ld16c(rs, WS_APO + (h * 64 + w4 * 16 + (i ^ (8 * b))) * 1024 + lane * 16); SCHED(); }
    lds_wait(cnt + 7, 2u);
    float a8[8];
#pragma unroll
    for (int k = 0; k < 8; ++k) a8[k] = 0.f;
    const float wv = wsh[w4 * 16 + (c16 ^ (8 * b))];
#pragma unroll
    for (int i = 0; i < 16; ++i) {
      const float wt = rdlane(wv, i);
#pragma unroll
      for (int k = 0; k < 4; ++k) { a8[2 * k] = fmaf(wt, bflo(pp[i][k]), a8[2 * k]); a8[2 * k + 1] = fmaf(wt, bfhi(pp[i][k]), a8[2 * k + 1]); }
      asm volatile("" : "+v"(a8[0]), "+v"(a8[1]), "+v"(a8[2]), "+v"(a8[3]), "+v"(a8[4]), "+v"(a8[5]), "+v"(a8[6]), "+v"(a8[7]));
      SCHED();
    }
    *reinterpret_cast<f32x4*>(red + w4 * 512 + 8 * lane) = f32x4{a8[0], a8[1], a8[2], a8[3]};
    *reinterpret_cast<f32x4*>(red + w4 * 512 + 8 * lane + 4) = f32x4{a8[4], a8[5], a8[6], a8[7]};
    sbar4(cnt + 3, lane);
    STAMP(4);
#pragma unroll
    for (int i = 0; i < 4; ++i) {
      const float* src = red + (i >> 1) * 1024 + w4 * 128 + 16 * (lane >> 3) + 8 * (i & 1);
      const f32x4 t0 = *reinterpret_cast<const f32x4*>(src) + *reinterpret_cast<const f32x4*>(src + 512), t1 = *reinterpret_cast<const f32x4*>(src + 4) + *reinterpret_cast<const f32x4*>(src + 516);
      xv[i] = u32x4{pk_bf(t0[0], t0[1]), pk_bf(t0[2], t0[3]), pk_bf(t1[0], t1[1]), pk_bf(t1[2], t1[3])};
    }
    gemv(red2 + w4 * 128);
    sbar4(cnt + 4, lane);
    STAMP(12);
    unsigned op;
    {
      f32x2 t = {0.f, 0.f};
#pragma unroll
      for (int w = 0; w < 4; ++w) t += *reinterpret_cast<const f32x2*>(red2 + w * 128 + (lane >> 5) * 64 + 8 * (lane & 7) + 2 * w4);
      op = pk_bf(t[0], t[1]);
    }
    float* red3 = red + 4096;
    float a[2][8];
#pragma unroll
    for (int bb = 0; bb < 2; ++bb)
#pragma unroll
      for (int k = 0; k < 8; ++k) a[bb][k] = 0.f;
#pragma unroll
    for (int ip = 0; ip < 8; ++ip) {
      const unsigned v0 = __builtin_amdgcn_readlane(op, ip), v1 = __builtin_amdgcn_readlane(op, 32 + ip);
#pragma unroll
      for (int k = 0; k < 4; ++k) {
        const unsigned lo = plo(Wb[2 * ip][k], Wb[2 * ip + 1][k]), hi = phi(Wb[2 * ip][k], Wb[2 * ip + 1][k]);
        a[0][2 * k] = dot2(lo, v0, a[0][2 * k]); a[0][2 * k + 1] = dot2(hi, v0, a[0][2 * k + 1]);
        a[1][2 * k] = dot2(lo, v1, a[1][2 * k]); a[1][2 * k + 1] = dot2(hi, v1, a[1][2 * k + 1]);
      }
    }
#pragma unroll
    for (int bb = 0; bb < 2; ++bb) {
      *reinterpret_cast<f32x4*>(red3 + (w4 * 2 + bb) * 512 + 8 * lane) = f32x4{a[bb][0], a[bb][1], a[bb][2], a[bb][3]};
      *reinterpret_cast<f32x4*>(red3 + (w4 * 2 + bb) * 512 + 8 * lane + 4) = f32x4{a[bb][4], a[bb][5], a[bb][6], a[bb][7]};
    }
    sbar4(cnt + 5, lane);
    {
      f32x4 t = {0.f, 0.f, 0.f, 0.f};
#pragma unroll
      for (int w = 0; w < 4; ++w) t += *reinterpret_cast<const f32x4*>(red3 + w * 1024 + 4 * tid);
      st16c(rs, WS_GA + ((tid >> 7) * 64 + idx) * 2048 + (tid & 127) * 16, u32x4{pk_bf(t[0], t[1]), tg.hi, pk_bf(t[2], t[3]), tg.lo});
    }
    STAMP(5);
    STAMP_FLUSH(0); TR_FLUSH();
    if (PADN) asm volatile("s_endpgm\n\t.fill %0,4,0xBF800000" :: "n"(PADN));
    return;
  }
}

extern "C" void kernel_launch(void* const* d_in, const int* in_sizes, int n_in, void* d_out, int out_size, void* d_ws, size_t ws_size, hipStream_t stream) {
  const int sel[5][3] = {{1, 3, 6}, {2, 4, 0}, {3, 2, 6}, {9, 9, 6}, {7, 8, 10}};
  Args a;
  for (int r = 0; r < 5; ++r) {
    for (int c = 0; c < 3; ++c) a.t[r][c] = (gptr)d_in[sel[r][c]];
    a.t[r][3] = (gptr)d_ws;
  }
  a.w_o = (gptr)d_in[5];
  a.out = (gptr)d_out;
  kmain<<<256, 512, 0, stream>>>(a);
}
```

```cpp
#include <hip/hip_runtime.h>
#include <hip/hip_bf16.h>
#include <stdint.h>

#define DI __device__ __forceinline__
typedef __attribute__((ext_vector_type(4))) unsigned u32x4;
typedef __attribute__((ext_vector_type(2))) unsigned u32x2;
typedef __attribute__((ext_vector_type(8))) unsigned u32x8;
typedef __attribute__((ext_vector_type(16))) __bf16 bf16x16_t;
typedef __attribute__((ext_vector_type(4))) float f32x4;
typedef __attribute__((ext_vector_type(2))) float f32x2;
typedef __attribute__((ext_vector_type(16))) float f32x16;
typedef __attribute__((ext_vector_type(8))) __bf16 bf16x8_t;
typedef __attribute__((ext_vector_type(2))) __bf16 bf16x2_t;
typedef __attribute__((ext_vector_type(4))) short s16x4;
typedef __attribute__((ext_vector_type(8))) short s16x8;
typedef unsigned short u16;
typedef unsigned long long u64;

#define SHARES 48, 48, 80, 80
constexpr int SH[4] = {SHARES};
constexpr int NR[4] = {8, 8, 10, 10};
static_assert(SH[0] + SH[1] + SH[2] + SH[3] == 256 && SH[0] <= 64 && SH[1] <= 64 && SH[2] <= 80 && SH[3] <= 80 && SH[0] >= 8 && SH[1] >= 8, "every key once");
#define TQX 470
#define TQA 870
#define TAML 1710
#define TFIN 0
#define CPF 16384
#define PADN 200
constexpr float C2 = 0.08838834764831845f * 1.4426950408889634f;
constexpr u64 GOLD = 0x9E3779B97F4A7C15ull;

constexpr unsigned WS_EP = 0;
constexpr unsigned WS_QX = 4096;
constexpr unsigned WS_QA = WS_QX + 2 * 2 * 32 * 64 * 8;
constexpr unsigned WS_AML = WS_QA + 2 * 32 * 256 * 8;
constexpr unsigned WS_APO = WS_AML + 64 * 32 * 16;
constexpr unsigned WS_GB = WS_APO + 32 * 64 * 512 * 2;
constexpr unsigned WS_GA = WS_GB + 2 * 128 * 128 * 16;
constexpr unsigned WS_GZ = WS_GA + 2 * 64 * 128 * 16;
constexpr unsigned WS_END = WS_GZ + 256 * 128 + 128;
#define STAMP(i) do {} while (0)
#define STAMP_DECL do {} while (0)
#define STAMP_FLUSH(grp) do {} while (0)

#define TR_DECL do {} while (0)
#define TR() do {} while (0)
#define TR_FLUSH() do {} while (0)
DI float bflo(unsigned u) { return __uint_as_float(u << 16); }
DI float bfhi(unsigned u) { return __uint_as_float(u & 0xffff0000u); }
DI unsigned pk_bf(float a, float b) { const f32x2 v = {a, b}; return __builtin_bit_cast(unsigned, __builtin_convertvector(v, bf16x2_t)); }
DI float dot2(unsigned k, unsigned q, float acc) {
  return __builtin_amdgcn_fdot2_f32_bf16(__builtin_bit_cast(bf16x2_t, k), __builtin_bit_cast(bf16x2_t, q), acc, false);
}
DI unsigned plo(unsigned w0, unsigned w1) { return __builtin_amdgcn_perm(w1, w0, 0x05040100u); }
DI unsigned phi(unsigned w0, unsigned w1) { return __builtin_amdgcn_perm(w1, w0, 0x07060302u); }
template <int CTRL> DI float dpp(float x) {
  return __builtin_bit_cast(float, __builtin_amdgcn_mov_dpp(__builtin_bit_cast(int, x), CTRL, 0xf, 0xf, true));
}
DI float row16_sum(float x) { x += dpp<0xB1>(x); x += dpp<0x4E>(x); x += dpp<0x141>(x); x += dpp<0x128>(x); return x; }
DI float row16_max(float x) { x = fmaxf(x, dpp<0xB1>(x)); x = fmaxf(x, dpp<0x4E>(x)); x = fmaxf(x, dpp<0x141>(x)); x = fmaxf(x, dpp<0x128>(x)); return x; }
DI float x16_sum(float x) { auto s = __builtin_amdgcn_permlane16_swap(__float_as_uint(x), __float_as_uint(x), false, false); return __uint_as_float(s[0]) + __uint_as_float(s[1]); }
DI float x16_max(float x) { auto s = __builtin_amdgcn_permlane16_swap(__float_as_uint(x), __float_as_uint(x), false, false); return fmaxf(__uint_as_float(s[0]), __uint_as_float(s[1])); }
DI float xhalf_max(float x) { auto t = __builtin_amdgcn_permlane32_swap(__float_as_uint(x), __float_as_uint(x), false, false); return fmaxf(__uint_as_float(t[0]), __uint_as_float(t[1])); }
DI float xhalf_sum(float x) { auto t = __builtin_amdgcn_permlane32_swap(__float_as_uint(x), __float_as_uint(x), false, false); return __uint_as_float(t[0]) + __uint_as_float(t[1]); }
DI u32x4 ld16(const void* p) { return *reinterpret_cast<const u32x4*>(p); }
DI u32x4 ld16nt(const void* p) { return __builtin_nontemporal_load(reinterpret_cast<const u32x4*>(p)); }
DI float ex2(float x) { return __builtin_amdgcn_exp2f(x); }
#define SCHED() __builtin_amdgcn_sched_barrier(0)

typedef __attribute__((address_space(1))) u64 gu64;
typedef __amdgpu_buffer_rsrc_t rsrc_t;
DI u32x4 ld16c(rsrc_t rs, unsigned off) { return __builtin_amdgcn_raw_buffer_load_b128(rs, (int)off, 0, 16); }
DI u32x2 ld8c(rsrc_t rs, unsigned off) { return __builtin_amdgcn_raw_buffer_load_b64(rs, (int)off, 0, 16); }
DI void st8c(rsrc_t rs, unsigned off, u32x2 v) { __builtin_amdgcn_raw_buffer_store_b64(v, rs, (int)off, 0, 16); }
DI void st16c(rsrc_t rs, unsigned off, u32x4 v) { __builtin_amdgcn_raw_buffer_store_b128(v, rs, (int)off, 0, 16); }
constexpr int SPIN_MAX = 1 << 20;
constexpr unsigned OOB = 0x7ffffff0u;
DI void rawbar() { __builtin_amdgcn_sched_barrier(0); __builtin_amdgcn_s_barrier(); __builtin_amdgcn_sched_barrier(0); }
struct Tag { unsigned hi, lo; };
#define COMPILER_FENCE() asm volatile("" ::: "memory")

template <int N> struct IC { static constexpr int v = N; };
template <int I, int N, class F> DI void sfor(F&& f) { if constexpr (I < N) { f(IC<I>{}); sfor<I + 1, N>(f); } }
typedef __attribute__((address_space(3))) unsigned lu32;
DI void lds_wait(unsigned* cnt, unsigned target) {
#pragma unroll 1
  for (int spin = 0; spin < SPIN_MAX; ++spin) {
    if (__builtin_amdgcn_readfirstlane(__hip_atomic_load((lu32*)cnt, __ATOMIC_RELAXED, __HIP_MEMORY_SCOPE_WORKGROUP)) >= target) break;
    __builtin_amdgcn_s_sleep(1);
  }
  COMPILER_FENCE();
}
DI void lds_hold(unsigned* flag) {
#pragma unroll 1
  for (int spin = 0; spin < SPIN_MAX; ++spin) {
    if (__builtin_amdgcn_readfirstlane(__hip_atomic_load((lu32*)flag, __ATOMIC_RELAXED, __HIP_MEMORY_SCOPE_WORKGROUP)) == 0u) break;
    __builtin_amdgcn_s_sleep(2);
  }
}
DI unsigned lds_arrive_nw(unsigned addr) {
  unsigned old, one = 1u;
  asm volatile("s_mov_b64 exec, 1\n\tds_add_rtn_u32 %0, %1, %2\n\ts_mov_b64 exec, -1" : "=v"(old) : "v"(addr), "v"(one) : "memory");
  return old;
}
DI unsigned lds_done(unsigned old) { asm volatile("s_waitcnt lgkmcnt(0)" : "+v"(old) :: "memory"); return __builtin_amdgcn_readfirstlane(old); }
DI unsigned lds_arrive(unsigned* cnt, int) { return lds_done(lds_arrive_nw((unsigned)(size_t)(lu32*)cnt)); }
DI void lds_add(unsigned* cnt, unsigned v, int) {
  asm volatile("s_mov_b64 exec, 1\n\tds_add_u32 %0, %1\n\ts_mov_b64 exec, -1" :: "v"((unsigned)(size_t)(lu32*)cnt), "v"(v) : "memory");
}
DI void sbar4(unsigned* cnt, int lane) { if (lds_arrive(cnt, lane) < 3u) lds_wait(cnt, 4u); }
DI float rs32(float a, float b) { auto t = __builtin_amdgcn_permlane32_swap(__float_as_uint(a), __float_as_uint(b), false, false); return __uint_as_float(t[0]) + __uint_as_float(t[1]); }
DI float rs16(float a, float b) { auto t = __builtin_amdgcn_permlane16_swap(__float_as_uint(a), __float_as_uint(b), false, false); return __uint_as_float(t[0]) + __uint_as_float(t[1]); }
template <int N> DI unsigned rowbcast(unsigned x) { return (unsigned)__builtin_amdgcn_mov_dpp((int)x, 0x150 + N, 0xf, 0xf, true); }
DI void pin2w(u32x8& a, u32x8& b) { asm volatile("" : "+v"(a), "+v"(b)); }
DI float rdlane(float x, int l) { return __builtin_bit_cast(float, __builtin_amdgcn_readlane(__builtin_bit_cast(int, x), l)); }

DI unsigned toff(unsigned row, unsigned ch) { return 256u * row + 16u * (ch ^ (((row & 3) << 2) | ((row >> 2) & 3))); }
constexpr int SMEM = 160 * 1024;
constexpr int L_SCR = 131072;
constexpr int L_MRG = 65536;
constexpr int L_CNT = L_SCR + 32 * 1008;
constexpr int L_ML = L_CNT + 64;

struct KV { u32x4 r[8]; };

typedef const __attribute__((address_space(1))) char* gptr;
struct Args { gptr t[5][4]; gptr w_o; gptr out; };

__global__ __launch_bounds__(512, 2) __attribute__((aligned(4096))) void kmain(Args a) {
  __shared__ __attribute__((aligned(16))) char smem[SMEM];
  const int tid = threadIdx.x, blk = blockIdx.x, lane = tid & 63;
  const int wave = __builtin_amdgcn_readfirstlane(tid >> 6), w4 = wave & 3;
  const int cls = ((blk >> 3) + (blk >> 5)) & 3;
  const int arow = wave < 4 ? cls : 4;
  gptr p0 = a.t[arow][0], p1 = a.t[arow][1], p2 = a.t[arow][2], p3 = a.t[arow][3], p4 = a.w_o, p5 = a.out;
  u64 pc0;
  asm volatile("s_getpc_b64 %0" : "=s"(pc0));
  SCHED();
  int idx = 8 * (blk & 7) + (blk >> 5);
  if (wave < 4) idx = (idx & ~7) | ((idx + (cls == 3 ? 2 : (cls == 2 ? 5 : 0))) & 7);
  const int h = idx >> 1, b = idx & 1;
  const int r4 = lane >> 4, c16 = lane & 15;
  unsigned* cnt = reinterpret_cast<unsigned*>(smem + L_CNT);
  STAMP_DECL;
  TR_DECL;
  STAMP(0);
  Tag tg;
  if (wave >= 4) {
    if (tid - 256 < 16) cnt[tid - 256] = 0u;
    const int nr = cls < 2 ? 8 : 10;
    unsigned tab;
    {
      int j = 2 * nr - 1 - lane;
      j = lane < nr ? lane : (j > 2 ? j : (j == 2 ? 0 : j + 1));
      int n = j + (h * nr >> 5);
      n = n >= nr ? n - nr : n;
      int off = 0, wd = 0;
#pragma unroll
      for (int d = 0; d < 4; ++d) {
        const int k = ((2 * n + 1) * NR[d] + (d < cls) - 1 + nr) * (nr == 8 ? 4096 : 3277) >> 16;
        off += k * (SH[d] / NR[d]) + min(k, SH[d] % NR[d]);
        wd = d == cls ? SH[d] / NR[d] + (n < SH[d] % NR[d]) : wd;
      }
      tab = lane < 2 * nr ? (unsigned)(4 * off + w4 * wd) << 10 | (unsigned)wd : 0u;
    }
    asm volatile("" : "+v"(tab));
    asm volatile("" : "+s"(p0), "+s"(p1), "+s"(p2), "+s"(p3));
    SCHED();
    char* ws = (char*)p3;
    const rsrc_t rs = __builtin_amdgcn_make_buffer_rsrc(ws, 0, (int)WS_END, 0x00020000);
    const char* kb = (const char*)p0 + ((size_t)(b * 32 + h) << 20);
    const char* vb = (const char*)p1 + ((size_t)(b * 32 + h) << 20);
    const unsigned lo = lane * 16u, lo2 = lo + 4096u;
    int nx = 0;
    auto nexts = [&]() {
      const unsigned e = __builtin_amdgcn_readlane(tab, nx);
      const rsrc_t r = __builtin_amdgcn_make_buffer_rsrc(const_cast<char*>((nx < nr ? kb : vb) + (e & ~1023u)), 0, (int)((e & 15u) << 10), 0x00020000);
      ++nx;
      return r;
    };
    const rsrc_t s0 = nexts();
    const int rot = cls;
    const char* wb = (const char*)p2 + h * 131072;
    unsigned wl = r4 * 8192 + w4 * 256 + c16 * 16;
    asm volatile("s_waitcnt lgkmcnt(0)" : "+v"(wl), "+v"(tab) :: "memory");
    rawbar();
    KV A, B, X;
    u32x4 W[8][4];
    u32x8 T[2][8];
    auto ldp = [&](KV& S, auto P, rsrc_t q) {
      constexpr int p = P.v;
      S.r[2 * p] = __builtin_amdgcn_raw_buffer_load_b128(q, (int)((p < 2 ? lo : lo2) + (p & 1) * 2048), 0, 2);
      S.r[2 * p + 1] = __builtin_amdgcn_raw_buffer_load_b128(q, (int)((p < 2 ? lo : lo2) + (p & 1) * 2048 + 1024), 0, 2);
    };
    auto landed = [&](KV& S, auto P) { asm volatile("" : "+v"(S.r[2 * P.v]), "+v"(S.r[2 * P.v + 1])); };
    auto ldw = [&](auto G, auto J) { W[G.v][J.v] = ld16(wb + ((((G.v >> 1) + rot) & 3) * 32768 + (G.v & 1) * 4096) + wl + J.v * 1024); };
    auto conv = [&](auto G) {
      constexpr int t = G.v >> 2, j0 = (G.v & 3) * 2;
#pragma unroll
      for (int jj = 0; jj < 2; ++jj)
#pragma unroll
        for (int e = 0; e < 4; ++e) {
          const unsigned a_ = W[G.v][2 * jj][e], b_ = W[G.v][2 * jj + 1][e];
          T[t][2 * e][j0 + jj] = plo(a_, b_); T[t][2 * e + 1][j0 + jj] = phi(a_, b_);
        }
#pragma unroll
      for (int e = 0; e < 8; e += 2) pin2w(T[t][e], T[t][e + 1]);
    };
    const unsigned qo = WS_QX + (((2 + b) * 32 + h) * 64 + 4 * c16) * 8;
    u32x4 g0, g1;
    unsigned cpf;
    u32x2 tw;
    {
      const rsrc_t s1 = nexts(), s2 = nexts();
      sfor<0, 12>([&](auto I) {
        constexpr int i = I.v;
        if constexpr (i >= 4) { if constexpr (i < 8) landed(X, IC<i & 3>{}); else landed(B, IC<i & 3>{}); }
        if constexpr (i == 7 || i == 11) TR();
        if constexpr (i == 8) tw = ld8c(rs, lane == 0 ? WS_END - 128 : OOB);
        if constexpr (i == 0) cpf = __builtin_amdgcn_raw_buffer_load_b32(__builtin_amdgcn_make_buffer_rsrc((char*)(gptr)((pc0 & ~4095ull) + 4096), 0, CPF, 0x00020000), lane < 4 ? (((blk >> 3) * 4 + w4) * 4 + lane) * 64 : OOB, 0, 0);
        if constexpr (i == 9) { g0 = ld16c(rs, qo); g1 = ld16c(rs, qo + 16); }
        if constexpr (i < 4) { ldp(X, IC<i & 3>{}, s0); ldw(IC<0>{}, IC<i & 3>{}); }
        else if constexpr (i < 8) { ldp(B, IC<i & 3>{}, s1); ldw(IC<1>{}, IC<i & 3>{}); }
        else { ldp(A, IC<i & 3>{}, s2); ldw(IC<2>{}, IC<i & 3>{}); }
        SCHED();
        if constexpr (i == 8) { conv(IC<0>{}); SCHED(); }
      });
    }
    lds_wait(cnt + 15, 1u);
    tg.hi = __builtin_amdgcn_readfirstlane(cnt[13]); tg.lo = __builtin_amdgcn_readfirstlane(cnt[14]);
    asm volatile("" :: "v"(tw), "v"(cpf));
    u32x4 qpk;
    {
      SCHED();
      conv(IC<1>{});
      SCHED();
      if (!__all((g0[1] == tg.hi) & (g0[3] == tg.hi) & (g1[1] == tg.hi) & (g1[3] == tg.hi))) {
#pragma unroll 1
        for (int spin = 0; spin < SPIN_MAX; ++spin) {
          COMPILER_FENCE();
          g0 = ld16c(rs, qo); g1 = ld16c(rs, qo + 16);
          if (__all((g0[1] == tg.hi) & (g0[3] == tg.hi) & (g1[1] == tg.hi) & (g1[3] == tg.hi))) break;
        }
      }
      qpk = u32x4{g0[0], g0[2], g1[0], g1[2]};
    }
    STAMP(7);
    const bool holds = cls == 0;
    float sc[10], acc[8];
#pragma unroll
    for (int k = 0; k < 10; ++k) sc[k] = 0.f;
    auto point = [&](KV& S, KV& N, rsrc_t sn, auto P, auto REFILL, auto AH, auto WG) {
      constexpr int ah = AH.v;
      if constexpr (REFILL.v) {
        if constexpr (P.v == 0 && ah > 0) { if (holds) lds_hold(cnt + 8); sfor<0, ah>([&](auto Q) { ldp(N, Q, sn); }); SCHED(); }
        landed(S, P);
        if constexpr (P.v + ah < 4) { if constexpr ((P.v & 1) == 0) { if (holds) lds_hold(cnt + 8); } ldp(N, IC<(P.v + ah) & 3>{}, sn); }
        if constexpr (WG.v >= 0) ldw(WG, P);
        SCHED();
      }
    };
    auto step = [&](KV& S, KV& N, auto REFILL, auto WG, auto ISV) {
      rsrc_t sn = s0;
      if constexpr (REFILL.v) sn = nexts();
      sfor<0, 4>([&](auto P) {
        point(S, N, sn, P, REFILL, IC<(ISV.v || WG.v < 0 ? 1 : 0)>{}, WG);
        if constexpr (!ISV.v) {
#pragma unroll
          for (int t = 2 * P.v; t < 2 * P.v + 2; ++t) {
            acc[t] = 0.f;
#pragma unroll
            for (int k = 0; k < 4; ++k) acc[t] = dot2(S.r[t][k], qpk[k], acc[t]);
          }
          asm volatile("" : "+v"(acc[2 * P.v]), "+v"(acc[2 * P.v + 1]));
        } else {
          const unsigned pp = rowbcast<4 * P.v>(__float_as_uint(sc[0]));
#pragma unroll
          for (int k = 0; k < 4; ++k) { acc[2 * k] = dot2(plo(S.r[2 * P.v][k], S.r[2 * P.v + 1][k]), pp, acc[2 * k]); acc[2 * k + 1] = dot2(phi(S.r[2 * P.v][k], S.r[2 * P.v + 1][k]), pp, acc[2 * k + 1]); }
          asm volatile("" : "+v"(acc[0]), "+v"(acc[1]), "+v"(acc[2]), "+v"(acc[3]), "+v"(acc[4]), "+v"(acc[5]), "+v"(acc[6]), "+v"(acc[7]));
        }
        SCHED();
      });
      if constexpr (!ISV.v) {
        float u[4], v[2];
#pragma unroll
        for (int i = 0; i < 4; ++i) u[i] = (c16 & 8 ? acc[i + 4] : acc[i]) + dpp<0x128>(c16 & 8 ? acc[i] : acc[i + 4]);
#pragma unroll
        for (int i = 0; i < 2; ++i) v[i] = (c16 & 4 ? u[i + 2] : u[i]) + dpp<0x141>(c16 & 4 ? u[i] : u[i + 2]);
        float x = (c16 & 2 ? v[1] : v[0]) + dpp<0x4E>(c16 & 2 ? v[0] : v[1]);
        x += dpp<0xB1>(x);
#pragma unroll
        for (int k = 9; k > 0; --k) sc[k] = sc[k - 1];
        sc[0] = x;
      } else {
#pragma unroll
        for (int k = 0; k < 9; ++k) sc[k] = sc[k + 1];
      }
      SCHED();
      if constexpr (REFILL.v || ISV.v) TR();
    };
    step(B, B, IC<0>{}, IC<-1>{}, IC<0>{});
    step(A, B, IC<1>{}, IC<3>{}, IC<0>{});
    step(X, X, IC<0>{}, IC<-1>{}, IC<0>{});
    conv(IC<2>{});
    sfor<3, 6>([&](auto I) {
      if constexpr (I.v & 1) step(B, A, IC<1>{}, IC<I.v + 1>{}, IC<0>{}); else step(A, B, IC<1>{}, IC<I.v + 1>{}, IC<0>{});
      conv(I);
    });
#pragma unroll 1
    for (int i = 6; i < nr; i += 2) {
      step(A, B, IC<1>{}, IC<-1>{}, IC<0>{});
      step(B, A, IC<1>{}, IC<-1>{}, IC<0>{});
    }
    float m = -INFINITY, l = 0.f;
#pragma unroll
    for (int k = 0; k < 10; ++k) { sc[k] = (unsigned)(c16 >> 1) < (__builtin_amdgcn_readlane(tab, nr + k) & 15u) ? sc[k] : -INFINITY; m = fmaxf(m, sc[k]); }
    m = row16_max(m);
    {
      const float mc = m * C2;
#pragma unroll
      for (int k = 0; k < 10; ++k) {
        const float p = ex2(fmaf(sc[k], C2, -mc));
        const unsigned pk = pk_bf(dpp<0x00>(p), dpp<0xAA>(p));
        l = dot2(pk, 0x3f803f80u, l);
        sc[k] = __uint_as_float(pk);
      }
      l += dpp<0x124>(l); l += dpp<0x128>(l);
      *reinterpret_cast<float*>(smem + L_ML + ((c16 & 1) * 16 + w4 * 4 + r4) * 4) = (c16 & 1) ? l : m;
      COMPILER_FENCE();
      const unsigned got = lds_arrive_nw(L_CNT + 40);
      const float mr = *reinterpret_cast<const float*>(smem + L_ML + c16 * 4), lr = *reinterpret_cast<const float*>(smem + L_ML + 64 + c16 * 4);
      SCHED();
      const bool last = lds_done(got) == 3u;
      const float M = row16_max(mr);
      st8c(rs, last && lane == 0 ? WS_GZ + (b * 128 + h * 4 + cls) * 128 : OOB, u32x2{__float_as_uint(fmaf(M, C2, __builtin_amdgcn_logf(row16_sum(ex2((mr - M) * C2) * lr)))), tg.hi});
    }
#pragma unroll
    for (int k = 0; k < 8; ++k) acc[k] = 0.f;
    SCHED();
    STAMP(8);
    unsigned yoff = (r4 & 1) ? OOB : WS_GB + (b * 128 + h * 4 + cls) * 2048 + 512 * w4 + 32 * c16 + 16 * (lane >> 5), zoff = WS_GZ + (b * 128 + h * 4 + (lane & 3)) * 128;
    unsigned rwo = L_MRG + (w4 * 4 + r4) * 144 + ((c16 - 4 * rot) & 15) * 8, rro = L_MRG + c16 * 144 + r4 * 16;
    asm volatile("" : "+v"(yoff), "+v"(zoff), "+v"(rwo), "+v"(rro));
    u32x2 zq;
    step(A, B, IC<1>{}, IC<7>{}, IC<1>{});
    conv(IC<6>{});
#pragma unroll 1
    for (int i = 2;;) {
      step(B, A, IC<1>{}, IC<-1>{}, IC<1>{});
      i += 2;
      zq = ld8c(rs, i >= nr ? zoff : OOB);
      SCHED();
      step(A, B, IC<1>{}, IC<-1>{}, IC<1>{});
      if (i >= nr) break;
    }
    conv(IC<7>{});
    if (!__all(zq[1] == tg.hi)) {
#pragma unroll 1
      for (int spin = 0; spin < SPIN_MAX; ++spin) {
        COMPILER_FENCE();
        zq = ld8c(rs, zoff);
        if (__all(zq[1] == tg.hi)) break;
        __builtin_amdgcn_s_sleep(8);
      }
    }
    float wt;
    int six = (lane & 1) ? 0xEEEE : 0x4444;
    {
      const float z = __uint_as_float(zq[0]);
      float Z = fmaxf(z, dpp<0xB1>(z)); Z = fmaxf(Z, dpp<0x4E>(Z));
      float L = ex2(z - Z); L += dpp<0xB1>(L); L += dpp<0x4E>(L);
      Z += __builtin_amdgcn_logf(L);
      wt = ex2(fmaf(m, C2, -Z));
      asm volatile("" : "+v"(wt), "+v"(six));
    }
    f32x4 d[8];
#pragma unroll
    for (int e = 0; e < 8; ++e) { d[e] = f32x4{0.f, 0.f, 0.f, 0.f}; asm volatile("" : "+v"(d[e])); }
    if (cls == 0) lds_wait(cnt + 1, 4u);
    SCHED();
    step(B, B, IC<0>{}, IC<-1>{}, IC<1>{});
    STAMP(1);
#pragma unroll
    for (int k = 0; k < 8; ++k) acc[k] *= wt;
    *reinterpret_cast<u32x2*>(smem + rwo) = u32x2{pk_bf(rs16(acc[0], acc[2]), rs16(acc[1], acc[3])), pk_bf(rs16(acc[4], acc[6]), rs16(acc[5], acc[7]))};
    {
      u32x4 af[2];
      unsigned got = lds_arrive_nw(L_CNT), need = 3u;
#pragma unroll 1
      for (int spin = 0; spin < SPIN_MAX; ++spin) {
#pragma unroll
        for (int t = 0; t < 2; ++t) af[t] = *reinterpret_cast<const u32x4*>(smem + rro + t * 64);
        SCHED();
        if (lds_done(got) >= need) break;
        need = 4u;
        __builtin_amdgcn_s_sleep(1);
        asm volatile("ds_read_b32 %0, %1" : "=v"(got) : "v"((unsigned)L_CNT) : "memory");
      }
      SCHED();
      STAMP(3);
      float v4[4];
#pragma unroll
      for (int k = 0; k < 4; ++k)
#pragma unroll
        for (int t = 0; t < 2; ++t) {
          d[k] = __builtin_amdgcn_smfmac_f32_16x16x64_bf16(__builtin_bit_cast(bf16x8_t, af[t]), __builtin_bit_cast(bf16x16_t, T[t][k]), d[k], six, 0, 0);
          d[k + 4] = __builtin_amdgcn_smfmac_f32_16x16x64_bf16(__builtin_bit_cast(bf16x8_t, af[t]), __builtin_bit_cast(bf16x16_t, T[t][k + 4]), d[k + 4], six, 0, 0);
        }
#pragma unroll
      for (int k = 0; k < 4; ++k) {
        const f32x2 x = d[k].xy + d[k].zw, y = d[k + 4].xy + d[k + 4].zw;
        v4[k] = x16_sum(rs32(x[0] + x[1], y[0] + y[1]));
      }
      STAMP(4);
      st16c(rs, yoff, u32x4{pk_bf(v4[0], v4[1]), tg.hi, pk_bf(v4[2], v4[3]), tg.lo});
      SCHED();
      lds_add(cnt + 11, 1u, lane);
    }
    STAMP(2);
    STAMP_FLUSH(1); TR_FLUSH();
    return;
  }
  u32x4 Wa[16], Wb[16], xv[4];
  unsigned pf;
  u64 epw, tstart;
  {
    unsigned oa[16];
    oa[0] = (cls & 1) ? (w4 * 128 + 16 * (lane >> 3)) * 8192 + idx * 128 + 16 * (lane & 7) : (cls == 2 ? (b * 256 + w4 * 64 + 16 * r4) * 8192 + h * 256 + 16 * c16 : (idx * 128 + w4) * 1024 + 16 * lane);
#pragma unroll
    for (int i = 1; i < 16; ++i) oa[i] = oa[i - 1] + (cls ? 8192 : 4096);
    unsigned op = (((blk >> 5) * 4 + w4) * 16 + c16) * 8192 + ((idx & 56) + 3 + (cls == 0 ? -1 : (idx >= 56 ? 4 : 0)) * (lane >> 5)) * 128 + (r4 & 1) * 64;
    unsigned ox = w4 * 256 + 32 * (lane >> 3);
#pragma unroll
    for (int i = 0; i < 16; i += 8) asm volatile("" : "+v"(oa[i]), "+v"(oa[i + 1]), "+v"(oa[i + 2]), "+v"(oa[i + 3]), "+v"(oa[i + 4]), "+v"(oa[i + 5]), "+v"(oa[i + 6]), "+v"(oa[i + 7]));
    asm volatile("" : "+v"(op), "+v"(ox));
    asm volatile("" : "+s"(p0), "+s"(p1), "+s"(p2), "+s"(p3));
    SCHED();
    tstart = wall_clock64();
    SCHED();
    pf = *reinterpret_cast<const unsigned*>((const char*)p1 + op);
    epw = __hip_atomic_load((gu64*)p3, __ATOMIC_RELAXED, __HIP_MEMORY_SCOPE_AGENT);
#pragma unroll
    for (int i = 0; i < 4; ++i) xv[i] = ld16((const char*)p2 + ox + (i >> 1) * 1024 + 16 * (i & 1));
    SCHED();
#pragma unroll
    for (int i = 0; i < 16; ++i) { Wa[i] = ld16nt((const char*)p0 + oa[i]); SCHED(); }
  }
  char* ws = (char*)p3;
  const rsrc_t rs = __builtin_amdgcn_make_buffer_rsrc(ws, 0, (int)WS_END, 0x00020000);
  u16* out = (u16*)p5;
  {
    rawbar();
    const u64 t64 = (((u64)(unsigned)__builtin_amdgcn_readfirstlane((unsigned)(epw >> 32)) << 32) | (unsigned)__builtin_amdgcn_readfirstlane((unsigned)epw)) + GOLD;
    tg.hi = (unsigned)(t64 >> 32); tg.lo = (unsigned)t64;
    asm volatile("" : "+s"(tg.hi), "+s"(tg.lo) : "v"(pf) : "memory");
    if (w4 == 0) { cnt[13] = tg.hi; cnt[14] = tg.lo; COMPILER_FENCE(); lds_add(cnt + 15, 1u, lane); }
    STAMP(13);
  }
  float* red = reinterpret_cast<float*>(smem);
  auto gemv = [&](float* dst) {
    float acc[2][8];
#pragma unroll
    for (int bb = 0; bb < 2; ++bb)
#pragma unroll
      for (int k = 0; k < 8; ++k) acc[bb][k] = 0.f;
#pragma unroll
    for (int ip = 0; ip < 8; ++ip) {
      const unsigned xp0 = xv[ip >> 2][ip & 3], xp1 = xv[2 + (ip >> 2)][ip & 3];
#pragma unroll
      for (int k = 0; k < 4; ++k) {
        const unsigned lo = plo(Wa[2 * ip][k], Wa[2 * ip + 1][k]), hi = phi(Wa[2 * ip][k], Wa[2 * ip + 1][k]);
        acc[0][2 * k] = dot2(lo, xp0, acc[0][2 * k]); acc[0][2 * k + 1] = dot2(hi, xp0, acc[0][2 * k + 1]);
        acc[1][2 * k] = dot2(lo, xp1, acc[1][2 * k]); acc[1][2 * k + 1] = dot2(hi, xp1, acc[1][2 * k + 1]);
      }
    }
    float v8[8], v4[4];
#pragma unroll
    for (int k = 0; k < 8; ++k) v8[k] = rs32(acc[0][k], acc[1][k]);
#pragma unroll
    for (int k = 0; k < 4; ++k) { const float t = rs16(v8[k], v8[k + 4]); v4[k] = t + dpp<0x128>(t); }
    *reinterpret_cast<f32x4*>(dst + (lane >> 5) * 64 + 8 * (lane & 7) + 4 * (r4 & 1)) = f32x4{v4[0], v4[1], v4[2], v4[3]};
    COMPILER_FENCE();
  };
  if (cls & 1) {
    gemv(red + w4 * 128);
    const unsigned got = lds_arrive_nw(L_CNT + 8);
    f32x4 t4[4];
#pragma unroll
    for (int w = 0; w < 4; ++w) t4[w] = *reinterpret_cast<const f32x4*>(red + w * 128 + 4 * (lane & 31));
    SCHED();
    if (lds_done(got) == 3u) {
      const f32x4 t = (t4[0] + t4[1]) + (t4[2] + t4[3]);
      st16c(rs, lane < 32 ? WS_QX + ((((cls >> 1) * 2 + r4) * 32 + h) * 64 + b * 32 + 2 * c16) * 8 : OOB, u32x4{pk_bf(t[0], t[1]), tg.hi, pk_bf(t[2], t[3]), tg.hi});
      SCHED();
      lds_add(cnt + 2, 1u, lane);
    } else lds_wait(cnt + 2, 5u);
    STAMP(8);
  } else if (cls == 2) {
    while (wall_clock64() - tstart < TQX) __builtin_amdgcn_s_sleep(4);
    u32x4 q0, q1;
    const unsigned qo = WS_QX + (h * 64 + 4 * c16) * 8;
#pragma unroll 1
    for (int spin = 0; spin < SPIN_MAX; ++spin) {
      COMPILER_FENCE();
      const u32x4 g0 = ld16c(rs, qo), g1 = ld16c(rs, qo + 16), g2 = ld16c(rs, qo + 16384), g3 = ld16c(rs, qo + 16400);
      q0 = u32x4{g0[0], g0[2], g1[0], g1[2]}; q1 = u32x4{g2[0], g2[2], g3[0], g3[2]};
      if (__all((g0[1] == tg.hi) & (g0[3] == tg.hi) & (g1[1] == tg.hi) & (g1[3] == tg.hi) & (g2[1] == tg.hi) & (g2[3] == tg.hi) & (g3[1] == tg.hi) & (g3[3] == tg.hi))) break;
      __builtin_amdgcn_s_sleep(1);
    }
    STAMP(9);
    float v[4];
    {
      float u[16], t8[8];
#pragma unroll
      for (int i = 0; i < 16; ++i) {
        float a = 0.f, c = 0.f;
#pragma unroll
        for (int k = 0; k < 4; ++k) { a = dot2(Wa[i][k], q0[k], a); c = dot2(Wa[i][k], q1[k], c); }
        u[i] = (c16 & 4 ? c : a) + dpp<0x141>(c16 & 4 ? a : c);
        asm volatile("" : "+v"(u[i]));
      }
#pragma unroll
      for (int i = 0; i < 8; ++i) { t8[i] = (c16 & 2 ? u[i + 8] : u[i]) + dpp<0x4E>(c16 & 2 ? u[i] : u[i + 8]); asm volatile("" : "+v"(t8[i])); }
#pragma unroll
      for (int i = 0; i < 4; ++i) {
        float t = (c16 & 1 ? t8[i + 4] : t8[i]) + dpp<0xB1>(c16 & 1 ? t8[i] : t8[i + 4]);
        asm volatile("" : "+v"(t));
        v[i] = t + dpp<0x128>(t);
      }
    }
    st16c(rs, c16 < 8 ? WS_QA + ((c16 >> 2) * 32 + h) * 2048 + (b * 256 + w4 * 64 + 16 * r4 + 4 * (c16 & 3)) * 4 : OOB, u32x4{pk_bf(v[0], v[1]), tg.hi, pk_bf(v[2], v[3]), tg.hi});
    STAMP(10);
  } else {
    {
      const u16* pb = (const u16*)p0 + (idx * 128 + 64 + w4) * 512 + 8 * lane;
#pragma unroll
      for (int i = 0; i < 16; ++i) {
        Wb[i] = ld16nt(pb + i * 2048);
        if (i >= 4 && (i & 1)) asm volatile("" : "+v"(Wb[i - 4]));
        if (i == 9) TR();
        SCHED();
      }
    }
    lds_add(cnt + 8, 1u, lane);
#pragma unroll
    for (int i = 0; i < 16; ++i) *reinterpret_cast<u32x4*>(smem + ((i >> 3) * 4 + r4) * 8192 + toff(4 * (i & 7) + w4, c16)) = Wa[i];
#pragma unroll
    for (int i = 0; i < 16; ++i) *reinterpret_cast<u32x4*>(smem + ((2 + (i >> 3)) * 4 + r4) * 8192 + toff(4 * (i & 7) + w4, c16)) = Wb[i];
    STAMP(8);
    TR();
  }
  if (cls >= 2) {
    const unsigned tw = __builtin_nontemporal_load(reinterpret_cast<const unsigned*>(out + (idx >> 5) * 1024 + (cls & 1) * 512 + 16 * (idx & 31)));
    asm volatile("" :: "v"(tw));
  }
  asm volatile("" :: "v"(xv[0]), "v"(xv[1]), "v"(xv[2]), "v"(xv[3]));
  SCHED();
  if (cls >= 2) {
    unsigned d0 = (cls == 3 ? WS_GB + (idx >> 5) * 128 * 2048 : WS_GA + (idx >> 5) * 64 * 2048) + (16 * w4 + (lane >> 2)) * 2048 + (idx & 31) * 64 + (lane & 3) * 16;
    unsigned d1 = cls == 3 ? d0 + 64 * 2048 : d0;
    unsigned fw = (lane & 12) ? 32768 + 256 + lane * 4 : 32768 + (w4 * 16 + 4 * (lane & 3) + r4) * 4, fr = 32768 + 8 * (lane & 7);
    unsigned oo = lane < 8 ? ((idx >> 5) * 1024 + (cls & 1) * 512 + 16 * (idx & 31) + 2 * lane) * 2 : OOB;
    float f1 = cls == 3 ? 1.f : 0.f;
    const rsrc_t ro = __builtin_amdgcn_make_buffer_rsrc(out, 0, 4096, 0x00020000);
    asm volatile("" : "+v"(d0), "+v"(d1), "+v"(fw), "+v"(fr), "+v"(oo), "+v"(f1));
    lds_wait(cnt + 11, 4u);
    while (wall_clock64() - tstart < TFIN) __builtin_amdgcn_s_sleep(2);
    STAMP(3);
    u32x4 x, y;
    bool ok = false;
#pragma unroll 1
    for (int spin = 0; spin < SPIN_MAX; ++spin) {
      COMPILER_FENCE();
      if (!ok) { x = ld16c(rs, d0); y = ld16c(rs, d1); }
      ok = (((x[1] ^ tg.hi) | (y[1] ^ tg.hi)) | ((x[3] ^ tg.lo) | (y[3] ^ tg.lo))) == 0u;
      if (__all(ok)) break;
    }
    STAMP(14);
    float s = rs16(rs32(fmaf(f1, bflo(y[0]), bflo(x[0])), fmaf(f1, bflo(y[2]), bflo(x[2]))), rs32(fmaf(f1, bfhi(y[0]), bfhi(x[0])), fmaf(f1, bfhi(y[2]), bfhi(x[2]))));
    s += dpp<0x124>(s); s += dpp<0x128>(s);
    *reinterpret_cast<float*>(smem + fw) = s;
    COMPILER_FENCE();
    const unsigned got = lds_arrive_nw(L_CNT + 24);
    f32x2 t[4];
#pragma unroll
    for (int w = 0; w < 4; ++w) t[w] = *reinterpret_cast<const f32x2*>(smem + fr + w * 64);
    SCHED();
    if (lds_done(got) == 3u) {
      const f32x2 r = (t[0] + t[1]) + (t[2] + t[3]);
      __builtin_amdgcn_raw_buffer_store_b32(pk_bf(r[0], r[1]), ro, (int)oo, 0, 0);
    }
    STAMP(6);
    STAMP_FLUSH(0); TR_FLUSH();
    return;
  }
  if (cls == 0) {
    char* scr = smem + L_SCR;
    const int ab = idx >> 5, r = lane & 31, hh = lane >> 5;
    u32x4 qlast = {0u, 0u, 0u, 0u};
    {
      u32x4 g[18];
      const int fl = (idx & 1) * 8;
      const unsigned o8 = WS_QA + (ab * 32 + w4 * 8) * 2048 + lane * 16, ol = WS_QA + (ab * 32 + r) * 2048 + 1984 + 32 * hh;
      while (wall_clock64() - tstart < TQA) __builtin_amdgcn_s_sleep(2);
      unsigned pend = 0x3ffffu;
#pragma unroll 1
      for (int spin = 0; spin < SPIN_MAX && pend; ++spin) {
        COMPILER_FENCE();
#pragma unroll
        for (int i = 0; i < 18; ++i) g[i] = ld16c(rs, (pend >> i & 1) ? (i < 16 ? o8 + (i ^ fl) * 1024 : ol + 16 * (i - 16)) : OOB);
        if (spin == 0) { SCHED(); lds_add(cnt + 8, ~0u, lane); SCHED(); }
#pragma unroll
        for (int i = 0; i < 18; ++i) {
          if ((pend >> i & 1) && __all((g[i][1] == tg.hi) & (g[i][3] == tg.hi))) {
            pend &= ~(1u << i);
            if (i >= 16) { qlast[2 * (i - 16)] = g[i][0]; qlast[2 * (i - 16) + 1] = g[i][2]; }
            else if (!(i & 1) || lane < 60) *reinterpret_cast<u32x2*>(scr + (w4 * 8 + ((i ^ fl) >> 1)) * 1008 + (i & 1) * 512 + 8 * lane) = u32x2{g[i][0], g[i][2]};
          }
        }
        if (pend) __builtin_amdgcn_s_sleep(8);
      }
    }
    sbar4(cnt + 3, lane);
    STAMP(10);
    f32x16 sacc;
#pragma unroll
    for (int i = 0; i < 16; ++i) sacc[i] = 0.f;
#pragma unroll
    for (int jj = 0; jj < 32; ++jj) {
      const u32x4 af = *reinterpret_cast<const u32x4*>(smem + (w4 * 4 + (jj >> 3)) * 8192 + toff(r, 2 * (jj & 7) + hh));
      const u32x4 bf = jj < 31 ? *reinterpret_cast<const u32x4*>(scr + r * 1008 + 16 * (2 * (jj < 31 ? jj : 0) + hh)) : qlast;
      sacc = __builtin_amdgcn_mfma_f32_32x32x16_bf16(__builtin_bit_cast(bf16x8_t, af), __builtin_bit_cast(bf16x8_t, bf), sacc, 0, 0, 0);
    }
    float m = sacc[0];
#pragma unroll
    for (int i = 1; i < 16; ++i) m = fmaxf(m, sacc[i]);
    m = xhalf_max(m);
    if (hh == 0) *reinterpret_cast<float*>(scr + r * 1008 + 992 + 4 * w4) = m;
    sbar4(cnt + 4, lane);
    STAMP(11);
    {
      const f32x4 m4 = *reinterpret_cast<const f32x4*>(scr + r * 1008 + 992);
      m = fmaxf(fmaxf(m4[0], m4[1]), fmaxf(m4[2], m4[3]));
    }
    const float mc = m * C2;
    float p[16], ls = 0.f;
#pragma unroll
    for (int i = 0; i < 16; ++i) { p[i] = ex2(fmaf(sacc[i], C2, -mc)); ls += p[i]; }
    ls = xhalf_sum(ls);
    sbar4(cnt + 5, lane);
    u32x4* pf = reinterpret_cast<u32x4*>(scr);
    float* wl = reinterpret_cast<float*>(scr + 8192);
    if (hh == 0) wl[r * 4 + w4] = ls;
    pf[(2 * w4) * 64 + lane] = u32x4{pk_bf(p[0], p[1]), pk_bf(p[2], p[3]), pk_bf(p[4], p[5]), pk_bf(p[6], p[7])};
    pf[(2 * w4 + 1) * 64 + lane] = u32x4{pk_bf(p[8], p[9]), pk_bf(p[10], p[11]), pk_bf(p[12], p[13]), pk_bf(p[14], p[15])};
    sbar4(cnt + 6, lane);
    u32x4 pb8[8];
#pragma unroll
    for (int ks = 0; ks < 8; ++ks) pb8[ks] = pf[ks * 64 + lane];
    const f32x4 l4 = *reinterpret_cast<const f32x4*>(wl + r * 4);
    const int qq = (lane & 15) >> 2, pp = lane & 3, blk2 = (lane >> 4) & 1;
    char* ot = scr + 12288 + w4 * 4096;
#pragma unroll
    for (int c2 = 0; c2 < 2; ++c2) {
      f32x16 oacc[2];
#pragma unroll
      for (int ct = 0; ct < 2; ++ct)
#pragma unroll
        for (int i = 0; i < 16; ++i) oacc[ct][i] = 0.f;
#pragma unroll
      for (int ks = 0; ks < 8; ++ks)
#pragma unroll
        for (int ct = 0; ct < 2; ++ct) {
          typedef __attribute__((address_space(3))) s16x4* lp;
          const char* tile = smem + ((ks >> 1) * 4 + w4) * 8192;
          const int cc = 2 * c2 + ct;
          const s16x4 lo = __builtin_amdgcn_ds_read_tr16_b64_v4i16((lp)(tile + toff(16 * (ks & 1) + 4 * hh + qq, 4 * cc + 2 * blk2 + (pp >> 1)) + 8 * (pp & 1)));
          const s16x4 hi = __builtin_amdgcn_ds_read_tr16_b64_v4i16((lp)(tile + toff(16 * (ks & 1) + 8 + 4 * hh + qq, 4 * cc + 2 * blk2 + (pp >> 1)) + 8 * (pp & 1)));
          const s16x8 afr = __builtin_shufflevector(lo, hi, 0, 1, 2, 3, 4, 5, 6, 7);
          oacc[ct] = __builtin_amdgcn_mfma_f32_32x32x16_bf16(__builtin_bit_cast(bf16x8_t, afr), __builtin_bit_cast(bf16x8_t, pb8[ks]), oacc[ct], 0, 0, 0);
        }
#pragma unroll
      for (int ct = 0; ct < 2; ++ct)
#pragma unroll
        for (int g = 0; g < 4; ++g)
          *reinterpret_cast<u32x2*>(ot + r * 128 + 16 * ((4 * ct + g) ^ (r & 7)) + 8 * hh) = u32x2{pk_bf(oacc[ct][4 * g], oacc[ct][4 * g + 1]), pk_bf(oacc[ct][4 * g + 2], oacc[ct][4 * g + 3])};
      if (c2 == 0) lds_add(cnt + 8, 1u, lane);
      __builtin_amdgcn_wave_barrier();
#pragma unroll
      for (int i = 0; i < 4; ++i) {
        const int hd = 8 * i + (lane >> 3), pc = lane & 7;
        const u32x4 vv = *reinterpret_cast<const u32x4*>(ot + hd * 128 + 16 * (pc ^ (hd & 7)));
        st16c(rs, WS_APO + ((hd * 64 + idx) * 512 + w4 * 128 + 64 * c2 + 8 * pc) * 2, vv);
      }
      __builtin_amdgcn_wave_barrier();
    }
    lds_arrive(cnt + 1, lane);
    STAMP(1);
    asm volatile("s_waitcnt vmcnt(0)" ::: "memory");
    if (lds_arrive(cnt + 9, lane) == 3u && hh == 0)
      st16c(rs, WS_AML + (idx * 32 + r) * 16, u32x4{__float_as_uint(m), tg.hi, __float_as_uint((l4[0] + l4[1]) + (l4[2] + l4[3])), tg.hi});
    lds_add(cnt + 8, ~0u, lane);
    STAMP(2);
    if (idx == 0 && w4 == 0) {
      while (wall_clock64() - tstart < TAML + 200) __builtin_amdgcn_s_sleep(8);
#pragma unroll 1
      for (int spin = 0; spin < SPIN_MAX; ++spin) {
        COMPILER_FENCE();
        const u32x2 t0 = ld8c(rs, WS_AML + lane * 512), t1 = ld8c(rs, WS_QX + (64 * 64 + lane * 32) * 8);
        if (__all((t0[1] == tg.hi) & (t1[1] == tg.hi))) break;
        __builtin_amdgcn_s_sleep(8);
      }
      if (lane == 0) __hip_atomic_store((gu64*)(ws + WS_EP), ((u64)tg.hi << 32) | tg.lo, __ATOMIC_RELAXED, __HIP_MEMORY_SCOPE_AGENT);
    }
    STAMP_FLUSH(0); TR_FLUSH();
    return;
  }
  if (cls == 1) {
    {
      const u16* pa = (const u16*)p1 + (w4 * 128 + 16 * (lane >> 3)) * 4096 + h * 128 + b * 64 + 8 * (lane & 7);
      const u16* pb = (const u16*)p4 + (h * 128 + b * 64 + w4 * 2) * 512 + 8 * lane;
#pragma unroll
      for (int i = 0; i < 16; ++i) {
        Wa[i] = ld16nt(pa + i * 4096);
        SCHED();
        if ((i & 1) && !((idx & 7) == 3 || idx == 63)) asm volatile("s_waitcnt vmcnt(6)" ::: "memory");
        if ((i & 7) == 7) TR();
        SCHED();
      }
#pragma unroll
      for (int i = 0; i < 16; ++i) {
        Wb[i] = ld16nt(pb + (i >> 1) * 4096 + (i & 1) * 512);
        SCHED();
        if (i & 1) asm volatile("s_waitcnt vmcnt(6)" ::: "memory");
        if ((i & 7) == 7) TR();
        SCHED();
      }
    }
    float* red2 = red + 2048;
    float* wsh = red + 3072;
    if (w4 == 0) {
      const unsigned fo = WS_AML + (lane * 32 + h) * 16;
      while (wall_clock64() - tstart < TAML) __builtin_amdgcn_s_sleep(8);
      u32x4 g;
      bool ok = false;
#pragma unroll 1
      for (int spin = 0; spin < SPIN_MAX; ++spin) {
        COMPILER_FENCE();
        if (!ok) g = ld16c(rs, fo);
        ok = (g[1] == tg.hi) & (g[3] == tg.hi);
        if (__all(ok)) break;
        __builtin_amdgcn_s_sleep(2);
      }
      lds_add(cnt + 7, 1u, lane);
      const float mu = __uint_as_float(g[0]), lu = __uint_as_float(g[2]);
      const float M = x16_max(row16_max(mu));
      const float wl = ex2((mu - M) * C2);
      wsh[lane] = wl / x16_sum(row16_sum(wl * lu));
      lds_arrive(cnt + 7, lane);
    } else lds_wait(cnt + 7, 1u);
    COMPILER_FENCE();
    STAMP(3);
    u32x4 pp[16];
#pragma unroll
    for (int i = 0; i < 16; ++i) { pp[i] = ld16c(rs, WS_APO + (h * 64 + w4 * 16 + (i ^ (8 * b))) * 1024 + lane * 16); SCHED(); }
    lds_wait(cnt + 7, 2u);
    float a8[8];
#pragma unroll
    for (int k = 0; k < 8; ++k) a8[k] = 0.f;
    const float wv = wsh[w4 * 16 + (c16 ^ (8 * b))];
#pragma unroll
    for (int i = 0; i < 16; ++i) {
      const float wt = rdlane(wv, i);
#pragma unroll
      for (int k = 0; k < 4; ++k) { a8[2 * k] = fmaf(wt, bflo(pp[i][k]), a8[2 * k]); a8[2 * k + 1] = fmaf(wt, bfhi(pp[i][k]), a8[2 * k + 1]); }
      asm volatile("" : "+v"(a8[0]), "+v"(a8[1]), "+v"(a8[2]), "+v"(a8[3]), "+v"(a8[4]), "+v"(a8[5]), "+v"(a8[6]), "+v"(a8[7]));
      SCHED();
    }
    *reinterpret_cast<f32x4*>(red + w4 * 512 + 8 * lane) = f32x4{a8[0], a8[1], a8[2], a8[3]};
    *reinterpret_cast<f32x4*>(red + w4 * 512 + 8 * lane + 4) = f32x4{a8[4], a8[5], a8[6], a8[7]};
    sbar4(cnt + 3, lane);
    STAMP(4);
#pragma unroll
    for (int i = 0; i < 4; ++i) {
      const float* src = red + (i >> 1) * 1024 + w4 * 128 + 16 * (lane >> 3) + 8 * (i & 1);
      const f32x4 t0 = *reinterpret_cast<const f32x4*>(src) + *reinterpret_cast<const f32x4*>(src + 512), t1 = *reinterpret_cast<const f32x4*>(src + 4) + *reinterpret_cast<const f32x4*>(src + 516);
      xv[i] = u32x4{pk_bf(t0[0], t0[1]), pk_bf(t0[2], t0[3]), pk_bf(t1[0], t1[1]), pk_bf(t1[2], t1[3])};
    }
    gemv(red2 + w4 * 128);
    sbar4(cnt + 4, lane);
    STAMP(12);
    unsigned op;
    {
      f32x2 t = {0.f, 0.f};
#pragma unroll
      for (int w = 0; w < 4; ++w) t += *reinterpret_cast<const f32x2*>(red2 + w * 128 + (lane >> 5) * 64 + 8 * (lane & 7) + 2 * w4);
      op = pk_bf(t[0], t[1]);
    }
    float* red3 = red + 4096;
    float a[2][8];
#pragma unroll
    for (int bb = 0; bb < 2; ++bb)
#pragma unroll
      for (int k = 0; k < 8; ++k) a[bb][k] = 0.f;
#pragma unroll
    for (int ip = 0; ip < 8; ++ip) {
      const unsigned v0 = __builtin_amdgcn_readlane(op, ip), v1 = __builtin_amdgcn_readlane(op, 32 + ip);
#pragma unroll
      for (int k = 0; k < 4; ++k) {
        const unsigned lo = plo(Wb[2 * ip][k], Wb[2 * ip + 1][k]), hi = phi(Wb[2 * ip][k], Wb[2 * ip + 1][k]);
        a[0][2 * k] = dot2(lo, v0, a[0][2 * k]); a[0][2 * k + 1] = dot2(hi, v0, a[0][2 * k + 1]);
        a[1][2 * k] = dot2(lo, v1, a[1][2 * k]); a[1][2 * k + 1] = dot2(hi, v1, a[1][2 * k + 1]);
      }
    }
#pragma unroll
    for (int bb = 0; bb < 2; ++bb) {
      *reinterpret_cast<f32x4*>(red3 + (w4 * 2 + bb) * 512 + 8 * lane) = f32x4{a[bb][0], a[bb][1], a[bb][2], a[bb][3]};
      *reinterpret_cast<f32x4*>(red3 + (w4 * 2 + bb) * 512 + 8 * lane + 4) = f32x4{a[bb][4], a[bb][5], a[bb][6], a[bb][7]};
    }
    sbar4(cnt + 5, lane);
    {
      f32x4 t = {0.f, 0.f, 0.f, 0.f};
#pragma unroll
      for (int w = 0; w < 4; ++w) t += *reinterpret_cast<const f32x4*>(red3 + w * 1024 + 4 * tid);
      st16c(rs, WS_GA + ((tid >> 7) * 64 + idx) * 2048 + (tid & 127) * 16, u32x4{pk_bf(t[0], t[1]), tg.hi, pk_bf(t[2], t[3]), tg.lo});
    }
    STAMP(5);
    STAMP_FLUSH(0); TR_FLUSH();
    if (PADN) asm volatile("s_endpgm\n\t.fill %0,4,0xBF800000" :: "n"(PADN));
    return;
  }
}

extern "C" void kernel_launch(void* const* d_in, const int* in_sizes, int n_in, void* d_out, int out_size, void* d_ws, size_t ws_size, hipStream_t stream) {
  const int sel[5][3] = {{1, 3, 6}, {2, 4, 0}, {3, 2, 6}, {9, 9, 6}, {7, 8, 10}};
  Args a;
  for (int r = 0; r < 5; ++r) {
    for (int c = 0; c < 3; ++c) a.t[r][c] = (gptr)d_in[sel[r][c]];
    a.t[r][3] = (gptr)d_ws;
  }
  a.w_o = (gptr)d_in[5];
  a.out = (gptr)d_out;
  kmain<<<256, 512, 0, stream>>>(a);
}
```

```cpp
#include <hip/hip_runtime.h>
#include <hip/hip_bf16.h>
#include <stdint.h>

#define DI __device__ __forceinline__
typedef __attribute__((ext_vector_type(4))) unsigned u32x4;
typedef __attribute__((ext_vector_type(2))) unsigned u32x2;
typedef __attribute__((ext_vector_type(8))) unsigned u32x8;
typedef __attribute__((ext_vector_type(16))) __bf16 bf16x16_t;
typedef __attribute__((ext_vector_type(4))) float f32x4;
typedef __attribute__((ext_vector_type(2))) float f32x2;
typedef __attribute__((ext_vector_type(16))) float f32x16;
typedef __attribute__((ext_vector_type(8))) __bf16 bf16x8_t;
typedef __attribute__((ext_vector_type(2))) __bf16 bf16x2_t;
typedef __attribute__((ext_vector_type(4))) short s16x4;
typedef __attribute__((ext_vector_type(8))) short s16x8;
typedef unsigned short u16;
typedef unsigned long long u64;

#define SHARES 48, 48, 80, 80
constexpr int SH[4] = {SHARES};
constexpr int NR[4] = {8, 8, 10, 10};
static_assert(SH[0] + SH[1] + SH[2] + SH[3] == 256 && SH[0] <= 64 && SH[1] <= 64 && SH[2] <= 80 && SH[3] <= 80 && SH[0] >= 8 && SH[1] >= 8, "every key once");
#define TQX 470
#define TQA 870
#define TAML 1710
#define TFIN 0
#define PADN 200
constexpr float C2 = 0.08838834764831845f * 1.4426950408889634f;
constexpr u64 GOLD = 0x9E3779B97F4A7C15ull;

constexpr unsigned WS_EP = 0;
constexpr unsigned WS_QX = 4096;
constexpr unsigned WS_QA = WS_QX + 2 * 2 * 32 * 64 * 8;
constexpr unsigned WS_AML = WS_QA + 2 * 32 * 256 * 8;
constexpr unsigned WS_APO = WS_AML + 64 * 32 * 16;
constexpr unsigned WS_GB = WS_APO + 32 * 64 * 512 * 2;
constexpr unsigned WS_GA = WS_GB + 2 * 128 * 128 * 16;
constexpr unsigned WS_GZ = WS_GA + 2 * 64 * 128 * 16;
constexpr unsigned WS_END = WS_GZ + 256 * 128 + 128;
#define STAMP(i) do {} while (0)
#define STAMP_DECL do {} while (0)
#define STAMP_FLUSH(grp) do {} while (0)

#define TR_DECL do {} while (0)
#define TR() do {} while (0)
#define TR_FLUSH() do {} while (0)
DI float bflo(unsigned u) { return __uint_as_float(u << 16); }
DI float bfhi(unsigned u) { return __uint_as_float(u & 0xffff0000u); }
DI unsigned pk_bf(float a, float b) { const f32x2 v = {a, b}; return __builtin_bit_cast(unsigned, __builtin_convertvector(v, bf16x2_t)); }
DI float dot2(unsigned k, unsigned q, float acc) {
  return __builtin_amdgcn_fdot2_f32_bf16(__builtin_bit_cast(bf16x2_t, k), __builtin_bit_cast(bf16x2_t, q), acc, false);
}
DI unsigned plo(unsigned w0, unsigned w1) { return __builtin_amdgcn_perm(w1, w0, 0x05040100u); }
DI unsigned phi(unsigned w0, unsigned w1) { return __builtin_amdgcn_perm(w1, w0, 0x07060302u); }
template <int CTRL> DI float dpp(float x) {
  return __builtin_bit_cast(float, __builtin_amdgcn_mov_dpp(__builtin_bit_cast(int, x), CTRL, 0xf, 0xf, true));
}
DI float row16_sum(float x) { x += dpp<0xB1>(x); x += dpp<0x4E>(x); x += dpp<0x141>(x); x += dpp<0x128>(x); return x; }
DI float row16_max(float x) { x = fmaxf(x, dpp<0xB1>(x)); x = fmaxf(x, dpp<0x4E>(x)); x = fmaxf(x, dpp<0x141>(x)); x = fmaxf(x, dpp<0x128>(x)); return x; }
DI float x16_sum(float x) { auto s = __builtin_amdgcn_permlane16_swap(__float_as_uint(x), __float_as_uint(x), false, false); return __uint_as_float(s[0]) + __uint_as_float(s[1]); }
DI float x16_max(float x) { auto s = __builtin_amdgcn_permlane16_swap(__float_as_uint(x), __float_as_uint(x), false, false); return fmaxf(__uint_as_float(s[0]), __uint_as_float(s[1])); }
DI float xhalf_max(float x) { auto t = __builtin_amdgcn_permlane32_swap(__float_as_uint(x), __float_as_uint(x), false, false); return fmaxf(__uint_as_float(t[0]), __uint_as_float(t[1])); }
DI float xhalf_sum(float x) { auto t = __builtin_amdgcn_permlane32_swap(__float_as_uint(x), __float_as_uint(x), false, false); return __uint_as_float(t[0]) + __uint_as_float(t[1]); }
DI u32x4 ld16(const void* p) { return *reinterpret_cast<const u32x4*>(p); }
DI u32x4 ld16nt(const void* p) { return __builtin_nontemporal_load(reinterpret_cast<const u32x4*>(p)); }
DI float ex2(float x) { return __builtin_amdgcn_exp2f(x); }
#define SCHED() __builtin_amdgcn_sched_barrier(0)

typedef __attribute__((address_space(1))) u64 gu64;
typedef __amdgpu_buffer_rsrc_t rsrc_t;
DI u32x4 ld16c(rsrc_t rs, unsigned off) { return __builtin_amdgcn_raw_buffer_load_b128(rs, (int)off, 0, 16); }
DI u32x2 ld8c(rsrc_t rs, unsigned off) { return __builtin_amdgcn_raw_buffer_load_b64(rs, (int)off, 0, 16); }
DI void st8c(rsrc_t rs, unsigned off, u32x2 v) { __builtin_amdgcn_raw_buffer_store_b64(v, rs, (int)off, 0, 16); }
DI void st16c(rsrc_t rs, unsigned off, u32x4 v) { __builtin_amdgcn_raw_buffer_store_b128(v, rs, (int)off, 0, 16); }
constexpr int SPIN_MAX = 1 << 20;
constexpr unsigned OOB = 0x7ffffff0u;
DI void rawbar() { __builtin_amdgcn_sched_barrier(0); __builtin_amdgcn_s_barrier(); __builtin_amdgcn_sched_barrier(0); }
struct Tag { unsigned hi, lo; };
#define COMPILER_FENCE() asm volatile("" ::: "memory")

template <int N> struct IC { static constexpr int v = N; };
template <int I, int N, class F> DI void sfor(F&& f) { if constexpr (I < N) { f(IC<I>{}); sfor<I + 1, N>(f); } }
typedef __attribute__((address_space(3))) unsigned lu32;
DI void lds_wait(unsigned* cnt, unsigned target) {
#pragma unroll 1
  for (int spin = 0; spin < SPIN_MAX; ++spin) {
    if (__builtin_amdgcn_readfirstlane(__hip_atomic_load((lu32*)cnt, __ATOMIC_RELAXED, __HIP_MEMORY_SCOPE_WORKGROUP)) >= target) break;
    __builtin_amdgcn_s_sleep(1);
  }
  COMPILER_FENCE();
}
DI void lds_hold(unsigned* flag) {
#pragma unroll 1
  for (int spin = 0; spin < SPIN_MAX; ++spin) {
    if (__builtin_amdgcn_readfirstlane(__hip_atomic_load((lu32*)flag, __ATOMIC_RELAXED, __HIP_MEMORY_SCOPE_WORKGROUP)) == 0u) break;
    __builtin_amdgcn_s_sleep(2);
  }
}
DI unsigned lds_arrive_nw(unsigned addr) {
  unsigned old, one = 1u;
  asm volatile("s_mov_b64 exec, 1\n\tds_add_rtn_u32 %0, %1, %2\n\ts_mov_b64 exec, -1" : "=v"(old) : "v"(addr), "v"(one) : "memory");
  return old;
}
DI unsigned lds_done(unsigned old) { asm volatile("s_waitcnt lgkmcnt(0)" : "+v"(old) :: "memory"); return __builtin_amdgcn_readfirstlane(old); }
DI unsigned lds_arrive(unsigned* cnt, int) { return lds_done(lds_arrive_nw((unsigned)(size_t)(lu32*)cnt)); }
DI void lds_add(unsigned* cnt, unsigned v, int) {
  asm volatile("s_mov_b64 exec, 1\n\tds_add_u32 %0, %1\n\ts_mov_b64 exec, -1" :: "v"((unsigned)(size_t)(lu32*)cnt), "v"(v) : "memory");
}
DI void sbar4(unsigned* cnt, int lane) { if (lds_arrive(cnt, lane) < 3u) lds_wait(cnt, 4u); }
DI float rs32(float a, float b) { auto t = __builtin_amdgcn_permlane32_swap(__float_as_uint(a), __float_as_uint(b), false, false); return __uint_as_float(t[0]) + __uint_as_float(t[1]); }
DI float rs16(float a, float b) { auto t = __builtin_amdgcn_permlane16_swap(__float_as_uint(a), __float_as_uint(b), false, false); return __uint_as_float(t[0]) + __uint_as_float(t[1]); }
template <int N> DI unsigned rowbcast(unsigned x) { return (unsigned)__builtin_amdgcn_mov_dpp((int)x, 0x150 + N, 0xf, 0xf, true); }
DI void pin2w(u32x8& a, u32x8& b) { asm volatile("" : "+v"(a), "+v"(b)); }
DI float rdlane(float x, int l) { return __builtin_bit_cast(float, __builtin_amdgcn_readlane(__builtin_bit_cast(int, x), l)); }

DI unsigned toff(unsigned row, unsigned ch) { return 256u * row + 16u * (ch ^ (((row & 3) << 2) | ((row >> 2) & 3))); }
constexpr int SMEM = 160 * 1024;
constexpr int L_SCR = 131072;
constexpr int L_MRG = 65536;
constexpr int L_CNT = L_SCR + 32 * 1008;
constexpr int L_ML = L_CNT + 64;

struct KV { u32x4 r[8]; };

typedef const __attribute__((address_space(1))) char* gptr;
struct Args { gptr t[5][4]; gptr w_o; gptr out; };

__global__ __launch_bounds__(512, 2) __attribute__((aligned(4096))) void kmain(Args a) {
  __shared__ __attribute__((aligned(16))) char smem[SMEM];
  const int tid = threadIdx.x, blk = blockIdx.x, lane = tid & 63;
  const int wave = __builtin_amdgcn_readfirstlane(tid >> 6), w4 = wave & 3;
  const int cls = ((blk >> 3) + (blk >> 5)) & 3;
  const int arow = wave < 4 ? cls : 4;
  gptr p0 = a.t[arow][0], p1 = a.t[arow][1], p2 = a.t[arow][2], p3 = a.t[arow][3], p4 = a.w_o, p5 = a.out;
  SCHED();
  int idx = 8 * (blk & 7) + (blk >> 5);
  if (wave < 4) idx = (idx & ~7) | ((idx + (cls == 3 ? 2 : (cls == 2 ? 5 : 0))) & 7);
  const int h = idx >> 1, b = idx & 1;
  const int r4 = lane >> 4, c16 = lane & 15;
  unsigned* cnt = reinterpret_cast<unsigned*>(smem + L_CNT);
  STAMP_DECL;
  TR_DECL;
  STAMP(0);
  Tag tg;
  if (wave >= 4) {
    if (tid - 256 < 16) cnt[tid - 256] = 0u;
    const int nr = cls < 2 ? 8 : 10;
    unsigned tab;
    {
      int j = 2 * nr - 1 - lane;
      j = lane < nr ? lane : (j > 2 ? j : (j == 2 ? 0 : j + 1));
      int n = j + (h * nr >> 5);
      n = n >= nr ? n - nr : n;
      int off = 0, wd = 0;
#pragma unroll
      for (int d = 0; d < 4; ++d) {
        const int k = ((2 * n + 1) * NR[d] + (d < cls) - 1 + nr) * (nr == 8 ? 4096 : 3277) >> 16;
        off += k * (SH[d] / NR[d]) + min(k, SH[d] % NR[d]);
        wd = d == cls ? SH[d] / NR[d] + (n < SH[d] % NR[d]) : wd;
      }
      tab = lane < 2 * nr ? (unsigned)(4 * off + w4 * wd) << 10 | (unsigned)wd : 0u;
    }
    asm volatile("" : "+v"(tab));
    asm volatile("" : "+s"(p0), "+s"(p1), "+s"(p2), "+s"(p3));
    SCHED();
    char* ws = (char*)p3;
    const rsrc_t rs = __builtin_amdgcn_make_buffer_rsrc(ws, 0, (int)WS_END, 0x00020000);
    const char* kb = (const char*)p0 + ((size_t)(b * 32 + h) << 20);
    const char* vb = (const char*)p1 + ((size_t)(b * 32 + h) << 20);
    const unsigned lo = lane * 16u, lo2 = lo + 4096u;
    int nx = 0;
    auto nexts = [&]() {
      const unsigned e = __builtin_amdgcn_readlane(tab, nx);
      const rsrc_t r = __builtin_amdgcn_make_buffer_rsrc(const_cast<char*>((nx < nr ? kb : vb) + (e & ~1023u)), 0, (int)((e & 15u) << 10), 0x00020000);
      ++nx;
      return r;
    };
    const rsrc_t s0 = nexts();
    const int rot = cls;
    const char* wb = (const char*)p2 + h * 131072;
    unsigned wl = r4 * 8192 + w4 * 256 + c16 * 16;
    asm volatile("s_waitcnt lgkmcnt(0)" : "+v"(wl), "+v"(tab) :: "memory");
    rawbar();
    KV A, B, X;
    u32x4 W[8][4];
    u32x8 T[2][8];
    auto ldp = [&](KV& S, auto P, rsrc_t q) {
      constexpr int p = P.v;
      S.r[2 * p] = __builtin_amdgcn_raw_buffer_load_b128(q, (int)((p < 2 ? lo : lo2) + (p & 1) * 2048), 0, 2);
      S.r[2 * p + 1] = __builtin_amdgcn_raw_buffer_load_b128(q, (int)((p < 2 ? lo : lo2) + (p & 1) * 2048 + 1024), 0, 2);
    };
    auto landed = [&](KV& S, auto P) { asm volatile("" : "+v"(S.r[2 * P.v]), "+v"(S.r[2 * P.v + 1])); };
    auto ldw = [&](auto G, auto J) { W[G.v][J.v] = ld16(wb + ((((G.v >> 1) + rot) & 3) * 32768 + (G.v & 1) * 4096) + wl + J.v * 1024); };
    auto conv = [&](auto G) {
      constexpr int t = G.v >> 2, j0 = (G.v & 3) * 2;
#pragma unroll
      for (int jj = 0; jj < 2; ++jj)
#pragma unroll
        for (int e = 0; e < 4; ++e) {
          const unsigned a_ = W[G.v][2 * jj][e], b_ = W[G.v][2 * jj + 1][e];
          T[t][2 * e][j0 + jj] = plo(a_, b_); T[t][2 * e + 1][j0 + jj] = phi(a_, b_);
        }
#pragma unroll
      for (int e = 0; e < 8; e += 2) pin2w(T[t][e], T[t][e + 1]);
    };
    const unsigned qo = WS_QX + (((2 + b) * 32 + h) * 64 + 4 * c16) * 8;
    u32x4 g0, g1;
    u32x2 tw;
    {
      const rsrc_t s1 = nexts(), s2 = nexts();
      sfor<0, 12>([&](auto I) {
        constexpr int i = I.v;
        if constexpr (i >= 4) { if constexpr (i < 8) landed(X, IC<i & 3>{}); else landed(B, IC<i & 3>{}); }
        if constexpr (i == 7 || i == 11) TR();
        if constexpr (i == 8) tw = ld8c(rs, lane == 0 ? WS_END - 128 : OOB);
        if constexpr (i == 9) { g0 = ld16c(rs, qo); g1 = ld16c(rs, qo + 16); }
        if constexpr (i < 4) { ldp(X, IC<i & 3>{}, s0); ldw(IC<0>{}, IC<i & 3>{}); }
        else if constexpr (i < 8) { ldp(B, IC<i & 3>{}, s1); ldw(IC<1>{}, IC<i & 3>{}); }
        else { ldp(A, IC<i & 3>{}, s2); ldw(IC<2>{}, IC<i & 3>{}); }
        SCHED();
        if constexpr (i == 8) { conv(IC<0>{}); SCHED(); }
      });
    }
    lds_wait(cnt + 15, 1u);
    tg.hi = __builtin_amdgcn_readfirstlane(cnt[13]); tg.lo = __builtin_amdgcn_readfirstlane(cnt[14]);
    asm volatile("" :: "v"(tw));
    u32x4 qpk;
    {
      SCHED();
      conv(IC<1>{});
      SCHED();
      if (!__all((g0[1] == tg.hi) & (g0[3] == tg.hi) & (g1[1] == tg.hi) & (g1[3] == tg.hi))) {
#pragma unroll 1
        for (int spin = 0; spin < SPIN_MAX; ++spin) {
          COMPILER_FENCE();
          g0 = ld16c(rs, qo); g1 = ld16c(rs, qo + 16);
          if (__all((g0[1] == tg.hi) & (g0[3] == tg.hi) & (g1[1] == tg.hi) & (g1[3] == tg.hi))) break;
        }
      }
      qpk = u32x4{g0[0], g0[2], g1[0], g1[2]};
    }
    STAMP(7);
    const bool holds = cls == 0;
    float sc[10], acc[8];
#pragma unroll
    for (int k = 0; k < 10; ++k) sc[k] = 0.f;
    auto point = [&](KV& S, KV& N, rsrc_t sn, auto P, auto REFILL, auto AH, auto WG) {
      constexpr int ah = AH.v;
      if constexpr (REFILL.v) {
        if constexpr (P.v == 0 && ah > 0) { if (holds) lds_hold(cnt + 8); sfor<0, ah>([&](auto Q) { ldp(N, Q, sn); }); SCHED(); }
        landed(S, P);
        if constexpr (P.v + ah < 4) { if constexpr ((P.v & 1) == 0) { if (holds) lds_hold(cnt + 8); } ldp(N, IC<(P.v + ah) & 3>{}, sn); }
        if constexpr (WG.v >= 0) ldw(WG, P);
        SCHED();
      }
    };
    auto step = [&](KV& S, KV& N, auto REFILL, auto WG, auto ISV) {
      rsrc_t sn = s0;
      if constexpr (REFILL.v) sn = nexts();
      sfor<0, 4>([&](auto P) {
        point(S, N, sn, P, REFILL, IC<(ISV.v || WG.v < 0 ? 1 : 0)>{}, WG);
        if constexpr (!ISV.v) {
#pragma unroll
          for (int t = 2 * P.v; t < 2 * P.v + 2; ++t) {
            acc[t] = 0.f;
#pragma unroll
            for (int k = 0; k < 4; ++k) acc[t] = dot2(S.r[t][k], qpk[k], acc[t]);
          }
          asm volatile("" : "+v"(acc[2 * P.v]), "+v"(acc[2 * P.v + 1]));
        } else {
          const unsigned pp = rowbcast<4 * P.v>(__float_as_uint(sc[0]));
#pragma unroll
          for (int k = 0; k < 4; ++k) { acc[2 * k] = dot2(plo(S.r[2 * P.v][k], S.r[2 * P.v + 1][k]), pp, acc[2 * k]); acc[2 * k + 1] = dot2(phi(S.r[2 * P.v][k], S.r[2 * P.v + 1][k]), pp, acc[2 * k + 1]); }
          asm volatile("" : "+v"(acc[0]), "+v"(acc[1]), "+v"(acc[2]), "+v"(acc[3]), "+v"(acc[4]), "+v"(acc[5]), "+v"(acc[6]), "+v"(acc[7]));
        }
        SCHED();
      });
      if constexpr (!ISV.v) {
        float u[4], v[2];
#pragma unroll
        for (int i = 0; i < 4; ++i) u[i] = (c16 & 8 ? acc[i + 4] : acc[i]) + dpp<0x128>(c16 & 8 ? acc[i] : acc[i + 4]);
#pragma unroll
        for (int i = 0; i < 2; ++i) v[i] = (c16 & 4 ? u[i + 2] : u[i]) + dpp<0x141>(c16 & 4 ? u[i] : u[i + 2]);
        float x = (c16 & 2 ? v[1] : v[0]) + dpp<0x4E>(c16 & 2 ? v[0] : v[1]);
        x += dpp<0xB1>(x);
#pragma unroll
        for (int k = 9; k > 0; --k) sc[k] = sc[k - 1];
        sc[0] = x;
      } else {
#pragma unroll
        for (int k = 0; k < 9; ++k) sc[k] = sc[k + 1];
      }
      SCHED();
      if constexpr (REFILL.v || ISV.v) TR();
    };
    step(B, B, IC<0>{}, IC<-1>{}, IC<0>{});
    step(A, B, IC<1>{}, IC<3>{}, IC<0>{});
    step(X, X, IC<0>{}, IC<-1>{}, IC<0>{});
    conv(IC<2>{});
    sfor<3, 6>([&](auto I) {
      if constexpr (I.v & 1) step(B, A, IC<1>{}, IC<I.v + 1>{}, IC<0>{}); else step(A, B, IC<1>{}, IC<I.v + 1>{}, IC<0>{});
      conv(I);
    });
#pragma unroll 1
    for (int i = 6; i < nr; i += 2) {
      step(A, B, IC<1>{}, IC<-1>{}, IC<0>{});
      step(B, A, IC<1>{}, IC<-1>{}, IC<0>{});
    }
    float m = -INFINITY, l = 0.f;
#pragma unroll
    for (int k = 0; k < 10; ++k) { sc[k] = (unsigned)(c16 >> 1) < (__builtin_amdgcn_readlane(tab, nr + k) & 15u) ? sc[k] : -INFINITY; m = fmaxf(m, sc[k]); }
    m = row16_max(m);
    {
      const float mc = m * C2;
#pragma unroll
      for (int k = 0; k < 10; ++k) {
        const float p = ex2(fmaf(sc[k], C2, -mc));
        const unsigned pk = pk_bf(dpp<0x00>(p), dpp<0xAA>(p));
        l = dot2(pk, 0x3f803f80u, l);
        sc[k] = __uint_as_float(pk);
      }
      l += dpp<0x124>(l); l += dpp<0x128>(l);
      *reinterpret_cast<float*>(smem + L_ML + ((c16 & 1) * 16 + w4 * 4 + r4) * 4) = (c16 & 1) ? l : m;
      COMPILER_FENCE();
      const unsigned got = lds_arrive_nw(L_CNT + 40);
      const float mr = *reinterpret_cast<const float*>(smem + L_ML + c16 * 4), lr = *reinterpret_cast<const float*>(smem + L_ML + 64 + c16 * 4);
      SCHED();
      const bool last = lds_done(got) == 3u;
      const float M = row16_max(mr);
      st8c(rs, last && lane == 0 ? WS_GZ + (b * 128 + h * 4 + cls) * 128 : OOB, u32x2{__float_as_uint(fmaf(M, C2, __builtin_amdgcn_logf(row16_sum(ex2((mr - M) * C2) * lr)))), tg.hi});
    }
#pragma unroll
    for (int k = 0; k < 8; ++k) acc[k] = 0.f;
    SCHED();
    STAMP(8);
    unsigned yoff = (r4 & 1) ? OOB : WS_GB + (b * 128 + h * 4 + cls) * 2048 + 512 * w4 + 32 * c16 + 16 * (lane >> 5), zoff = WS_GZ + (b * 128 + h * 4 + (lane & 3)) * 128;
    unsigned rwo = L_MRG + (w4 * 4 + r4) * 144 + ((c16 - 4 * rot) & 15) * 8, rro = L_MRG + c16 * 144 + r4 * 16;
    asm volatile("" : "+v"(yoff), "+v"(zoff), "+v"(rwo), "+v"(rro));
    u32x2 zq;
    step(A, B, IC<1>{}, IC<7>{}, IC<1>{});
    conv(IC<6>{});
#pragma unroll 1
    for (int i = 2;;) {
      step(B, A, IC<1>{}, IC<-1>{}, IC<1>{});
      i += 2;
      zq = ld8c(rs, i >= nr ? zoff : OOB);
      SCHED();
      step(A, B, IC<1>{}, IC<-1>{}, IC<1>{});
      if (i >= nr) break;
    }
    conv(IC<7>{});
    if (!__all(zq[1] == tg.hi)) {
#pragma unroll 1
      for (int spin = 0; spin < SPIN_MAX; ++spin) {
        COMPILER_FENCE();
        zq = ld8c(rs, zoff);
        if (__all(zq[1] == tg.hi)) break;
        __builtin_amdgcn_s_sleep(8);
      }
    }
    float wt;
    int six = (lane & 1) ? 0xEEEE : 0x4444;
    {
      const float z = __uint_as_float(zq[0]);
      float Z = fmaxf(z, dpp<0xB1>(z)); Z = fmaxf(Z, dpp<0x4E>(Z));
      float L = ex2(z - Z); L += dpp<0xB1>(L); L += dpp<0x4E>(L);
      Z += __builtin_amdgcn_logf(L);
      wt = ex2(fmaf(m, C2, -Z));
      asm volatile("" : "+v"(wt), "+v"(six));
    }
    f32x4 d[8];
#pragma unroll
    for (int e = 0; e < 8; ++e) { d[e] = f32x4{0.f, 0.f, 0.f, 0.f}; asm volatile("" : "+v"(d[e])); }
    if (cls == 0) lds_wait(cnt + 1, 4u);
    SCHED();
    step(B, B, IC<0>{}, IC<-1>{}, IC<1>{});
    STAMP(1);
#pragma unroll
    for (int k = 0; k < 8; ++k) acc[k] *= wt;
    *reinterpret_cast<u32x2*>(smem + rwo) = u32x2{pk_bf(rs16(acc[0], acc[2]), rs16(acc[1], acc[3])), pk_bf(rs16(acc[4], acc[6]), rs16(acc[5], acc[7]))};
    {
      u32x4 af[2];
      unsigned got = lds_arrive_nw(L_CNT), need = 3u;
#pragma unroll 1
      for (int spin = 0; spin < SPIN_MAX; ++spin) {
#pragma unroll
        for (int t = 0; t < 2; ++t) af[t] = *reinterpret_cast<const u32x4*>(smem + rro + t * 64);
        SCHED();
        if (lds_done(got) >= need) break;
        need = 4u;
        __builtin_amdgcn_s_sleep(1);
        asm volatile("ds_read_b32 %0, %1" : "=v"(got) : "v"((unsigned)L_CNT) : "memory");
      }
      SCHED();
      STAMP(3);
      float v4[4];
#pragma unroll
      for (int k = 0; k < 4; ++k)
#pragma unroll
        for (int t = 0; t < 2; ++t) {
          d[k] = __builtin_amdgcn_smfmac_f32_16x16x64_bf16(__builtin_bit_cast(bf16x8_t, af[t]), __builtin_bit_cast(bf16x16_t, T[t][k]), d[k], six, 0, 0);
          d[k + 4] = __builtin_amdgcn_smfmac_f32_16x16x64_bf16(__builtin_bit_cast(bf16x8_t, af[t]), __builtin_bit_cast(bf16x16_t, T[t][k + 4]), d[k + 4], six, 0, 0);
        }
#pragma unroll
      for (int k = 0; k < 4; ++k) {
        const f32x2 x = d[k].xy + d[k].zw, y = d[k + 4].xy + d[k + 4].zw;
        v4[k] = x16_sum(rs32(x[0] + x[1], y[0] + y[1]));
      }
      STAMP(4);
      st16c(rs, yoff, u32x4{pk_bf(v4[0], v4[1]), tg.hi, pk_bf(v4[2], v4[3]), tg.lo});
      SCHED();
      lds_add(cnt + 11, 1u, lane);
    }
    STAMP(2);
    STAMP_FLUSH(1); TR_FLUSH();
    return;
  }
  u32x4 Wa[16], Wb[16], xv[4];
  unsigned pf;
  u64 epw, tstart;
  {
    unsigned oa[16];
    oa[0] = (cls & 1) ? (w4 * 128 + 16 * (lane >> 3)) * 8192 + idx * 128 + 16 * (lane & 7) : (cls == 2 ? (b * 256 + w4 * 64 + 16 * r4) * 8192 + h * 256 + 16 * c16 : (idx * 128 + w4) * 1024 + 16 * lane);
#pragma unroll
    for (int i = 1; i < 16; ++i) oa[i] = oa[i - 1] + (cls ? 8192 : 4096);
    unsigned op = (((blk >> 5) * 4 + w4) * 16 + c16) * 8192 + ((idx & 56) + 3 + (cls == 0 ? -1 : (idx >= 56 ? 4 : 0)) * (lane >> 5)) * 128 + (r4 & 1) * 64;
    unsigned ox = w4 * 256 + 32 * (lane >> 3);
#pragma unroll
    for (int i = 0; i < 16; i += 8) asm volatile("" : "+v"(oa[i]), "+v"(oa[i + 1]), "+v"(oa[i + 2]), "+v"(oa[i + 3]), "+v"(oa[i + 4]), "+v"(oa[i + 5]), "+v"(oa[i + 6]), "+v"(oa[i + 7]));
    asm volatile("" : "+v"(op), "+v"(ox));
    asm volatile("" : "+s"(p0), "+s"(p1), "+s"(p2), "+s"(p3));
    SCHED();
    tstart = wall_clock64();
    SCHED();
    pf = *reinterpret_cast<const unsigned*>((const char*)p1 + op);
    epw = __hip_atomic_load((gu64*)p3, __ATOMIC_RELAXED, __HIP_MEMORY_SCOPE_AGENT);
#pragma unroll
    for (int i = 0; i < 4; ++i) xv[i] = ld16((const char*)p2 + ox + (i >> 1) * 1024 + 16 * (i & 1));
    SCHED();
#pragma unroll
    for (int i = 0; i < 16; ++i) { Wa[i] = ld16nt((const char*)p0 + oa[i]); SCHED(); }
  }
  char* ws = (char*)p3;
  const rsrc_t rs = __builtin_amdgcn_make_buffer_rsrc(ws, 0, (int)WS_END, 0x00020000);
  u16* out = (u16*)p5;
  {
    rawbar();
    const u64 t64 = (((u64)(unsigned)__builtin_amdgcn_readfirstlane((unsigned)(epw >> 32)) << 32) | (unsigned)__builtin_amdgcn_readfirstlane((unsigned)epw)) + GOLD;
    tg.hi = (unsigned)(t64 >> 32); tg.lo = (unsigned)t64;
    asm volatile("" : "+s"(tg.hi), "+s"(tg.lo) : "v"(pf) : "memory");
    if (w4 == 0) { cnt[13] = tg.hi; cnt[14] = tg.lo; COMPILER_FENCE(); lds_add(cnt + 15, 1u, lane); }
    STAMP(13);
  }
  float* red = reinterpret_cast<float*>(smem);
  auto gemv = [&](float* dst) {
    float acc[2][8];
#pragma unroll
    for (int bb = 0; bb < 2; ++bb)
#pragma unroll
      for (int k = 0; k < 8; ++k) acc[bb][k] = 0.f;
#pragma unroll
    for (int ip = 0; ip < 8; ++ip) {
      const unsigned xp0 = xv[ip >> 2][ip & 3], xp1 = xv[2 + (ip >> 2)][ip & 3];
#pragma unroll
      for (int k = 0; k < 4; ++k) {
        const unsigned lo = plo(Wa[2 * ip][k], Wa[2 * ip + 1][k]), hi = phi(Wa[2 * ip][k], Wa[2 * ip + 1][k]);
        acc[0][2 * k] = dot2(lo, xp0, acc[0][2 * k]); acc[0][2 * k + 1] = dot2(hi, xp0, acc[0][2 * k + 1]);
        acc[1][2 * k] = dot2(lo, xp1, acc[1][2 * k]); acc[1][2 * k + 1] = dot2(hi, xp1, acc[1][2 * k + 1]);
      }
    }
    float v8[8], v4[4];
#pragma unroll
    for (int k = 0; k < 8; ++k) v8[k] = rs32(acc[0][k], acc[1][k]);
#pragma unroll
    for (int k = 0; k < 4; ++k) { const float t = rs16(v8[k], v8[k + 4]); v4[k] = t + dpp<0x128>(t); }
    *reinterpret_cast<f32x4*>(dst + (lane >> 5) * 64 + 8 * (lane & 7) + 4 * (r4 & 1)) = f32x4{v4[0], v4[1], v4[2], v4[3]};
    COMPILER_FENCE();
  };
  if (cls & 1) {
    gemv(red + w4 * 128);
    const unsigned got = lds_arrive_nw(L_CNT + 8);
    f32x4 t4[4];
#pragma unroll
    for (int w = 0; w < 4; ++w) t4[w] = *reinterpret_cast<const f32x4*>(red + w * 128 + 4 * (lane & 31));
    SCHED();
    if (lds_done(got) == 3u) {
      const f32x4 t = (t4[0] + t4[1]) + (t4[2] + t4[3]);
      st16c(rs, lane < 32 ? WS_QX + ((((cls >> 1) * 2 + r4) * 32 + h) * 64 + b * 32 + 2 * c16) * 8 : OOB, u32x4{pk_bf(t[0], t[1]), tg.hi, pk_bf(t[2], t[3]), tg.hi});
      SCHED();
      lds_add(cnt + 2, 1u, lane);
    } else lds_wait(cnt + 2, 5u);
    STAMP(8);
  } else if (cls == 2) {
    while (wall_clock64() - tstart < TQX) __builtin_amdgcn_s_sleep(4);
    u32x4 q0, q1;
    const unsigned qo = WS_QX + (h * 64 + 4 * c16) * 8;
#pragma unroll 1
    for (int spin = 0; spin < SPIN_MAX; ++spin) {
      COMPILER_FENCE();
      const u32x4 g0 = ld16c(rs, qo), g1 = ld16c(rs, qo + 16), g2 = ld16c(rs, qo + 16384), g3 = ld16c(rs, qo + 16400);
      q0 = u32x4{g0[0], g0[2], g1[0], g1[2]}; q1 = u32x4{g2[0], g2[2], g3[0], g3[2]};
      if (__all((g0[1] == tg.hi) & (g0[3] == tg.hi) & (g1[1] == tg.hi) & (g1[3] == tg.hi) & (g2[1] == tg.hi) & (g2[3] == tg.hi) & (g3[1] == tg.hi) & (g3[3] == tg.hi))) break;
      __builtin_amdgcn_s_sleep(1);
    }
    STAMP(9);
    float v[4];
    {
      float u[16], t8[8];
#pragma unroll
      for (int i = 0; i < 16; ++i) {
        float a = 0.f, c = 0.f;
#pragma unroll
        for (int k = 0; k < 4; ++k) { a = dot2(Wa[i][k], q0[k], a); c = dot2(Wa[i][k], q1[k], c); }
        u[i] = (c16 & 4 ? c : a) + dpp<0x141>(c16 & 4 ? a : c);
        asm volatile("" : "+v"(u[i]));
      }
#pragma unroll
      for (int i = 0; i < 8; ++i) { t8[i] = (c16 & 2 ? u[i + 8] : u[i]) + dpp<0x4E>(c16 & 2 ? u[i] : u[i + 8]); asm volatile("" : "+v"(t8[i])); }
#pragma unroll
      for (int i = 0; i < 4; ++i) {
        float t = (c16 & 1 ? t8[i + 4] : t8[i]) + dpp<0xB1>(c16 & 1 ? t8[i] : t8[i + 4]);
        asm volatile("" : "+v"(t));
        v[i] = t + dpp<0x128>(t);
      }
    }
    st16c(rs, c16 < 8 ? WS_QA + ((c16 >> 2) * 32 + h) * 2048 + (b * 256 + w4 * 64 + 16 * r4 + 4 * (c16 & 3)) * 4 : OOB, u32x4{pk_bf(v[0], v[1]), tg.hi, pk_bf(v[2], v[3]), tg.hi});
    STAMP(10);
  } else {
    {
      const u16* pb = (const u16*)p0 + (idx * 128 + 64 + w4) * 512 + 8 * lane;
#pragma unroll
      for (int i = 0; i < 16; ++i) {
        Wb[i] = ld16nt(pb + i * 2048);
        if (i >= 4 && (i & 1)) asm volatile("" : "+v"(Wb[i - 4]));
        if (i == 9) TR();
        SCHED();
      }
    }
    lds_add(cnt + 8, 1u, lane);
#pragma unroll
    for (int i = 0; i < 16; ++i) *reinterpret_cast<u32x4*>(smem + ((i >> 3) * 4 + r4) * 8192 + toff(4 * (i & 7) + w4, c16)) = Wa[i];
#pragma unroll
    for (int i = 0; i < 16; ++i) *reinterpret_cast<u32x4*>(smem + ((2 + (i >> 3)) * 4 + r4) * 8192 + toff(4 * (i & 7) + w4, c16)) = Wb[i];
    STAMP(8);
    TR();
  }
  if (cls >= 2) {
    const unsigned tw = __builtin_nontemporal_load(reinterpret_cast<const unsigned*>(out + (idx >> 5) * 1024 + (cls & 1) * 512 + 16 * (idx & 31)));
    asm volatile("" :: "v"(tw));
  }
  asm volatile("" :: "v"(xv[0]), "v"(xv[1]), "v"(xv[2]), "v"(xv[3]));
  SCHED();
  if (cls >= 2) {
    unsigned d0 = (cls == 3 ? WS_GB + (idx >> 5) * 128 * 2048 : WS_GA + (idx >> 5) * 64 * 2048) + (16 * w4 + (lane >> 2)) * 2048 + (idx & 31) * 64 + (lane & 3) * 16;
    unsigned d1 = cls == 3 ? d0 + 64 * 2048 : d0;
    unsigned fw = (lane & 12) ? 32768 + 256 + lane * 4 : 32768 + (w4 * 16 + 4 * (lane & 3) + r4) * 4, fr = 32768 + 8 * (lane & 7);
    unsigned oo = lane < 8 ? ((idx >> 5) * 1024 + (cls & 1) * 512 + 16 * (idx & 31) + 2 * lane) * 2 : OOB;
    float f1 = cls == 3 ? 1.f : 0.f;
    const rsrc_t ro = __builtin_amdgcn_make_buffer_rsrc(out, 0, 4096, 0x00020000);
    asm volatile("" : "+v"(d0), "+v"(d1), "+v"(fw), "+v"(fr), "+v"(oo), "+v"(f1));
    lds_wait(cnt + 11, 4u);
    while (wall_clock64() - tstart < TFIN) __builtin_amdgcn_s_sleep(2);
    STAMP(3);
    u32x4 x, y;
    bool ok = false;
#pragma unroll 1
    for (int spin = 0; spin < SPIN_MAX; ++spin) {
      COMPILER_FENCE();
      if (!ok) { x = ld16c(rs, d0); y = ld16c(rs, d1); }
      ok = (((x[1] ^ tg.hi) | (y[1] ^ tg.hi)) | ((x[3] ^ tg.lo) | (y[3] ^ tg.lo))) == 0u;
      if (__all(ok)) break;
    }
    STAMP(14);
    float s = rs16(rs32(fmaf(f1, bflo(y[0]), bflo(x[0])), fmaf(f1, bflo(y[2]), bflo(x[2]))), rs32(fmaf(f1, bfhi(y[0]), bfhi(x[0])), fmaf(f1, bfhi(y[2]), bfhi(x[2]))));
    s += dpp<0x124>(s); s += dpp<0x128>(s);
    *reinterpret_cast<float*>(smem + fw) = s;
    COMPILER_FENCE();
    const unsigned got = lds_arrive_nw(L_CNT + 24);
    f32x2 t[4];
#pragma unroll
    for (int w = 0; w < 4; ++w) t[w] = *reinterpret_cast<const f32x2*>(smem + fr + w * 64);
    SCHED();
    if (lds_done(got) == 3u) {
      const f32x2 r = (t[0] + t[1]) + (t[2] + t[3]);
      __builtin_amdgcn_raw_buffer_store_b32(pk_bf(r[0], r[1]), ro, (int)oo, 0, 0);
    }
    STAMP(6);
    STAMP_FLUSH(0); TR_FLUSH();
    return;
  }
  if (cls == 0) {
    char* scr = smem + L_SCR;
    const int ab = idx >> 5, r = lane & 31, hh = lane >> 5;
    u32x4 qlast = {0u, 0u, 0u, 0u};
    {
      u32x4 g[18];
      const int fl = (idx & 1) * 8;
      const unsigned o8 = WS_QA + (ab * 32 + w4 * 8) * 2048 + lane * 16, ol = WS_QA + (ab * 32 + r) * 2048 + 1984 + 32 * hh;
      while (wall_clock64() - tstart < TQA) __builtin_amdgcn_s_sleep(2);
      unsigned pend = 0x3ffffu;
#pragma unroll 1
      for (int spin = 0; spin < SPIN_MAX && pend; ++spin) {
        COMPILER_FENCE();
#pragma unroll
        for (int i = 0; i < 18; ++i) g[i] = ld16c(rs, (pend >> i & 1) ? (i < 16 ? o8 + (i ^ fl) * 1024 : ol + 16 * (i - 16)) : OOB);
        if (spin == 0) { SCHED(); lds_add(cnt + 8, ~0u, lane); SCHED(); }
#pragma unroll
        for (int i = 0; i < 18; ++i) {
          if ((pend >> i & 1) && __all((g[i][1] == tg.hi) & (g[i][3] == tg.hi))) {
            pend &= ~(1u << i);
            if (i >= 16) { qlast[2 * (i - 16)] = g[i][0]; qlast[2 * (i - 16) + 1] = g[i][2]; }
            else if (!(i & 1) || lane < 60) *reinterpret_cast<u32x2*>(scr + (w4 * 8 + ((i ^ fl) >> 1)) * 1008 + (i & 1) * 512 + 8 * lane) = u32x2{g[i][0], g[i][2]};
          }
        }
        if (pend) __builtin_amdgcn_s_sleep(8);
      }
    }
    sbar4(cnt + 3, lane);
    STAMP(10);
    f32x16 sacc;
#pragma unroll
    for (int i = 0; i < 16; ++i) sacc[i] = 0.f;
#pragma unroll
    for (int jj = 0; jj < 32; ++jj) {
      const u32x4 af = *reinterpret_cast<const u32x4*>(smem + (w4 * 4 + (jj >> 3)) * 8192 + toff(r, 2 * (jj & 7) + hh));
      const u32x4 bf = jj < 31 ? *reinterpret_cast<const u32x4*>(scr + r * 1008 + 16 * (2 * (jj < 31 ? jj : 0) + hh)) : qlast;
      sacc = __builtin_amdgcn_mfma_f32_32x32x16_bf16(__builtin_bit_cast(bf16x8_t, af), __builtin_bit_cast(bf16x8_t, bf), sacc, 0, 0, 0);
    }
    float m = sacc[0];
#pragma unroll
    for (int i = 1; i < 16; ++i) m = fmaxf(m, sacc[i]);
    m = xhalf_max(m);
    if (hh == 0) *reinterpret_cast<float*>(scr + r * 1008 + 992 + 4 * w4) = m;
    sbar4(cnt + 4, lane);
    STAMP(11);
    {
      const f32x4 m4 = *reinterpret_cast<const f32x4*>(scr + r * 1008 + 992);
      m = fmaxf(fmaxf(m4[0], m4[1]), fmaxf(m4[2], m4[3]));
    }
    const float mc = m * C2;
    float p[16], ls = 0.f;
#pragma unroll
    for (int i = 0; i < 16; ++i) { p[i] = ex2(fmaf(sacc[i], C2, -mc)); ls += p[i]; }
    ls = xhalf_sum(ls);
    sbar4(cnt + 5, lane);
    u32x4* pf = reinterpret_cast<u32x4*>(scr);
    float* wl = reinterpret_cast<float*>(scr + 8192);
    if (hh == 0) wl[r * 4 + w4] = ls;
    pf[(2 * w4) * 64 + lane] = u32x4{pk_bf(p[0], p[1]), pk_bf(p[2], p[3]), pk_bf(p[4], p[5]), pk_bf(p[6], p[7])};
    pf[(2 * w4 + 1) * 64 + lane] = u32x4{pk_bf(p[8], p[9]), pk_bf(p[10], p[11]), pk_bf(p[12], p[13]), pk_bf(p[14], p[15])};
    sbar4(cnt + 6, lane);
    u32x4 pb8[8];
#pragma unroll
    for (int ks = 0; ks < 8; ++ks) pb8[ks] = pf[ks * 64 + lane];
    const f32x4 l4 = *reinterpret_cast<const f32x4*>(wl + r * 4);
    const int qq = (lane & 15) >> 2, pp = lane & 3, blk2 = (lane >> 4) & 1;
    char* ot = scr + 12288 + w4 * 4096;
#pragma unroll
    for (int c2 = 0; c2 < 2; ++c2) {
      f32x16 oacc[2];
#pragma unroll
      for (int ct = 0; ct < 2; ++ct)
#pragma unroll
        for (int i = 0; i < 16; ++i) oacc[ct][i] = 0.f;
#pragma unroll
      for (int ks = 0; ks < 8; ++ks)
#pragma unroll
        for (int ct = 0; ct < 2; ++ct) {
          typedef __attribute__((address_space(3))) s16x4* lp;
          const char* tile = smem + ((ks >> 1) * 4 + w4) * 8192;
          const int cc = 2 * c2 + ct;
          const s16x4 lo = __builtin_amdgcn_ds_read_tr16_b64_v4i16((lp)(tile + toff(16 * (ks & 1) + 4 * hh + qq, 4 * cc + 2 * blk2 + (pp >> 1)) + 8 * (pp & 1)));
          const s16x4 hi = __builtin_amdgcn_ds_read_tr16_b64_v4i16((lp)(tile + toff(16 * (ks & 1) + 8 + 4 * hh + qq, 4 * cc + 2 * blk2 + (pp >> 1)) + 8 * (pp & 1)));
          const s16x8 afr = __builtin_shufflevector(lo, hi, 0, 1, 2, 3, 4, 5, 6, 7);
          oacc[ct] = __builtin_amdgcn_mfma_f32_32x32x16_bf16(__builtin_bit_cast(bf16x8_t, afr), __builtin_bit_cast(bf16x8_t, pb8[ks]), oacc[ct], 0, 0, 0);
        }
#pragma unroll
      for (int ct = 0; ct < 2; ++ct)
#pragma unroll
        for (int g = 0; g < 4; ++g)
          *reinterpret_cast<u32x2*>(ot + r * 128 + 16 * ((4 * ct + g) ^ (r & 7)) + 8 * hh) = u32x2{pk_bf(oacc[ct][4 * g], oacc[ct][4 * g + 1]), pk_bf(oacc[ct][4 * g + 2], oacc[ct][4 * g + 3])};
      if (c2 == 0) lds_add(cnt + 8, 1u, lane);
      __builtin_amdgcn_wave_barrier();
#pragma unroll
      for (int i = 0; i < 4; ++i) {
        const int hd = 8 * i + (lane >> 3), pc = lane & 7;
        const u32x4 vv = *reinterpret_cast<const u32x4*>(ot + hd * 128 + 16 * (pc ^ (hd & 7)));
        st16c(rs, WS_APO + ((hd * 64 + idx) * 512 + w4 * 128 + 64 * c2 + 8 * pc) * 2, vv);
      }
      __builtin_amdgcn_wave_barrier();
    }
    lds_arrive(cnt + 1, lane);
    STAMP(1);
    asm volatile("s_waitcnt vmcnt(0)" ::: "memory");
    if (lds_arrive(cnt + 9, lane) == 3u && hh == 0)
      st16c(rs, WS_AML + (idx * 32 + r) * 16, u32x4{__float_as_uint(m), tg.hi, __float_as_uint((l4[0] + l4[1]) + (l4[2] + l4[3])), tg.hi});
    lds_add(cnt + 8, ~0u, lane);
    STAMP(2);
    if (idx == 0 && w4 == 0) {
      while (wall_clock64() - tstart < TAML + 200) __builtin_amdgcn_s_sleep(8);
#pragma unroll 1
      for (int spin = 0; spin < SPIN_MAX; ++spin) {
        COMPILER_FENCE();
        const u32x2 t0 = ld8c(rs, WS_AML + lane * 512), t1 = ld8c(rs, WS_QX + (64 * 64 + lane * 32) * 8);
        if (__all((t0[1] == tg.hi) & (t1[1] == tg.hi))) break;
        __builtin_amdgcn_s_sleep(8);
      }
      if (lane == 0) __hip_atomic_store((gu64*)(ws + WS_EP), ((u64)tg.hi << 32) | tg.lo, __ATOMIC_RELAXED, __HIP_MEMORY_SCOPE_AGENT);
    }
    STAMP_FLUSH(0); TR_FLUSH();
    return;
  }
  if (cls == 1) {
    {
      const u16* pa = (const u16*)p1 + (w4 * 128 + 16 * (lane >> 3)) * 4096 + h * 128 + b * 64 + 8 * (lane & 7);
      const u16* pb = (const u16*)p4 + (h * 128 + b * 64 + w4 * 2) * 512 + 8 * lane;
#pragma unroll
      for (int i = 0; i < 16; ++i) {
        Wa[i] = ld16nt(pa + i * 4096);
        SCHED();
        if ((i & 1) && !((idx & 7) == 3 || idx == 63)) asm volatile("s_waitcnt vmcnt(6)" ::: "memory");
        if ((i & 7) == 7) TR();
        SCHED();
      }
#pragma unroll
      for (int i = 0; i < 16; ++i) {
        Wb[i] = ld16nt(pb + (i >> 1) * 4096 + (i & 1) * 512);
        SCHED();
        if (i & 1) asm volatile("s_waitcnt vmcnt(6)" ::: "memory");
        if ((i & 7) == 7) TR();
        SCHED();
      }
    }
    float* red2 = red + 2048;
    float* wsh = red + 3072;
    if (w4 == 0) {
      const unsigned fo = WS_AML + (lane * 32 + h) * 16;
      while (wall_clock64() - tstart < TAML) __builtin_amdgcn_s_sleep(8);
      u32x4 g;
      bool ok = false;
#pragma unroll 1
      for (int spin = 0; spin < SPIN_MAX; ++spin) {
        COMPILER_FENCE();
        if (!ok) g = ld16c(rs, fo);
        ok = (g[1] == tg.hi) & (g[3] == tg.hi);
        if (__all(ok)) break;
        __builtin_amdgcn_s_sleep(2);
      }
      lds_add(cnt + 7, 1u, lane);
      const float mu = __uint_as_float(g[0]), lu = __uint_as_float(g[2]);
      const float M = x16_max(row16_max(mu));
      const float wl = ex2((mu - M) * C2);
      wsh[lane] = wl / x16_sum(row16_sum(wl * lu));
      lds_arrive(cnt + 7, lane);
    } else lds_wait(cnt + 7, 1u);
    COMPILER_FENCE();
    STAMP(3);
    u32x4 pp[16];
#pragma unroll
    for (int i = 0; i < 16; ++i) { pp[i] = ld16c(rs, WS_APO + (h * 64 + w4 * 16 + (i ^ (8 * b))) * 1024 + lane * 16); SCHED(); }
    lds_wait(cnt + 7, 2u);
    float a8[8];
#pragma unroll
    for (int k = 0; k < 8; ++k) a8[k] = 0.f;
    const float wv = wsh[w4 * 16 + (c16 ^ (8 * b))];
#pragma unroll
    for (int i = 0; i < 16; ++i) {
      const float wt = rdlane(wv, i);
#pragma unroll
      for (int k = 0; k < 4; ++k) { a8[2 * k] = fmaf(wt, bflo(pp[i][k]), a8[2 * k]); a8[2 * k + 1] = fmaf(wt, bfhi(pp[i][k]), a8[2 * k + 1]); }
      asm volatile("" : "+v"(a8[0]), "+v"(a8[1]), "+v"(a8[2]), "+v"(a8[3]), "+v"(a8[4]), "+v"(a8[5]), "+v"(a8[6]), "+v"(a8[7]));
      SCHED();
    }
    *reinterpret_cast<f32x4*>(red + w4 * 512 + 8 * lane) = f32x4{a8[0], a8[1], a8[2], a8[3]};
    *reinterpret_cast<f32x4*>(red + w4 * 512 + 8 * lane + 4) = f32x4{a8[4], a8[5], a8[6], a8[7]};
    sbar4(cnt + 3, lane);
    STAMP(4);
#pragma unroll
    for (int i = 0; i < 4; ++i) {
      const float* src = red + (i >> 1) * 1024 + w4 * 128 + 16 * (lane >> 3) + 8 * (i & 1);
      const f32x4 t0 = *reinterpret_cast<const f32x4*>(src) + *reinterpret_cast<const f32x4*>(src + 512), t1 = *reinterpret_cast<const f32x4*>(src + 4) + *reinterpret_cast<const f32x4*>(src + 516);
      xv[i] = u32x4{pk_bf(t0[0], t0[1]), pk_bf(t0[2], t0[3]), pk_bf(t1[0], t1[1]), pk_bf(t1[2], t1[3])};
    }
    gemv(red2 + w4 * 128);
    sbar4(cnt + 4, lane);
    STAMP(12);
    unsigned op;
    {
      f32x2 t = {0.f, 0.f};
#pragma unroll
      for (int w = 0; w < 4; ++w) t += *reinterpret_cast<const f32x2*>(red2 + w * 128 + (lane >> 5) * 64 + 8 * (lane & 7) + 2 * w4);
      op = pk_bf(t[0], t[1]);
    }
    float* red3 = red + 4096;
    float a[2][8];
#pragma unroll
    for (int bb = 0; bb < 2; ++bb)
#pragma unroll
      for (int k = 0; k < 8; ++k) a[bb][k] = 0.f;
#pragma unroll
    for (int ip = 0; ip < 8; ++ip) {
      const unsigned v0 = __builtin_amdgcn_readlane(op, ip), v1 = __builtin_amdgcn_readlane(op, 32 + ip);
#pragma unroll
      for (int k = 0; k < 4; ++k) {
        const unsigned lo = plo(Wb[2 * ip][k], Wb[2 * ip + 1][k]), hi = phi(Wb[2 * ip][k], Wb[2 * ip + 1][k]);
        a[0][2 * k] = dot2(lo, v0, a[0][2 * k]); a[0][2 * k + 1] = dot2(hi, v0, a[0][2 * k + 1]);
        a[1][2 * k] = dot2(lo, v1, a[1][2 * k]); a[1][2 * k + 1] = dot2(hi, v1, a[1][2 * k + 1]);
      }
    }
#pragma unroll
    for (int bb = 0; bb < 2; ++bb) {
      *reinterpret_cast<f32x4*>(red3 + (w4 * 2 + bb) * 512 + 8 * lane) = f32x4{a[bb][0], a[bb][1], a[bb][2], a[bb][3]};
      *reinterpret_cast<f32x4*>(red3 + (w4 * 2 + bb) * 512 + 8 * lane + 4) = f32x4{a[bb][4], a[bb][5], a[bb][6], a[bb][7]};
    }
    sbar4(cnt + 5, lane);
    {
      f32x4 t = {0.f, 0.f, 0.f, 0.f};
#pragma unroll
      for (int w = 0; w < 4; ++w) t += *reinterpret_cast<const f32x4*>(red3 + w * 1024 + 4 * tid);
      st16c(rs, WS_GA + ((tid >> 7) * 64 + idx) * 2048 + (tid & 127) * 16, u32x4{pk_bf(t[0], t[1]), tg.hi, pk_bf(t[2], t[3]), tg.lo});
    }
    STAMP(5);
    STAMP_FLUSH(0); TR_FLUSH();
    if (PADN) asm volatile("s_endpgm\n\t.fill %0,4,0xBF800000" :: "n"(PADN));
    return;
  }
}

extern "C" void kernel_launch(void* const* d_in, const int* in_sizes, int n_in, void* d_out, int out_size, void* d_ws, size_t ws_size, hipStream_t stream) {
  const int sel[5][3] = {{1, 3, 6}, {2, 4, 0}, {3, 2, 6}, {9, 9, 6}, {7, 8, 10}};
  Args a;
  for (int r = 0; r < 5; ++r) {
    for (int c = 0; c < 3; ++c) a.t[r][c] = (gptr)d_in[sel[r][c]];
    a.t[r][3] = (gptr)d_ws;
  }
  a.w_o = (gptr)d_in[5];
  a.out = (gptr)d_out;
  kmain<<<256, 512, 0, stream>>>(a);
}
```
